```python
import torch

def _softmax(x, axis=-1):
    return torch.softmax(x, dim=axis)
def _gelu(x):
    return torch.nn.functional.gelu(x, approximate="tanh")
def _silu(x):
    return torch.nn.functional.silu(x)
def _sigmoid(x):
    return torch.sigmoid(x)
def _layernorm(x, axis=-1, eps=1e-5):
    m = x.mean(dim=axis, keepdim=True)
    v = x.var(dim=axis, keepdim=True, unbiased=False)
    return (x - m) / torch.sqrt(v + eps)
def _scatter_add(tgt, idx, src, axis=0):
    return tgt.clone().index_add_(axis, idx, src)
def _segment_sum(src, seg, n):
    out = torch.zeros((n,) + src.shape[1:], dtype=src.dtype)
    return out.index_add_(0, seg, src)
def _pad(x, pw):
    flat = tuple(v for pair in reversed(pw) for v in pair)
    return torch.nn.functional.pad(x, flat)

kernel_name = 'interleave_layers__linear_kernel_hedgehog__mixer_s_mlp__prompt'

def setup_inputs():

    _w = (lambda a: (lambda t: t.to(torch.bfloat16) if t.is_floating_point() else t)(torch.as_tensor(a)))
    def _A_setup():
        B, S, D, H = 1, 2048, 512, 32
        Dh = D // H
        return {
            "x": _w((lambda *s: torch.randn(*s, dtype=torch.bfloat16) * 0.05)(B, S, D)),
            "wqkv": _w((lambda *s: torch.randn(*s, dtype=torch.bfloat16) * 0.4)(D, 3 * D)),
            "w_phi": _w((lambda *s: torch.randn(*s, dtype=torch.bfloat16) * 0.4)(H, Dh, Dh) * (Dh ** -0.5)),
            "wo": _w((lambda *s: torch.randn(*s, dtype=torch.bfloat16) * 0.05)(D, D)),
        }
    _A_in = _A_setup()

    def _B_setup():
        B, S, D, F = 1, 2048, 512, 1792
        return {
            "x":  _w((lambda *s: torch.randn(*s, dtype=torch.bfloat16) * 0.05)(B, S, D)),
            "w1": _w((lambda *s: torch.randn(*s, dtype=torch.bfloat16) * 3.2)(D, D) * (D ** -0.5)),
            "w2": _w((lambda *s: torch.randn(*s, dtype=torch.bfloat16) * 3.2)(D, D) * (D ** -0.5)),
            "w3": _w((lambda *s: torch.randn(*s, dtype=torch.bfloat16) * 3.2)(D, F) * (D ** -0.5)),
            "w4": _w((lambda *s: torch.randn(*s, dtype=torch.bfloat16) * 3.2)(F, D) * (F ** -0.5)),
        }
    _B_in = _B_setup()

    return {
        "A_x": _A_in["x"],
        "A_wqkv": _A_in["wqkv"],
        "A_w_phi": _A_in["w_phi"],
        "A_wo": _A_in["wo"],
        "B_x": _B_in["x"],
        "B_w1": _B_in["w1"],
        "B_w2": _B_in["w2"],
        "B_w3": _B_in["w3"],
        "B_w4": _B_in["w4"],
    }

def reference(A_x, A_wqkv, A_w_phi, A_wo, B_x, B_w1, B_w2, B_w3, B_w4):
    def _A_ref(x, wqkv, w_phi, wo):
        B, S, D = x.shape
        H = 32; Dh = D // H
        qkv = (lambda x, *d: x.permute(*d))((x @ wqkv).reshape(B, S, 3, H, Dh), 2, 0, 3, 1, 4)
        q, k, v = qkv[0], qkv[1], qkv[2]
        pq = torch.einsum("bhsd,hdf->bhsf", q, w_phi)
        pk = torch.einsum("bhsd,hdf->bhsf", k, w_phi)
        phi_q = torch.concatenate([_softmax(pq, axis=-1), _softmax(-pq, axis=-1)], axis=-1)
        phi_k = torch.concatenate([_softmax(pk, axis=-1), _softmax(-pk, axis=-1)], axis=-1)
        kv = torch.einsum("bhsf,bhsd->bhfd", phi_k, v)
        z  = torch.einsum("bhsf->bhf", phi_k)
        num = torch.einsum("bhqf,bhfd->bhqd", phi_q, kv)
        den = torch.einsum("bhqf,bhf->bhq", phi_q, z)[..., None] + 1e-6
        o = num / den
        return (lambda x, *d: x.permute(*d))(o, 0, 2, 1, 3).reshape(B, S, D) @ wo

    def _B_ref(x, w1, w2, w3, w4):
        B, S, D = x.shape
        h = _gelu(_layernorm(x, axis=-1) @ w1)
        c = D // 4
        g0 = _pad(h[:, :, 0*c:1*c], ((0, 0), (1, 0), (0, 0)))[:, :S, :]
        g1 = _pad(h[:, :, 1*c:2*c], ((0, 0), (0, 1), (0, 0)))[:, 1:, :]
        g2 = _pad(h[:, :, 2*c:3*c], ((0, 0), (2, 0), (0, 0)))[:, :S, :]
        g3 = _pad(h[:, :, 3*c:   ], ((0, 0), (0, 2), (0, 0)))[:, 2:, :]
        hs = torch.concatenate([g0, g1, g2, g3], axis=-1) @ w2
        x1 = x + hs
        h2 = _gelu(_layernorm(x1, axis=-1) @ w3) @ w4
        return x1 + h2

    x = A_x
    x = x + _A_ref(_layernorm(x, axis=-1), A_wqkv, A_w_phi, A_wo)
    x = x + _B_ref(_layernorm(x, axis=-1), B_w1, B_w2, B_w3, B_w4)
    x = x + _A_ref(_layernorm(x, axis=-1), A_wqkv, A_w_phi, A_wo)
    x = x + _B_ref(_layernorm(x, axis=-1), B_w1, B_w2, B_w3, B_w4)
    return x
```

```cpp
#include <hip/hip_runtime.h>
#include <hip/hip_bf16.h>
typedef __hip_bfloat16 bf16;
static constexpr int S=2048, D=512, H=32, DH=16, F=1792;
__device__ __forceinline__ float b2f(bf16 v){ return __bfloat162float(v);}
__global__ void k_cvt(const bf16* in, float* out, int n){ int i=blockIdx.x*blockDim.x+threadIdx.x; if(i<n) out[i]=b2f(in[i]); }
__global__ void k_cvt_out(const float* in, bf16* out, int n){ int i=blockIdx.x*blockDim.x+threadIdx.x; if(i<n) out[i]=__float2bfloat16(in[i]); }
__global__ void k_ln(const float* in, float* out){
  __shared__ float red[256]; int r=blockIdx.x, t=threadIdx.x; float a=in[r*D+t], b=in[r*D+256+t];
  red[t]=a+b; __syncthreads(); for(int s=128;s>0;s>>=1){ if(t<s) red[t]+=red[t+s]; __syncthreads(); } float m=red[0]/D; __syncthreads();
  float da=a-m, db=b-m; red[t]=da*da+db*db; __syncthreads(); for(int s=128;s>0;s>>=1){ if(t<s) red[t]+=red[t+s]; __syncthreads(); } float v=red[0]/D;
  float rs=1.0f/sqrtf(v+1e-5f); out[r*D+t]=da*rs; out[r*D+256+t]=db*rs; }
__global__ void k_gemm(const float* A, const bf16* W, float* C, int M, int N, int K, int act){
  __shared__ float As[16][17], Ws[16][17]; int tx=threadIdx.x, ty=threadIdx.y; int row=blockIdx.y*16+ty, col=blockIdx.x*16+tx; float acc=0;
  for(int k0=0;k0<K;k0+=16){ As[ty][tx]=A[row*K+k0+tx]; Ws[ty][tx]=b2f(W[(k0+ty)*N+col]); __syncthreads();
    for(int k=0;k<16;k++) acc+=As[ty][k]*Ws[k][tx]; __syncthreads(); }
  if(act==1){ float x=acc; float u=0.7978845608028654f*(x+0.044715f*x*x*x); acc=0.5f*x*(1.0f+tanhf(u)); }
  C[row*N+col]=acc; }
__global__ void k_phi(const float* qkv, const bf16* wphi, float* phiq, float* phik){
  int idx=blockIdx.x*blockDim.x+threadIdx.x; if(idx>=S*H*2) return; int which=idx&1; int h=(idx>>1)%H; int s=(idx>>1)/H;
  const float* q=qkv+s*1536+which*512+h*16; float p[16]; float mx=-1e30f, mn=1e30f;
  for(int f=0;f<16;f++){ float a=0; for(int d=0;d<16;d++) a+=q[d]*b2f(wphi[(h*16+d)*16+f]); p[f]=a; mx=fmaxf(mx,a); mn=fminf(mn,a);}
  float s1=0,s2=0; float e1[16],e2[16]; for(int f=0;f<16;f++){ e1[f]=expf(p[f]-mx); s1+=e1[f]; e2[f]=expf(mn-p[f]); s2+=e2[f]; }
  float* o=(which?phik:phiq)+(s*H+h)*32; for(int f=0;f<16;f++){ o[f]=e1[f]/s1; o[16+f]=e2[f]/s2; } }
__global__ void k_kv(const float* qkv, const float* phik, float* kv, float* z){
  int h=blockIdx.x; int f=threadIdx.x/17, d=threadIdx.x%17; if(f>=32) return; float a=0;
  for(int s=0;s<S;s++){ float p=phik[(s*H+h)*32+f]; float v= d<16? qkv[s*1536+1024+h*16+d]:1.0f; a+=p*v; }
  if(d<16) kv[(h*32+f)*16+d]=a; else z[h*32+f]=a; }
__global__ void k_o(const float* phiq, const float* kv, const float* z, float* o){
  int idx=blockIdx.x*blockDim.x+threadIdx.x; if(idx>=S*D) return; int d=idx&15; int h=(idx>>4)&31; int s=idx>>9; const float* p=phiq+(s*H+h)*32; float num=0, den=0;
  for(int f=0;f<32;f++){ num+=p[f]*kv[(h*32+f)*16+d]; den+=p[f]*z[h*32+f]; } o[idx]=num/(den+1e-6f); }
__global__ void k_add(const float* a, const float* b, float* c, int n){ int i=blockIdx.x*blockDim.x+threadIdx.x; if(i<n) c[i]=a[i]+b[i]; }
__global__ void k_shift(const float* h, float* o){ int idx=blockIdx.x*blockDim.x+threadIdx.x; if(idx>=S*D) return; int c=idx&511, s=idx>>9; int g=c>>7; int src= g==0? s-1: g==1? s+1: g==2? s-2: s+2; o[idx]=(src>=0&&src<S)? h[src*D+c]:0.f; }
extern "C" void kernel_launch(void* const* d_in, const int* in_sizes, int n_in, void* d_out, int out_size, void* d_ws, size_t ws_size, hipStream_t stream){
  const bf16* x0=(const bf16*)d_in[0]; const bf16* wqkv=(const bf16*)d_in[1]; const bf16* wphi=(const bf16*)d_in[2]; const bf16* wo=(const bf16*)d_in[3];
  const bf16* w1=(const bf16*)d_in[5]; const bf16* w2=(const bf16*)d_in[6]; const bf16* w3=(const bf16*)d_in[7]; const bf16* w4=(const bf16*)d_in[8];
  float* ws=(float*)d_ws; int n=S*D;
  float* x=ws; float* xn=x+n; float* t1=xn+n; float* t2=t1+n; float* t3=t2+n; float* qkv=t3+n; float* phiq=qkv+S*1536; float* phik=phiq+S*1024; float* kv=phik+S*1024; float* z=kv+32*32*16; float* h3=z+1024; float* x1=h3+S*F;
  k_cvt<<<n/256,256,0,stream>>>(x0,x,n);
  for(int L=0;L<2;L++){
    k_ln<<<S,256,0,stream>>>(x,xn);
    k_gemm<<<dim3(1536/16,S/16),dim3(16,16),0,stream>>>(xn,wqkv,qkv,S,1536,D,0);
    k_phi<<<S*H*2/256,256,0,stream>>>(qkv,wphi,phiq,phik);
    k_kv<<<H,544,0,stream>>>(qkv,phik,kv,z);
    k_o<<<n/256,256,0,stream>>>(phiq,kv,z,t1);
    k_gemm<<<dim3(D/16,S/16),dim3(16,16),0,stream>>>(t1,wo,t2,S,D,D,0);
    k_add<<<n/256,256,0,stream>>>(x,t2,x,n);
    k_ln<<<S,256,0,stream>>>(x,xn);
    k_ln<<<S,256,0,stream>>>(xn,t1);
    k_gemm<<<dim3(D/16,S/16),dim3(16,16),0,stream>>>(t1,w1,t2,S,D,D,1);
    k_shift<<<n/256,256,0,stream>>>(t2,t3);
    k_gemm<<<dim3(D/16,S/16),dim3(16,16),0,stream>>>(t3,w2,t2,S,D,D,0);
    k_add<<<n/256,256,0,stream>>>(xn,t2,x1,n);
    k_ln<<<S,256,0,stream>>>(x1,t1);
    k_gemm<<<dim3(F/16,S/16),dim3(16,16),0,stream>>>(t1,w3,h3,S,F,D,1);
    k_gemm<<<dim3(D/16,S/16),dim3(16,16),0,stream>>>(h3,w4,t2,S,D,F,0);
    k_add<<<n/256,256,0,stream>>>(x1,t2,t2,n);
    k_add<<<n/256,256,0,stream>>>(x,t2,x,n);
  }
  k_cvt_out<<<n/256,256,0,stream>>>(x,(bf16*)d_out,n);
}
```

```cpp
#include <hip/hip_runtime.h>
#include <stdint.h>

#define GAS __attribute__((address_space(1)))
#define LAS __attribute__((address_space(3)))
#define DI __device__ __forceinline__
typedef unsigned v4u __attribute__((ext_vector_type(4)));
typedef unsigned v2u __attribute__((ext_vector_type(2)));
typedef float f4 __attribute__((ext_vector_type(4)));
typedef float f2 __attribute__((ext_vector_type(2)));
typedef short s8 __attribute__((ext_vector_type(8)));
typedef short s4 __attribute__((ext_vector_type(4)));
typedef __bf16 bf2 __attribute__((ext_vector_type(2)));
typedef GAS unsigned gu32;
typedef __amdgpu_buffer_rsrc_t rsrc_t;
#define RLX_AGENT __ATOMIC_RELAXED, __HIP_MEMORY_SCOPE_AGENT

#define DBG_GBAR 0

namespace {
constexpr unsigned MB = 1u << 20, KB = 1024u;
constexpr unsigned OFF_EPOCH = 0, OFF_ABORT = 256, OFF_GBAR = 512, OFF_FLAGS = 4096;
constexpr unsigned OFF_WT = 1 * MB;
constexpr unsigned W1_F = 0, W2_F = 1536, W3_F = 2048, W4_F = 2560, W5_F = 3072, W6_F = 4864;
constexpr unsigned OFF_CSP = 8 * MB;
constexpr unsigned OFF_L0 = 9 * MB, L_STRIDE = 24 * MB;
constexpr unsigned O_XB = 0, O_XS = 2 * MB, O_KVP = 2 * MB + 512 * KB, O_KVZ = 3 * MB + 512 * KB, O_OB = 4 * MB, O_X1B = 6 * MB,
                   O_X1S = 8 * MB, O_HB = 9 * MB, O_X2B = 11 * MB, O_X2S = 13 * MB, O_H3B = 14 * MB;
constexpr unsigned WS_BYTES = OFF_L0 + 2 * L_STRIDE;
constexpr unsigned OOB = 0x7F000000u;
constexpr int RING = 78;
constexpr unsigned HALF_B = RING * KB;
constexpr unsigned MISC = 2 * HALF_B;
constexpr unsigned M_CS = MISC, M_KVA = MISC + 1088, M_ZA = MISC + 3136, M_FLAG = MISC + 3264;
constexpr unsigned LDS_BYTES = 163840;
constexpr int POS1 = 0, POS2 = 48, POS3 = 80, POS4 = 112, POS5 = 144, POS6 = 200, POSE = 312;
constexpr unsigned SENT = 0xFFFFFFFFu;
constexpr unsigned SPIN_CAP = 1u << 13;
constexpr int AUX_SC1 = 16;
#define DLY_A 9

struct Params {
  const unsigned short *x, *wqkv, *wphi, *wo, *w1, *w2, *w3, *w4;
  unsigned short *out;
  unsigned char *ws;
};

DI unsigned pk2(float a, float b) { f2 v = {a, b}; bf2 r = __builtin_convertvector(v, bf2); return __builtin_bit_cast(unsigned, r); }
DI float asf(unsigned u) { return __builtin_bit_cast(float, u); }
DI float blo(unsigned u) { return __builtin_bit_cast(float, u << 16); }
DI float bhi(unsigned u) { return __builtin_bit_cast(float, u & 0xFFFF0000u); }
DI unsigned clampw(unsigned u) { return u < 0xFFFFFFFEu ? u : 0xFFFFFFFEu; }
DI unsigned clampf(float f) { return clampw(__builtin_bit_cast(unsigned, f)); }
DI f4 mfma32(v4u a, v4u b, f4 c) {
  return __builtin_amdgcn_mfma_f32_16x16x32_bf16(__builtin_bit_cast(s8, a), __builtin_bit_cast(s8, b), c, 0, 0, 0);
}
DI f4 mfma16(v2u a, v2u b, f4 c) {
  return __builtin_amdgcn_mfma_f32_16x16x16bf16_1k(__builtin_bit_cast(s4, a), __builtin_bit_cast(s4, b), c, 0, 0, 0);
}
DI v4u ldg(rsrc_t rs, unsigned voff, unsigned soff) { return __builtin_amdgcn_raw_buffer_load_b128(rs, voff, soff, AUX_SC1); }
DI v2u ldg2(rsrc_t rs, unsigned voff, unsigned soff) { return __builtin_amdgcn_raw_buffer_load_b64(rs, voff, soff, AUX_SC1); }
DI void stg(rsrc_t rs, unsigned voff, unsigned soff, v4u v) { __builtin_amdgcn_raw_buffer_store_b128(v, rs, voff, soff, AUX_SC1); }
DI void stg2(rsrc_t rs, unsigned voff, unsigned soff, v2u v) { __builtin_amdgcn_raw_buffer_store_b64(v, rs, voff, soff, AUX_SC1); }
DI unsigned max4(v4u v) { return max(max(v.x, v.y), max(v.z, v.w)); }
DI bool anybad(unsigned mx) { return __any(mx == SENT) != 0; }
DI float xsum(float x) {
  unsigned u = __builtin_bit_cast(unsigned, x);
  auto r = __builtin_amdgcn_permlane16_swap(u, u, false, false);
  x = __builtin_bit_cast(float, (unsigned)r[0]) + __builtin_bit_cast(float, (unsigned)r[1]);
  u = __builtin_bit_cast(unsigned, x);
  auto q = __builtin_amdgcn_permlane32_swap(u, u, false, false);
  return __builtin_bit_cast(float, (unsigned)q[0]) + __builtin_bit_cast(float, (unsigned)q[1]);
}
DI float xmax(float x) {
  unsigned u = __builtin_bit_cast(unsigned, x);
  auto r = __builtin_amdgcn_permlane16_swap(u, u, false, false);
  x = fmaxf(__builtin_bit_cast(float, (unsigned)r[0]), __builtin_bit_cast(float, (unsigned)r[1]));
  u = __builtin_bit_cast(unsigned, x);
  auto q = __builtin_amdgcn_permlane32_swap(u, u, false, false);
  return fmaxf(__builtin_bit_cast(float, (unsigned)q[0]), __builtin_bit_cast(float, (unsigned)q[1]));
}
DI float gelu(float x) {
  const float c1 = -2.302208198f, c2 = -0.1029432397f;
  const float t = x * __builtin_fmaf(x * x, c2, c1);
  const float e = __builtin_amdgcn_exp2f(t);
  return x * __builtin_amdgcn_rcpf(1.0f + e);
}
#define SB() __builtin_amdgcn_sched_barrier(0)
#define BAR() asm volatile("s_waitcnt lgkmcnt(0)\n\ts_barrier" ::: "memory")
#define WAITV_BAR(n) asm volatile("s_waitcnt vmcnt(" #n ") lgkmcnt(0)\n\ts_barrier" ::: "memory")
DI void sleepn(int n) {
  for (int i = 0; i < n; ++i) __builtin_amdgcn_s_sleep(1);
}

struct Ctx {
  rsrc_t rs;
  LAS unsigned char *sm;
  unsigned lds0;
  unsigned lane16;
  int lane, w, m, kh, g, tok;
  int t, c, tau, pp, b;
  int T, sO, TK, TS;
  unsigned fragO, statO;
  unsigned spins;
  gu32 *ctl;
};
DI void refresh(Ctx &x) {
  asm volatile("" : "+s"(x.w), "+s"(x.b), "+v"(x.lane));
  x.lane16 = (unsigned)x.lane * 16u;
  x.g = x.lane >> 4; x.tok = x.lane & 15;
  x.m = x.w & 3; x.kh = x.w >> 2;
  const int xc = x.b & 7, j = x.b >> 3;
  x.t = 4 * xc + (j >> 3); x.c = j & 7;
  x.tau = 2 * xc + (j >> 4); x.pp = j & 15;
  x.T = 4 * x.t + x.m; x.sO = 2 * x.c + x.kh;
  x.TK = 8 * x.tau + 2 * x.m + x.kh; x.TS = x.TK ^ 1;
  x.fragO = (unsigned)(x.T * 16 + x.sO) * KB;
  x.statO = (unsigned)(x.T * 16 + x.sO) * 128u;
}

DI void dma1(const Ctx &x, unsigned ldsaddr, unsigned soff) {
  asm volatile("s_mov_b32 m0, %0\n\ts_nop 0\n\tbuffer_load_dwordx4 %1, %2, %3 offen lds" ::"s"(ldsaddr), "v"(x.lane16), "s"(x.rs), "s"(soff)
               : "memory");
}
template <int LO, int HI, int WPOS> DI void dma_range(const Ctx &x, unsigned src) {
  asm volatile("s_nop 4" ::: "memory");
#pragma unroll
  for (int t = 0; t < (HI - LO + 3) / 4; ++t) {
    const int p0 = LO + 4 * t;
    if (p0 + 3 < HI || p0 + x.m < HI) {
      unsigned slot = (unsigned)(p0 % RING) + (unsigned)x.m;
      if ((p0 % RING) + 3 >= RING) slot = slot >= (unsigned)RING ? slot - RING : slot;
      dma1(x, x.lds0 + (unsigned)x.kh * HALF_B + slot * KB, src + (unsigned)(p0 - WPOS + x.m) * KB);
    }
  }
}
DI unsigned wsrc(const Ctx &x, unsigned wf, int slice, int half) { return OFF_WT + (wf + (unsigned)(slice * 2 * half + x.kh * half)) * KB; }

DI void fix4(Ctx &x, v4u &v, unsigned voff, unsigned soff) {
  while (anybad(max4(v))) {
    if (++x.spins > SPIN_CAP) {
      if (x.lane == 0) __hip_atomic_store(x.ctl + OFF_ABORT / 4, 1u, RLX_AGENT);
      break;
    }
    __builtin_amdgcn_s_sleep(1);
    asm volatile("" ::: "memory");
    v = ldg(x.rs, voff, soff);
  }
}
DI void fix2(Ctx &x, v2u &v, unsigned voff, unsigned soff) {
  while (anybad(max(v.x, v.y))) {
    if (++x.spins > SPIN_CAP) {
      if (x.lane == 0) __hip_atomic_store(x.ctl + OFF_ABORT / 4, 1u, RLX_AGENT);
      break;
    }
    __builtin_amdgcn_s_sleep(1);
    asm volatile("" ::: "memory");
    v = ldg2(x.rs, voff, soff);
  }
}

#define GBAR() do {} while (0)

DI void put_stats(const Ctx &x, v4u v, unsigned soff) {
  const float a0 = blo(v.x), a1 = bhi(v.x), a2 = blo(v.y), a3 = bhi(v.y), a4 = blo(v.z), a5 = bhi(v.z), a6 = blo(v.w), a7 = bhi(v.w);
  float s1 = ((a0 + a1) + (a2 + a3)) + ((a4 + a5) + (a6 + a7));
  float s2 = a0 * a0;
  s2 = __builtin_fmaf(a1, a1, s2); s2 = __builtin_fmaf(a2, a2, s2); s2 = __builtin_fmaf(a3, a3, s2);
  s2 = __builtin_fmaf(a4, a4, s2); s2 = __builtin_fmaf(a5, a5, s2); s2 = __builtin_fmaf(a6, a6, s2); s2 = __builtin_fmaf(a7, a7, s2);
  s1 = xsum(s1); s2 = xsum(s2);
  if (x.g == 0) stg2(x.rs, (unsigned)x.tok * 8u, soff, v2u{clampf(s1), clampf(s2)});
}
struct Stats { v2u v[4]; };
DI void stats_load(const Ctx &x, Stats &s, unsigned soffT) {
#pragma unroll
  for (int i = 0; i < 4; ++i) s.v[i] = ldg2(x.rs, (unsigned)(x.g * 512 + x.tok * 8 + i * 128), soffT);
}
DI void stats_fin(Ctx &x, Stats &s, unsigned soffT, float &mean, float &var) {
  unsigned mx = 0;
#pragma unroll
  for (int i = 0; i < 4; ++i) mx = max(mx, max(s.v[i].x, s.v[i].y));
  if (__builtin_expect(anybad(mx), 0)) {
#pragma unroll
    for (int i = 0; i < 4; ++i) fix2(x, s.v[i], (unsigned)(x.g * 512 + x.tok * 8 + i * 128), soffT);
  }
  float s1 = 0.f, s2 = 0.f;
#pragma unroll
  for (int i = 0; i < 4; ++i) { s1 += asf(s.v[i].x); s2 += asf(s.v[i].y); }
  s1 = xsum(s1); s2 = xsum(s2);
  mean = s1 * (1.0f / 512.0f);
  var = fmaxf(__builtin_fmaf(-mean, mean, s2 * (1.0f / 512.0f)), 0.0f);
}
DI v4u clamp4(v4u v) { return v4u{clampw(v.x), clampw(v.y), clampw(v.z), clampw(v.w)}; }
DI const LAS v4u *lds4(const Ctx &x, unsigned off) { return (const LAS v4u *)(x.sm + off); }

template <int NT, int POS> DI void kloop2(const Ctx &x, const v4u (&bf)[8][2], f4 (&acc)[NT][2], unsigned &mx) {
  LAS unsigned char *ab = x.sm + (unsigned)x.kh * HALF_B + x.lane16;
  v4u a[2][NT];
#pragma unroll
  for (int f = 0; f < NT; ++f) a[0][f] = *(const LAS v4u *)(ab + ((POS + f) % RING) * KB);
#pragma unroll
  for (int i = 0; i < 8; ++i) {
    if (i < 7) {
#pragma unroll
      for (int f = 0; f < NT; ++f) a[(i + 1) & 1][f] = *(const LAS v4u *)(ab + ((POS + (i + 1) * NT + f) % RING) * KB);
    }
    SB();
    mx = max(mx, max(max4(bf[i][0]), max4(bf[i][1])));
#pragma unroll
    for (int f = 0; f < NT; ++f) {
      acc[f][0] = mfma32(a[i & 1][f], bf[i][0], acc[f][0]);
      acc[f][1] = mfma32(a[i & 1][f], bf[i][1], acc[f][1]);
    }
    SB();
  }
}
template <int P> DI void lda4(const Ctx &x, v4u (&a)[4]) {
  constexpr int o0 = P % RING, o1 = (P + 2) % RING;
  LAS unsigned char *ab = x.sm + (unsigned)x.kh * HALF_B + x.lane16;
  if constexpr (o1 == o0 + 2) {
    LAS unsigned char *k = ab + (unsigned)x.kh * 2048u, *s = ab + (unsigned)(1 - x.kh) * 2048u;
    a[0] = *(const LAS v4u *)(k + o0 * KB); a[1] = *(const LAS v4u *)(k + (o0 + 1) * KB);
    a[2] = *(const LAS v4u *)(s + o0 * KB); a[3] = *(const LAS v4u *)(s + (o0 + 1) * KB);
  } else {
    LAS unsigned char *k = ab + (x.kh ? o1 : o0) * KB, *s = ab + (x.kh ? o0 : o1) * KB;
    a[0] = *(const LAS v4u *)(k); a[1] = *(const LAS v4u *)(k + KB);
    a[2] = *(const LAS v4u *)(s); a[3] = *(const LAS v4u *)(s + KB);
  }
}
template <int POS, int I> struct KL1 {
  static DI void run(const Ctx &x, const v4u (&bf)[8], f4 (&acc)[4], unsigned &mx, v4u (&cur)[4]) {
    v4u nxt[4];
    if constexpr (I < 7) lda4<POS + 4 * (I + 1)>(x, nxt);
    SB();
    mx = max(mx, max4(bf[I]));
#pragma unroll
    for (int f = 0; f < 4; ++f) acc[f] = mfma32(cur[f], bf[I], acc[f]);
    SB();
    if constexpr (I < 7) KL1<POS, I + 1>::run(x, bf, acc, mx, nxt);
  }
};
template <int POS> DI void kloop1(const Ctx &x, const v4u (&bf)[8], f4 (&acc)[4], unsigned &mx) {
  v4u a0[4];
  lda4<POS>(x, a0);
  KL1<POS, 0>::run(x, bf, acc, mx, a0);
}
template <int SCR> DI void xchg4(const Ctx &x, f4 (&acc)[4]) {
  LAS unsigned char *wr = x.sm + (unsigned)x.kh * HALF_B + x.lane16 + (unsigned)x.m * 2048u;
  LAS unsigned char *rd = x.sm + (unsigned)(1 - x.kh) * HALF_B + x.lane16 + (unsigned)x.m * 2048u;
  *(LAS f4 *)(wr + SCR * KB) = acc[2];
  *(LAS f4 *)(wr + (SCR + 1) * KB) = acc[3];
  BAR();
  acc[0] += *(const LAS f4 *)(rd + SCR * KB);
  acc[1] += *(const LAS f4 *)(rd + (SCR + 1) * KB);
}
template <int NT, int SCR> DI void xchg2(const Ctx &x, f4 (&acc)[NT][2]) {
  LAS unsigned char *wr = x.sm + (unsigned)x.kh * HALF_B + x.lane16 + (unsigned)x.m * (NT * KB);
  LAS unsigned char *rd = x.sm + (unsigned)(1 - x.kh) * HALF_B + x.lane16 + (unsigned)x.m * (NT * KB);
#pragma unroll
  for (int f = 0; f < NT; ++f) *(LAS f4 *)(wr + (SCR + f) * KB) = acc[f][1];
  BAR();
#pragma unroll
  for (int f = 0; f < NT; ++f) acc[f][0] += *(const LAS f4 *)(rd + (SCR + f) * KB);
}
DI f4 ldcs(const Ctx &x, int tile) { return *(const LAS f4 *)(x.sm + M_CS + (unsigned)tile * 64u + (unsigned)x.g * 16u); }

DI void featmap(f4 v, v2u wphi, v2u &plus, v2u &minus) {
  const v2u b = {pk2(v.x, v.y), pk2(v.z, v.w)};
  const f4 p = mfma16(wphi, b, f4{0.f, 0.f, 0.f, 0.f});
  const float mxv = xmax(fmaxf(fmaxf(p.x, p.y), fmaxf(p.z, p.w)));
  const float mnv = -xmax(fmaxf(fmaxf(-p.x, -p.y), fmaxf(-p.z, -p.w)));
  const float L2E = 1.4426950408889634f;
  const float e0 = __builtin_amdgcn_exp2f((p.x - mxv) * L2E), e1 = __builtin_amdgcn_exp2f((p.y - mxv) * L2E);
  const float e2 = __builtin_amdgcn_exp2f((p.z - mxv) * L2E), e3 = __builtin_amdgcn_exp2f((p.w - mxv) * L2E);
  const float n0 = __builtin_amdgcn_exp2f((mnv - p.x) * L2E), n1 = __builtin_amdgcn_exp2f((mnv - p.y) * L2E);
  const float n2 = __builtin_amdgcn_exp2f((mnv - p.z) * L2E), n3 = __builtin_amdgcn_exp2f((mnv - p.w) * L2E);
  const float rp = __builtin_amdgcn_rcpf(xsum((e0 + e1) + (e2 + e3)));
  const float rn = __builtin_amdgcn_rcpf(xsum((n0 + n1) + (n2 + n3)));
  plus = v2u{pk2(e0 * rp, e1 * rp), pk2(e2 * rp, e3 * rp)};
  minus = v2u{pk2(n0 * rn, n1 * rn), pk2(n2 * rn, n3 * rn)};
}
DI v2u trread(const Ctx &x, unsigned off) {
  s4 r = __builtin_amdgcn_ds_read_tr16_b64_v4i16((LAS s4 *)(x.sm + off));
  return __builtin_bit_cast(v2u, r);
}

#define LDA(dst, voff, soff, imm) \
  asm volatile("buffer_load_dwordx4 %0, %1, %2, %3 offen offset:%4 sc1" : "=v"(dst) : "v"(voff), "s"(x.rs), "s"(soff), "i"(imm) : "memory")
#define WAITV1(n, r) asm volatile("s_waitcnt vmcnt(%1)" : "+v"(r) : "i"(n) : "memory")

DI void fix4_asm(Ctx &x, v4u &v, unsigned voff, unsigned soff) {
  while (anybad(max4(v))) {
    if (++x.spins > SPIN_CAP) {
      if (x.lane == 0) __hip_atomic_store(x.ctl + OFF_ABORT / 4, 1u, RLX_AGENT);
      break;
    }
    __builtin_amdgcn_s_sleep(1);
    asm volatile("buffer_load_dwordx4 %0, %1, %2, %3 offen sc1\n\ts_waitcnt vmcnt(0)" : "=&v"(v) : "v"(voff), "s"(x.rs), "s"(soff) : "memory");
  }
}
template <int K0, int K1, int WN, int K> struct P6Step {
  static DI void run(Ctx &x, v4u (&hf)[28], f4 (&acc)[4], unsigned &mx, v4u (&cur)[4]) {
    v4u nxt[4];
    if constexpr (K + 1 < K1) lda4<POS6 + 4 * (K + 1)>(x, nxt);
    SB();
    if constexpr (WN >= 0) WAITV1(WN - (K - K0), hf[K]);
    else asm volatile("" : "+v"(hf[K]) :: "memory");
    mx = max(mx, max4(hf[K]));
#pragma unroll
    for (int f = 0; f < 4; ++f) acc[f] = mfma32(cur[f], hf[K], acc[f]);
    SB();
    if constexpr (K + 1 < K1) P6Step<K0, K1, WN, K + 1>::run(x, hf, acc, mx, nxt);
  }
};
template <int K0, int K1, int WN> DI void p6chunk(Ctx &x, v4u (&hf)[28], f4 (&acc)[4], unsigned soff) {
  f4 save[4];
#pragma unroll
  for (int f = 0; f < 4; ++f) save[f] = acc[f];
  bool redo;
  do {
    unsigned mx = 0;
    v4u a0[4];
    lda4<POS6 + 4 * K0>(x, a0);
    P6Step<K0, K1, WN, K0>::run(x, hf, acc, mx, a0);
    redo = anybad(mx) && x.spins <= SPIN_CAP;
    if (__builtin_expect(redo, 0)) {
#pragma unroll
      for (int k = K0; k < K1; ++k) fix4_asm(x, hf[k], x.lane16, soff + (unsigned)k * KB);
#pragma unroll
      for (int f = 0; f < 4; ++f) acc[f] = save[f];
    }
  } while (__builtin_expect(redo, 0));
}

}

__global__ void __launch_bounds__(512, 2) mega_kernel(Params p) {
  extern __shared__ __attribute__((aligned(16))) unsigned char smem_[];
  Ctx x;
  x.sm = (LAS unsigned char *)smem_;
  x.lds0 = (unsigned)(size_t)smem_;
  x.lane = threadIdx.x & 63;
  x.w = __builtin_amdgcn_readfirstlane(threadIdx.x >> 6);
  x.b = blockIdx.x;
  refresh(x);
  const int b = x.b;
  x.spins = 0;
  x.ctl = (gu32 *)p.ws;
  x.rs = __builtin_amdgcn_make_buffer_rsrc((void *)p.ws, 0, WS_BYTES, 0x00020000);
  const v4u POISON = {SENT, SENT, SENT, SENT};

  const unsigned tag = __hip_atomic_load(x.ctl + OFF_EPOCH / 4, RLX_AGENT) + 1u;
  const int bid = x.w * 256 + b;
  if (bid < 1664) {
    const unsigned short *src; int ld, ra, rb, n0, kstep, csid = -1; unsigned fr[4];
    if (bid < 384) {
      kstep = bid & 15; const int nb = bid >> 4; src = p.wqkv; ld = 1536; n0 = nb * 64; ra = 32 * kstep; rb = ra + 16;
      const int sec = nb >> 3, h0 = 4 * (nb & 7);
#pragma unroll
      for (int ct = 0; ct < 4; ++ct) { const int hh = h0 + ct; fr[ct] = W1_F + (unsigned)(((hh >> 1) * 16 + kstep) * 6 + sec * 2 + (hh & 1)); }
      csid = 0;
    } else if (bid < 768) {
      const int mi = (bid - 384) >> 7, r = (bid - 384) & 127; kstep = r & 15; const int nb = r >> 4;
      src = mi == 0 ? p.wo : (mi == 1 ? p.w1 : p.w2); ld = 512; n0 = nb * 64; ra = 32 * kstep; rb = ra + 16;
#pragma unroll
      for (int ct = 0; ct < 4; ++ct) fr[ct] = W2_F + (unsigned)(mi * 512 + (nb * 16 + kstep) * 4 + ct);
      if (mi == 1) csid = 1;
    } else if (bid < 1216) {
      const int r = bid - 768; kstep = r & 15; const int nb = r >> 4; src = p.w3; ld = 1792; n0 = nb * 64; ra = 32 * kstep; rb = ra + 16;
#pragma unroll
      for (int ct = 0; ct < 4; ++ct) { const int F = nb * 4 + ct; fr[ct] = W5_F + (unsigned)(((F / 7) * 16 + kstep) * 7 + F % 7); }
      csid = 2;
    } else {
      const int r = bid - 1216; const int nb = r & 7; kstep = r >> 3; src = p.w4; ld = 512; n0 = nb * 64;
      const int u = kstep / 7, q = kstep % 7;
      if (q < 3) { ra = 224 * u + 32 * q; rb = ra + 16; }
      else if (q == 3) { ra = 224 * u + 96; rb = 224 * u + 208; }
      else { ra = 224 * u + 112 + 32 * (q - 4); rb = ra + 16; }
#pragma unroll
      for (int ct = 0; ct < 4; ++ct) fr[ct] = W6_F + (unsigned)((nb * 56 + kstep) * 4 + ct);
    }
    const int ip = x.lane >> 5, uu = (x.lane >> 4) & 1, q = (x.lane >> 2) & 3, c1 = ((x.lane >> 1) & 1) ^ ip, bb = x.lane & 1;
    const int rl = 4 * ip + q, ch = 2 * (2 * uu + c1) + bb;
    const unsigned stg0 = x.lds0 + (unsigned)x.w * 4096u;
#pragma unroll
    for (int u = 0; u < 4; ++u) {
      const int row = (u < 2 ? ra + 8 * u : rb + 8 * (u - 2)) + rl;
      const GAS unsigned char *gp = (const GAS unsigned char *)src + ((size_t)row * ld + n0 + ch * 8) * 2;
      asm volatile("s_mov_b32 m0, %0\n\ts_nop 0\n\tglobal_load_lds_dwordx4 %1, off" ::"s"(stg0 + (unsigned)u * 1024u), "v"(gp) : "memory");
    }
    asm volatile("s_waitcnt vmcnt(0)" ::: "memory");
    const int gq = (x.lane & 15) >> 2, gp_ = x.lane & 3, gi = x.g & 1;
#pragma unroll
    for (int ct = 0; ct < 4; ++ct) {
      const unsigned L = (unsigned)((gi << 5) | ((ct >> 1) << 4) | (gq << 2) | (((ct & 1) ^ gi) << 1) | (gp_ >> 1));
      const unsigned a0 = (unsigned)x.w * 4096u + (unsigned)(x.g >> 1) * 1024u + L * 16u + (unsigned)(gp_ & 1) * 8u;
      const v2u lo = trread(x, a0), hi = trread(x, a0 + 2048u);
      const v4u fragv = {lo.x, lo.y, hi.x, hi.y};
      stg(x.rs, x.lane16, OFF_WT + fr[ct] * KB, fragv);
      if (csid >= 0) {
        const f4 cs = mfma32(fragv, v4u{0x3F803F80u, 0x3F803F80u, 0x3F803F80u, 0x3F803F80u}, f4{0.f, 0.f, 0.f, 0.f});
        unsigned id;
        if (csid == 0) { const int hh = 4 * ((bid >> 4) & 7) + ct; id = (unsigned)((hh >> 1) * 6 + ((bid >> 4) >> 3) * 2 + (hh & 1)); }
        else if (csid == 1) id = 96u + (unsigned)((((bid - 384) & 127) >> 4) * 4 + ct);
        else id = 128u + (unsigned)(((bid - 768) >> 4) * 4 + ct);
        if (x.tok == 0) stg(x.rs, (unsigned)x.g * 16u, OFF_CSP + (id * 16u + (unsigned)kstep) * 64u, __builtin_bit_cast(v4u, cs));
      }
    }
  }
  v4u xres;
  {
    const GAS unsigned char *xp = (const GAS unsigned char *)p.x + ((size_t)(16 * x.T + x.tok) * 512 + 32 * x.sO + 4 * x.g) * 2;
    const v2u lo = *(const GAS v2u *)xp, hi = *(const GAS v2u *)(xp + 32);
    xres = clamp4(v4u{lo.x, lo.y, hi.x, hi.y});
    stg(x.rs, x.lane16, OFF_L0 + O_XB + x.fragO, xres);
    put_stats(x, xres, OFF_L0 + O_XS + x.statO);
  }
#pragma unroll
  for (int L = 0; L < 2; ++L) {
    const unsigned lb = OFF_L0 + (unsigned)L * L_STRIDE;
    if (x.kh == 0) stg(x.rs, x.lane16, lb + O_KVP + (unsigned)((x.pp * 16 + x.tau) * 4 + x.m) * KB, POISON);
    else if (x.tok == 0) stg(x.rs, (unsigned)x.g * 16u, lb + O_KVZ + (unsigned)((x.pp * 4 + x.m) * 16 + x.tau) * 64u, POISON);
  }
  stg(x.rs, x.lane16, OFF_L0 + O_OB + (unsigned)(x.TK * 16 + x.pp) * KB, POISON);
  stg(x.rs, x.lane16, OFF_L0 + O_HB + x.fragO, POISON);
  v2u wph[2];
#pragma unroll
  for (int h = 0; h < 2; ++h) {
    const unsigned short *wp = p.wphi + ((2 * x.pp + h) * 16 + 4 * x.g) * 16 + x.tok;
    wph[h] = v2u{(unsigned)wp[0] | ((unsigned)wp[16] << 16), (unsigned)wp[32] | ((unsigned)wp[48] << 16)};
  }
  asm volatile("s_waitcnt vmcnt(0) lgkmcnt(0)\n\ts_barrier" ::: "memory");
  if (threadIdx.x == 0) __hip_atomic_store(x.ctl + OFF_FLAGS / 4 + b, tag, RLX_AGENT);
  if (x.w == 0) {
    unsigned sp = 0;
    while (true) {
      asm volatile("" ::: "memory");
      const v4u f = ldg(x.rs, x.lane16, OFF_FLAGS);
      if (__all(f.x == tag && f.y == tag && f.z == tag && f.w == tag)) break;
      if (++sp > SPIN_CAP) {
        if (x.lane == 0) __hip_atomic_store(x.ctl + OFF_ABORT / 4, 3u, RLX_AGENT);
        break;
      }
      __builtin_amdgcn_s_sleep(2);
    }
    __builtin_amdgcn_fence(__ATOMIC_ACQUIRE, "agent");
    asm volatile("s_waitcnt vmcnt(0)" ::: "memory");
    if (b == 0 && x.lane == 0) __hip_atomic_store(x.ctl + OFF_EPOCH / 4, tag, RLX_AGENT);
  }
  asm volatile("s_waitcnt vmcnt(0) lgkmcnt(0)\n\ts_barrier" ::: "memory");
  dma_range<POS1, POS2, POS1>(x, wsrc(x, W1_F, x.pp, 48));
  for (int u = x.w; u < 17; u += 8) {
    const unsigned id = u < 6 ? (unsigned)(x.pp * 6 + u) : (u < 10 ? 96u + (unsigned)(x.c * 4 + u - 6) : 128u + (unsigned)(x.pp * 7 + u - 10));
    const v4u v = ldg(x.rs, x.lane16, OFF_CSP + id * KB);
    f4 s = __builtin_bit_cast(f4, v);
#pragma unroll
    for (int o = 4; o < 64; o <<= 1) {
      s.x += __shfl_xor(s.x, o); s.y += __shfl_xor(s.y, o); s.z += __shfl_xor(s.z, o); s.w += __shfl_xor(s.w, o);
    }
    if (x.lane < 4) *(LAS f4 *)(x.sm + M_CS + (unsigned)u * 64u + (unsigned)x.lane * 16u) = s;
  }

  v4u x1r, x2r;
#pragma unroll 1
  for (int L = 0; L < 2; ++L) {
    const unsigned lb0 = OFF_L0 + (unsigned)L * L_STRIDE;
    v4u phq[2];
    GBAR();
    {
      refresh(x);
      unsigned lb = lb0;
      asm volatile("" : "+s"(lb));
      v4u bf[8][2];
      const unsigned sk = lb + O_XB + (unsigned)(x.TK * 16 + 8 * x.kh) * KB, ss = lb + O_XB + (unsigned)(x.TS * 16 + 8 * x.kh) * KB;
#pragma unroll
      for (int i = 0; i < 8; ++i) { bf[i][0] = ldg(x.rs, x.lane16 + i * KB, sk); bf[i][1] = ldg(x.rs, x.lane16 + i * KB, ss); }
      Stats st;
      stats_load(x, st, lb + O_XS + (unsigned)x.TK * 2048u);
      WAITV_BAR(20);
      stg(x.rs, x.lane16, lb + O_X1B + x.fragO, POISON);
      stg(x.rs, x.lane16, lb + O_X2B + x.fragO, POISON);
      if (x.g == 0) { stg2(x.rs, (unsigned)x.tok * 8u, lb + O_X1S + x.statO, v2u{SENT, SENT}); stg2(x.rs, (unsigned)x.tok * 8u, lb + O_X2S + x.statO, v2u{SENT, SENT}); }
      f4 acc[6][2];
      bool redo;
      do {
#pragma unroll
        for (int f = 0; f < 6; ++f) { acc[f][0] = f4{0.f, 0.f, 0.f, 0.f}; acc[f][1] = f4{0.f, 0.f, 0.f, 0.f}; }
        unsigned mx = 0;
        kloop2<6, POS1>(x, bf, acc, mx);
        redo = anybad(mx) && x.spins <= SPIN_CAP;
        if (__builtin_expect(redo, 0)) {
#pragma unroll
          for (int i = 0; i < 8; ++i) { fix4(x, bf[i][0], x.lane16 + i * KB, sk); fix4(x, bf[i][1], x.lane16 + i * KB, ss); }
        }
      } while (__builtin_expect(redo, 0));
      float mean, var;
      stats_fin(x, st, lb + O_XS + (unsigned)x.TK * 2048u, mean, var);
      dma_range<POS2, POS2 + 30, POS2>(x, wsrc(x, W2_F, x.c, 32));
      BAR();
      xchg2<6, 0>(x, acc);
      const float rstd = __builtin_amdgcn_rsqf(var + 1e-5f), mr = mean * rstd;
      f4 val[6];
#pragma unroll
      for (int f = 0; f < 6; ++f) {
        const f4 cs = ldcs(x, f);
        val[f] = acc[f][0] * rstd - cs * mr;
      }
      const unsigned img = 24u * KB + (unsigned)((2 * x.m + x.kh) * 16 + x.tok) * 32u + (unsigned)x.g * 8u;
#pragma unroll
      for (int h = 0; h < 2; ++h) {
        v2u pl, mi;
        featmap(val[2 + h], wph[h], pl, mi);
        *(LAS v2u *)(x.sm + img + (unsigned)(h * 3 + 0) * 4096u) = pl;
        *(LAS v2u *)(x.sm + img + (unsigned)(h * 3 + 1) * 4096u) = mi;
        *(LAS v2u *)(x.sm + img + (unsigned)(h * 3 + 2) * 4096u) = v2u{pk2(val[4 + h].x, val[4 + h].y), pk2(val[4 + h].z, val[4 + h].w)};
      }
      BAR();
      {
        const int h = x.w & 1, sg = (x.w >> 1) & 1;
        const unsigned lo = (unsigned)(4 * x.g + (x.tok >> 2)) * 32u + (unsigned)(x.tok & 3) * 8u;
        const unsigned ia = 24u * KB + (unsigned)(h * 3 + sg) * 4096u + lo, iv = 24u * KB + (unsigned)(h * 3 + 2) * 4096u + lo;
        f4 kv = {0.f, 0.f, 0.f, 0.f};
#pragma unroll
        for (int tb = 0; tb < 4; ++tb) {
          const v2u a0 = trread(x, ia + tb * 1024u), a1 = trread(x, ia + tb * 1024u + 512u);
          v4u bb = {0x3F803F80u, 0x3F803F80u, 0x3F803F80u, 0x3F803F80u};
          if (x.kh == 0) { const v2u b0 = trread(x, iv + tb * 1024u), b1 = trread(x, iv + tb * 1024u + 512u); bb = v4u{b0.x, b0.y, b1.x, b1.y}; }
          kv = mfma32(v4u{a0.x, a0.y, a1.x, a1.y}, bb, kv);
        }
        const v4u kvw = clamp4(__builtin_bit_cast(v4u, kv));
        if (x.kh == 0) stg(x.rs, x.lane16, lb + O_KVP + (unsigned)((x.pp * 16 + x.tau) * 4 + h * 2 + sg) * KB, kvw);
        else if (x.tok == 0) stg(x.rs, (unsigned)x.g * 16u, lb + O_KVZ + (unsigned)((x.pp * 4 + h * 2 + sg) * 16 + x.tau) * 64u, kvw);
      }
#pragma unroll
      for (int h = 0; h < 2; ++h) {
        v2u pl, mi;
        featmap(val[h], wph[h], pl, mi);
        phq[h] = v4u{pl.x, pl.y, mi.x, mi.y};
      }
    }
    GBAR();
    {
      refresh(x);
      unsigned lb = lb0;
      asm volatile("" : "+s"(lb));
      sleepn(4);
      if (x.kh == 0) {
        v4u pv[16];
        const unsigned so = lb + O_KVP + (unsigned)(x.pp * 64 + x.m) * KB;
#pragma unroll
        for (int i = 0; i < 16; ++i) pv[i] = ldg(x.rs, x.lane16, so + (unsigned)i * 4096u);
        unsigned mx = 0;
#pragma unroll
        for (int i = 0; i < 16; ++i) mx = max(mx, max4(pv[i]));
        if (__builtin_expect(anybad(mx), 0)) {
#pragma unroll
          for (int i = 0; i < 16; ++i) fix4(x, pv[i], x.lane16, so + (unsigned)i * 4096u);
        }
        f4 s = __builtin_bit_cast(f4, pv[0]);
#pragma unroll
        for (int i = 1; i < 16; ++i) s += __builtin_bit_cast(f4, pv[i]);
        *(LAS v2u *)(x.sm + M_KVA + (unsigned)(x.m >> 1) * 1024u + x.lane16 + (unsigned)(x.m & 1) * 8u) = v2u{pk2(s.x, s.y), pk2(s.z, s.w)};
      } else {
        const unsigned so = lb + O_KVZ + (unsigned)(x.pp * 4 + x.m) * KB;
        v4u zv = ldg(x.rs, x.lane16, so);
        fix4(x, zv, x.lane16, so);
        f4 s = __builtin_bit_cast(f4, zv);
#pragma unroll
        for (int o = 4; o < 64; o <<= 1) {
          s.x += __shfl_xor(s.x, o); s.y += __shfl_xor(s.y, o); s.z += __shfl_xor(s.z, o); s.w += __shfl_xor(s.w, o);
        }
        if (x.lane < 4) *(LAS v2u *)(x.sm + M_ZA + (unsigned)x.m * 32u + (unsigned)x.lane * 8u) = v2u{pk2(s.x, s.y), pk2(s.z, s.w)};
      }
      BAR();
      dma_range<POS2 + 30, POS3, POS2>(x, wsrc(x, W2_F, x.c, 32));
      dma_range<POS3, POS4, POS3>(x, wsrc(x, W3_F, x.c, 32));
      dma_range<POS4, POS4 + 14, POS4>(x, wsrc(x, W4_F, x.c, 32));
      unsigned ow[4];
#pragma unroll
      for (int h = 0; h < 2; ++h) {
        const v4u ka = *lds4(x, M_KVA + (unsigned)h * 1024u + x.lane16);
        const v2u z0 = *(const LAS v2u *)(x.sm + M_ZA + (unsigned)(h * 2) * 32u + (unsigned)x.g * 8u);
        const v2u z1 = *(const LAS v2u *)(x.sm + M_ZA + (unsigned)(h * 2 + 1) * 32u + (unsigned)x.g * 8u);
        const f4 num = mfma32(ka, phq[h], f4{0.f, 0.f, 0.f, 0.f});
        const f4 den = mfma32(v4u{z0.x, z0.y, z1.x, z1.y}, phq[h], f4{0.f, 0.f, 0.f, 0.f});
        const float r = __builtin_amdgcn_rcpf(den.x + 1e-6f);
        ow[2 * h] = pk2(num.x * r, num.y * r); ow[2 * h + 1] = pk2(num.z * r, num.w * r);
      }
      stg(x.rs, x.lane16, lb + O_OB + (unsigned)(x.TK * 16 + x.pp) * KB, clamp4(v4u{ow[0], ow[1], ow[2], ow[3]}));
    }
    GBAR();
    {
      refresh(x);
      unsigned lb = lb0;
      asm volatile("" : "+s"(lb));
      sleepn(DLY_A);
      v4u bf[8];
      const unsigned so = lb + O_OB + (unsigned)(x.T * 16 + 8 * x.kh) * KB;
#pragma unroll
      for (int i = 0; i < 8; ++i) bf[i] = ldg(x.rs, x.lane16 + i * KB, so);
      WAITV_BAR(8);
      {
        const int u = x.pp >> 1, odd = x.pp & 1;
        const unsigned hb = lb + O_H3B + (unsigned)(x.TK * 56 + 7 * u) * KB;
#pragma unroll
        for (int r = 0; r < 3; ++r) stg(x.rs, x.lane16, hb + (unsigned)(odd ? 4 + r : r) * KB, POISON);
        stg2(x.rs, x.lane16 + (unsigned)odd * 8u, hb + 3 * KB, v2u{SENT, SENT});
      }
      f4 acc[4];
      bool redo;
      do {
#pragma unroll
        for (int f = 0; f < 4; ++f) acc[f] = f4{0.f, 0.f, 0.f, 0.f};
        unsigned mx = 0;
        kloop1<POS2>(x, bf, acc, mx);
        redo = anybad(mx) && x.spins <= SPIN_CAP;
        if (__builtin_expect(redo, 0)) {
#pragma unroll
          for (int i = 0; i < 8; ++i) fix4(x, bf[i], x.lane16 + i * KB, so);
        }
      } while (__builtin_expect(redo, 0));
      BAR();
      xchg4<POS2>(x, acc);
      x1r = clamp4(v4u{pk2(blo(xres.x) + acc[0].x, bhi(xres.x) + acc[0].y), pk2(blo(xres.y) + acc[0].z, bhi(xres.y) + acc[0].w),
                       pk2(blo(xres.z) + acc[1].x, bhi(xres.z) + acc[1].y), pk2(blo(xres.w) + acc[1].z, bhi(xres.w) + acc[1].w)});
      stg(x.rs, x.lane16, lb + O_X1B + x.fragO, x1r);
      put_stats(x, x1r, lb + O_X1S + x.statO);
    }
    float mean1, rstd1;
    GBAR();
    {
      refresh(x);
      unsigned lb = lb0;
      asm volatile("" : "+s"(lb));
      sleepn(DLY_A);
      v4u bf[8];
      const unsigned so = lb + O_X1B + (unsigned)(x.T * 16 + 8 * x.kh) * KB;
#pragma unroll
      for (int i = 0; i < 8; ++i) bf[i] = ldg(x.rs, x.lane16 + i * KB, so);
      Stats st;
      stats_load(x, st, lb + O_X1S + (unsigned)x.T * 2048u);
      WAITV_BAR(12);
      f4 acc[4];
      bool redo;
      do {
#pragma unroll
        for (int f = 0; f < 4; ++f) acc[f] = f4{0.f, 0.f, 0.f, 0.f};
        unsigned mx = 0;
        kloop1<POS3>(x, bf, acc, mx);
        redo = anybad(mx) && x.spins <= SPIN_CAP;
        if (__builtin_expect(redo, 0)) {
#pragma unroll
          for (int i = 0; i < 8; ++i) fix4(x, bf[i], x.lane16 + i * KB, so);
        }
      } while (__builtin_expect(redo, 0));
      float var;
      stats_fin(x, st, lb + O_X1S + (unsigned)x.T * 2048u, mean1, var);
      dma_range<POS4 + 14, POS5, POS4>(x, wsrc(x, W4_F, x.c, 32));
      dma_range<POS5, POS5 + 14, POS5>(x, wsrc(x, W5_F, x.pp, 56));
      BAR();
      xchg4<POS3 % RING>(x, acc);
      rstd1 = __builtin_amdgcn_rsqf(var + 1e-5f);
      const float a = rstd1 * __builtin_amdgcn_rsqf(__builtin_fmaf(var * rstd1, rstd1, 1e-5f)), ma = mean1 * a;
      const f4 c0 = ldcs(x, 6 + 2 * x.kh), c1 = ldcs(x, 7 + 2 * x.kh);
      const f4 y0 = acc[0] * a - c0 * ma, y1 = acc[1] * a - c1 * ma;
      const v4u hv = clamp4(v4u{pk2(gelu(y0.x), gelu(y0.y)), pk2(gelu(y0.z), gelu(y0.w)), pk2(gelu(y1.x), gelu(y1.y)), pk2(gelu(y1.z), gelu(y1.w))});
      stg(x.rs, x.lane16, lb + O_HB + x.fragO, hv);
    }
    GBAR();
    {
      refresh(x);
      unsigned lb = lb0;
      asm volatile("" : "+s"(lb));
      sleepn(DLY_A);
      v4u bf[8];
      unsigned vo[2];
#pragma unroll
      for (int q = 0; q < 2; ++q) {
        const int d = x.kh == 0 ? (q == 0 ? -1 : 1) : (q == 0 ? -2 : 2);
        const int src = 16 * x.T + x.tok + d;
        vo[q] = (src >= 0 && src < 2048) ? (unsigned)((src >> 4) * 16 * 1024 + (x.g * 16 + (src & 15)) * 16) : OOB;
      }
      const unsigned so = lb + O_HB + (unsigned)(8 * x.kh) * KB;
#pragma unroll
      for (int i = 0; i < 8; ++i) bf[i] = ldg(x.rs, vo[i >> 2] + i * KB, so);
      WAITV_BAR(8);
      if (L == 0) {
        stg(x.rs, x.lane16, lb + L_STRIDE + O_XB + x.fragO, POISON);
        if (x.g == 0) stg2(x.rs, (unsigned)x.tok * 8u, lb + L_STRIDE + O_XS + x.statO, v2u{SENT, SENT});
      }
      f4 acc[4];
      bool redo;
      do {
#pragma unroll
        for (int f = 0; f < 4; ++f) acc[f] = f4{0.f, 0.f, 0.f, 0.f};
        unsigned mx = 0;
        kloop1<POS4>(x, bf, acc, mx);
        redo = anybad(mx) && x.spins <= SPIN_CAP;
        if (__builtin_expect(redo, 0)) {
#pragma unroll
          for (int i = 0; i < 8; ++i) fix4(x, bf[i], vo[i >> 2] + i * KB, so);
        }
      } while (__builtin_expect(redo, 0));
      dma_range<POS5 + 14, POS5 + 46, POS5>(x, wsrc(x, W5_F, x.pp, 56));
      BAR();
      dma_range<POS5 + 46, POS6, POS5>(x, wsrc(x, W5_F, x.pp, 56));
      xchg4<(POS4 + 24) % RING>(x, acc);
      const float mr = mean1 * rstd1;
      x2r = clamp4(v4u{pk2(__builtin_fmaf(blo(x1r.x), rstd1, -mr) + acc[0].x, __builtin_fmaf(bhi(x1r.x), rstd1, -mr) + acc[0].y),
                       pk2(__builtin_fmaf(blo(x1r.y), rstd1, -mr) + acc[0].z, __builtin_fmaf(bhi(x1r.y), rstd1, -mr) + acc[0].w),
                       pk2(__builtin_fmaf(blo(x1r.z), rstd1, -mr) + acc[1].x, __builtin_fmaf(bhi(x1r.z), rstd1, -mr) + acc[1].y),
                       pk2(__builtin_fmaf(blo(x1r.w), rstd1, -mr) + acc[1].z, __builtin_fmaf(bhi(x1r.w), rstd1, -mr) + acc[1].w)});
      stg(x.rs, x.lane16, lb + O_X2B + x.fragO, x2r);
      put_stats(x, x2r, lb + O_X2S + x.statO);
    }
    GBAR();
    {
      refresh(x);
      unsigned lb = lb0;
      asm volatile("" : "+s"(lb));
      sleepn(DLY_A);
      v4u bf[8][2];
      const unsigned sk = lb + O_X2B + (unsigned)(x.TK * 16 + 8 * x.kh) * KB, ss = lb + O_X2B + (unsigned)(x.TS * 16 + 8 * x.kh) * KB;
#pragma unroll
      for (int i = 0; i < 8; ++i) { bf[i][0] = ldg(x.rs, x.lane16 + i * KB, sk); bf[i][1] = ldg(x.rs, x.lane16 + i * KB, ss); }
      Stats st;
      stats_load(x, st, lb + O_X2S + (unsigned)x.TK * 2048u);
      WAITV_BAR(20);
      if (L == 0) {
        stg(x.rs, x.lane16, lb + L_STRIDE + O_OB + (unsigned)(x.TK * 16 + x.pp) * KB, POISON);
        stg(x.rs, x.lane16, lb + L_STRIDE + O_HB + x.fragO, POISON);
      }
      f4 acc[7][2];
      bool redo;
      do {
#pragma unroll
        for (int f = 0; f < 7; ++f) { acc[f][0] = f4{0.f, 0.f, 0.f, 0.f}; acc[f][1] = f4{0.f, 0.f, 0.f, 0.f}; }
        unsigned mx = 0;
        kloop2<7, POS5>(x, bf, acc, mx);
        redo = anybad(mx) && x.spins <= SPIN_CAP;
        if (__builtin_expect(redo, 0)) {
#pragma unroll
          for (int i = 0; i < 8; ++i) { fix4(x, bf[i][0], x.lane16 + i * KB, sk); fix4(x, bf[i][1], x.lane16 + i * KB, ss); }
        }
      } while (__builtin_expect(redo, 0));
      float mean, var;
      stats_fin(x, st, lb + O_X2S + (unsigned)x.TK * 2048u, mean, var);
      const unsigned w6s = wsrc(x, W6_F, x.c, 112);
      dma_range<POS6, POS6 + 22, POS6>(x, w6s);
      BAR();
      dma_range<POS6 + 22, POS6 + 50, POS6>(x, w6s);
      xchg2<7, 16>(x, acc);
      BAR();
      dma_range<POS6 + 50, POS6 + 78, POS6>(x, w6s);
      const float rstd = __builtin_amdgcn_rsqf(var + 1e-5f), mr = mean * rstd;
      unsigned hw[7][2];
#pragma unroll
      for (int f = 0; f < 7; ++f) {
        const f4 y = acc[f][0] * rstd - ldcs(x, 10 + f) * mr;
        hw[f][0] = pk2(gelu(y.x), gelu(y.y)); hw[f][1] = pk2(gelu(y.z), gelu(y.w));
      }
      const int u = x.pp >> 1, odd = x.pp & 1;
      const unsigned hb = lb + O_H3B + (unsigned)(x.TK * 56 + 7 * u) * KB;
#pragma unroll
      for (int r = 0; r < 3; ++r)
        stg(x.rs, x.lane16, hb + (unsigned)(odd ? 4 + r : r) * KB, clamp4(v4u{hw[2 * r][0], hw[2 * r][1], hw[2 * r + 1][0], hw[2 * r + 1][1]}));
      stg2(x.rs, x.lane16 + (unsigned)odd * 8u, hb + 3 * KB, v2u{clampw(hw[6][0]), clampw(hw[6][1])});
    }
    GBAR();
    {
      refresh(x);
      unsigned lb = lb0;
      asm volatile("" : "+s"(lb));
      sleepn(DLY_A);
      v4u hf[28];
      const unsigned so = lb + O_H3B + (unsigned)(x.T * 56 + 28 * x.kh) * KB;
      const unsigned w6s = wsrc(x, W6_F, x.c, 112);
      const unsigned w1n = L == 0 ? wsrc(x, W1_F, x.pp, 48) : OOB;
      asm volatile("s_nop 4" ::: "memory");
#define LD4(k0) LDA(hf[k0], x.lane16, so + (k0) * KB, 0); LDA(hf[k0 + 1], x.lane16, so + (k0) * KB, 1024); \
                LDA(hf[k0 + 2], x.lane16, so + (k0) * KB, 2048); LDA(hf[k0 + 3], x.lane16, so + (k0) * KB, 3072)
      LD4(0); LD4(4); LD4(8); LD4(12); LD4(16);
      LDA(hf[20], x.lane16, so + 20 * KB, 0);
      WAITV_BAR(21);
      f4 acc[4];
#pragma unroll
      for (int f = 0; f < 4; ++f) acc[f] = f4{0.f, 0.f, 0.f, 0.f};
      p6chunk<0, 7, 20>(x, hf, acc, so);
      BAR();
      dma_range<POS6 + 78, POS6 + 106, POS6>(x, w6s);
      asm volatile("s_nop 4" ::: "memory");
      LDA(hf[21], x.lane16, so + 21 * KB, 0); LDA(hf[22], x.lane16, so + 21 * KB, 1024); LDA(hf[23], x.lane16, so + 21 * KB, 2048);
      LDA(hf[24], x.lane16, so + 21 * KB, 3072); LDA(hf[25], x.lane16, so + 25 * KB, 0); LDA(hf[26], x.lane16, so + 25 * KB, 1024);
      LDA(hf[27], x.lane16, so + 25 * KB, 2048);
      p6chunk<7, 14, 27>(x, hf, acc, so);
      BAR();
      {
        asm volatile("s_nop 4" ::: "memory");
        const unsigned pa = 106u + (unsigned)x.m, pb = 110u + (unsigned)(x.m & 1);
        dma1(x, x.lds0 + (unsigned)x.kh * HALF_B + ((POS6 + pa) % RING) * KB, w6s + pa * KB);
        dma1(x, x.lds0 + (unsigned)x.kh * HALF_B + ((POS6 + pb) % RING) * KB, w6s + pb * KB);
        dma_range<POSE, POSE + 20, POSE>(x, w1n);
      }
      p6chunk<14, 19, 27>(x, hf, acc, so);
      WAITV_BAR(19);
      p6chunk<19, 21, 22>(x, hf, acc, so);
      WAITV_BAR(13);
      dma_range<POSE + 20, POSE + 48, POSE>(x, w1n);
      p6chunk<21, 26, 20>(x, hf, acc, so);
      WAITV_BAR(12);
      p6chunk<26, 28, -1>(x, hf, acc, so);
      BAR();
      xchg4<70>(x, acc);
      xres = clamp4(v4u{pk2(blo(x1r.x) + (blo(x2r.x) + acc[0].x), bhi(x1r.x) + (bhi(x2r.x) + acc[0].y)),
                        pk2(blo(x1r.y) + (blo(x2r.y) + acc[0].z), bhi(x1r.y) + (bhi(x2r.y) + acc[0].w)),
                        pk2(blo(x1r.z) + (blo(x2r.z) + acc[1].x), bhi(x1r.z) + (bhi(x2r.z) + acc[1].y)),
                        pk2(blo(x1r.w) + (blo(x2r.w) + acc[1].z), bhi(x1r.w) + (bhi(x2r.w) + acc[1].w))});
      if (L == 0) {
        stg(x.rs, x.lane16, lb + L_STRIDE + O_XB + x.fragO, xres);
        put_stats(x, xres, lb + L_STRIDE + O_XS + x.statO);
        sleepn(DLY_A);
      } else {
        GAS unsigned char *op = (GAS unsigned char *)p.out + ((size_t)(16 * x.T + x.tok) * 512 + 32 * x.sO + 4 * x.g) * 2;
        *(GAS v2u *)op = v2u{xres.x, xres.y};
        *(GAS v2u *)(op + 32) = v2u{xres.z, xres.w};
      }
    }
  }
  asm volatile("s_waitcnt vmcnt(0)" ::: "memory");
}

extern "C" void kernel_launch(void *const *d_in, const int *in_sizes, int n_in, void *d_out, int out_size, void *d_ws, size_t ws_size,
                              hipStream_t stream) {
  Params p;
  p.x = (const unsigned short *)d_in[0];
  p.wqkv = (const unsigned short *)d_in[1];
  p.wphi = (const unsigned short *)d_in[2];
  p.wo = (const unsigned short *)d_in[3];
  p.w1 = (const unsigned short *)d_in[5];
  p.w2 = (const unsigned short *)d_in[6];
  p.w3 = (const unsigned short *)d_in[7];
  p.w4 = (const unsigned short *)d_in[8];
  p.out = (unsigned short *)d_out;
  p.ws = (unsigned char *)d_ws;
  hipFuncSetAttribute((const void *)mega_kernel, hipFuncAttributeMaxDynamicSharedMemorySize, LDS_BYTES);
  hipLaunchKernelGGL(mega_kernel, dim3(256), dim3(512), LDS_BYTES, stream, p);
}
```

```cpp
#include <hip/hip_runtime.h>
#include <stdint.h>

#define GAS __attribute__((address_space(1)))
#define LAS __attribute__((address_space(3)))
#define DI __device__ __forceinline__
typedef unsigned v4u __attribute__((ext_vector_type(4)));
typedef unsigned v2u __attribute__((ext_vector_type(2)));
typedef float f4 __attribute__((ext_vector_type(4)));
typedef float f2 __attribute__((ext_vector_type(2)));
typedef short s8 __attribute__((ext_vector_type(8)));
typedef short s4 __attribute__((ext_vector_type(4)));
typedef __bf16 bf2 __attribute__((ext_vector_type(2)));
typedef GAS unsigned gu32;
typedef __amdgpu_buffer_rsrc_t rsrc_t;
#define RLX_AGENT __ATOMIC_RELAXED, __HIP_MEMORY_SCOPE_AGENT

#define DBG_GBAR 0
#define DBG_NOPOISON 0
#define DBG_STAMP 0
#define SPEC 0

namespace {
constexpr unsigned MB = 1u << 20, KB = 1024u;
constexpr unsigned OFF_EPOCH = 0, OFF_ABORT = 256, OFF_GBAR = 512, OFF_FLAGS = 16384, OFF_FLAGS2 = 32768;
constexpr unsigned OFF_WT = 1 * MB;
constexpr unsigned W1_F = 0, W2_F = 1536, W5_F = 3072, W6_F = 4864;
constexpr unsigned OFF_CSP = 8 * MB;
constexpr unsigned OFF_L0 = 9 * MB, L_STRIDE = 24 * MB;
constexpr unsigned O_XB = 0, O_XS = 2 * MB, O_KVP = 2 * MB + 512 * KB, O_KVZ = 3 * MB + 512 * KB, O_OB = 4 * MB, O_X1B = 6 * MB,
                   O_X1S = 8 * MB, O_HB = 9 * MB, O_X2B = 11 * MB, O_X2S = 13 * MB, O_H3B = 14 * MB;
constexpr unsigned WS_BYTES = OFF_L0 + 2 * L_STRIDE;
constexpr unsigned OOB = 0x7F000000u;
constexpr int RING = 156;
constexpr unsigned MISC = RING * KB;
constexpr unsigned M_CS = MISC, M_KVA = MISC + 1088, M_ZA = MISC + 3136, M_SIG = MISC + 3264, M_SIG2 = MISC + 3280, M_RDY = MISC + 3296, M_FLAG = MISC + 3300;
constexpr unsigned IMG = 0;
constexpr unsigned LDS_BYTES = 163840;
constexpr int POS1 = 0, POS2 = 96, POS3 = 160, POS4 = 224, POS5 = 288, POS6 = 400, POSE = 624;
constexpr unsigned SENT = 0xFFFFFFFFu;
constexpr unsigned SPIN_CAP = 1u << 13;
constexpr int AUX_SC1 = 16;
#define DLY_KV 16
#define DLY_1 16
#define DLY_2 16
#define DLY_3 16
#define DLY_4 16
#define DLY_5 16
#define DLY_6 16

struct Params {
  const unsigned short *x, *wqkv, *wphi, *wo, *w1, *w2, *w3, *w4;
  unsigned short *out;
  unsigned char *ws;
};

DI unsigned pk2(float a, float b) { f2 v = {a, b}; bf2 r = __builtin_convertvector(v, bf2); return __builtin_bit_cast(unsigned, r); }
DI float asf(unsigned u) { return __builtin_bit_cast(float, u); }
DI float blo(unsigned u) { return __builtin_bit_cast(float, u << 16); }
DI float bhi(unsigned u) { return __builtin_bit_cast(float, u & 0xFFFF0000u); }
DI unsigned clampw(unsigned u) { return u < 0xFFFFFFFEu ? u : 0xFFFFFFFEu; }
DI unsigned clampf(float f) { return clampw(__builtin_bit_cast(unsigned, f)); }
DI v4u clamp4(v4u v) { return v4u{clampw(v.x), clampw(v.y), clampw(v.z), clampw(v.w)}; }
DI v4u pack8(f4 a, f4 b) { return clamp4(v4u{pk2(a.x, a.y), pk2(a.z, a.w), pk2(b.x, b.y), pk2(b.z, b.w)}); }
DI f4 ulo(v4u v) { return f4{blo(v.x), bhi(v.x), blo(v.y), bhi(v.y)}; }
DI f4 uhi(v4u v) { return f4{blo(v.z), bhi(v.z), blo(v.w), bhi(v.w)}; }
DI f4 mfma32(v4u a, v4u b, f4 c) {
  return __builtin_amdgcn_mfma_f32_16x16x32_bf16(__builtin_bit_cast(s8, a), __builtin_bit_cast(s8, b), c, 0, 0, 0);
}
DI f4 mfma16(v2u a, v2u b, f4 c) {
  return __builtin_amdgcn_mfma_f32_16x16x16bf16_1k(__builtin_bit_cast(s4, a), __builtin_bit_cast(s4, b), c, 0, 0, 0);
}
DI v4u ldg(rsrc_t rs, unsigned voff, unsigned soff) { return __builtin_amdgcn_raw_buffer_load_b128(rs, voff, soff, AUX_SC1); }
DI void stg(rsrc_t rs, unsigned voff, unsigned soff, v4u v) {
  __builtin_amdgcn_raw_buffer_store_b128(v, rs, voff, soff, AUX_SC1);
  asm volatile("s_nop 1" ::"v"(v));
}
DI void stg2(rsrc_t rs, unsigned voff, unsigned soff, v2u v) { __builtin_amdgcn_raw_buffer_store_b64(v, rs, voff, soff, AUX_SC1); }
DI void poi4(rsrc_t rs, unsigned voff, unsigned soff) { if (!DBG_NOPOISON) stg(rs, voff, soff, v4u{0xFFFFFFFFu, 0xFFFFFFFFu, 0xFFFFFFFFu, 0xFFFFFFFFu}); }
DI void poi2(rsrc_t rs, unsigned voff, unsigned soff) { if (!DBG_NOPOISON) stg2(rs, voff, soff, v2u{0xFFFFFFFFu, 0xFFFFFFFFu}); }
DI unsigned max4(v4u v) { return max(max(v.x, v.y), max(v.z, v.w)); }
DI bool anybad(unsigned mx) { return __any(mx == SENT) != 0; }
DI float xsum(float x) {
  unsigned u = __builtin_bit_cast(unsigned, x);
  auto r = __builtin_amdgcn_permlane16_swap(u, u, false, false);
  x = __builtin_bit_cast(float, (unsigned)r[0]) + __builtin_bit_cast(float, (unsigned)r[1]);
  u = __builtin_bit_cast(unsigned, x);
  auto q = __builtin_amdgcn_permlane32_swap(u, u, false, false);
  return __builtin_bit_cast(float, (unsigned)q[0]) + __builtin_bit_cast(float, (unsigned)q[1]);
}
DI float xmax(float x) {
  unsigned u = __builtin_bit_cast(unsigned, x);
  auto r = __builtin_amdgcn_permlane16_swap(u, u, false, false);
  x = fmaxf(__builtin_bit_cast(float, (unsigned)r[0]), __builtin_bit_cast(float, (unsigned)r[1]));
  u = __builtin_bit_cast(unsigned, x);
  auto q = __builtin_amdgcn_permlane32_swap(u, u, false, false);
  return fmaxf(__builtin_bit_cast(float, (unsigned)q[0]), __builtin_bit_cast(float, (unsigned)q[1]));
}
DI f2 gelu2(f2 x) {
  const f2 c1 = {-2.302208198f, -2.302208198f}, c2 = {-0.1029432397f, -0.1029432397f}, one = {1.0f, 1.0f};
  const f2 t = x * __builtin_elementwise_fma(x * x, c2, c1);
  const f2 d = one + f2{__builtin_amdgcn_exp2f(t.x), __builtin_amdgcn_exp2f(t.y)};
  return x * f2{__builtin_amdgcn_rcpf(d.x), __builtin_amdgcn_rcpf(d.y)};
}
DI f4 gelu4(f4 y) {
  const f2 a = gelu2(f2{y.x, y.y}), b = gelu2(f2{y.z, y.w});
  return f4{a.x, a.y, b.x, b.y};
}

#define FSL 1
#define FS(i) do {} while (0)
#define FSA(i) do { unsigned long long t_; asm volatile("s_memtime %0\n\ts_waitcnt lgkmcnt(0)" : "=s"(t_) :: "memory"); \
  int ww_ = x.w; asm volatile("" : "+s"(ww_)); \
  unsigned a_ = (ww_ == 0) ? (70 + (i)) * 4u : ((ww_ == 4) ? 400u + (70 + (i)) * 4u : 780u); \
  asm volatile("" : "+s"(a_)); \
  *(LAS unsigned *)(x.sm + M_FLAG + a_) = (unsigned)t_; } while (0)
#define FSP(i) do {} while (0)
#define SB() __builtin_amdgcn_sched_barrier(0)
#define MARK(s) asm volatile("; MARK " s)
#define BAR() asm volatile("s_waitcnt lgkmcnt(0)\n\ts_barrier" ::: "memory")
#define DRAIN_BAR() asm volatile("s_waitcnt vmcnt(0) lgkmcnt(0)\n\ts_barrier" ::: "memory")
DI void sleepn(int n) {
  for (int i = 0; i < n; ++i) __builtin_amdgcn_s_sleep(1);
}

struct Ctx {
  rsrc_t rs;
  LAS unsigned char *sm;
  unsigned lds0;
  unsigned lane16;
  LAS unsigned char *ab[3];
  int lane, w, m, g, tok;
  int t, c, tau, pp, b;
  int T, TA;
  unsigned spins;
  gu32 *ctl;
};
DI void refresh(Ctx &x) {
  asm volatile("" : "+s"(x.w), "+s"(x.b), "+v"(x.lane));
  x.lane16 = (unsigned)x.lane * 16u;
  x.g = x.lane >> 4; x.tok = x.lane & 15;
  x.m = x.w & 3;
  const int xc = x.b & 7, j = x.b >> 3;
  x.t = 4 * xc + (j >> 3); x.c = j & 7;
  x.tau = 2 * xc + (j >> 4); x.pp = j & 15;
  x.T = 4 * x.t + x.m;
  x.TA = 8 * x.tau + 2 * x.m;
  x.ab[0] = x.sm + x.lane16;
  unsigned o1 = x.lane16 + 60u * KB, o2 = x.lane16 + 120u * KB;
  asm volatile("" : "+v"(o1), "+v"(o2));
  x.ab[1] = x.sm + o1; x.ab[2] = x.sm + o2;
}

template <int P> DI void dmaP(const Ctx &x, bool live = true) {
  constexpr int Q = P % POSE;
  int w = x.w, b = x.b;
  asm volatile("" : "+s"(w), "+s"(b));
  const int m = w & 3, j = b >> 3;
  unsigned slot = (unsigned)(P % RING) + (unsigned)m;
  unsigned fr;
  if constexpr (Q < POS2) fr = W1_F + (unsigned)((j & 15) * 96 + Q + m);
  else if constexpr (Q < POS5) fr = W2_F + (unsigned)(((Q - POS2) / 64) * 512 + (j & 7) * 64 + (Q - POS2) % 64 + m);
  else if constexpr (Q < POS6) fr = W5_F + (unsigned)((j & 15) * 112 + (Q - POS5) + m);
  else fr = W6_F + (unsigned)((j & 7) * 224 + (Q - POS6) + m);
  const unsigned lds = x.lds0 + slot * KB, so = live ? OFF_WT + fr * KB : OOB;
  asm volatile("s_mov_b32 m0, %0\n\ts_nop 0\n\tbuffer_load_dwordx4 %1, %2, %3 offen sc1 lds" ::"s"(lds), "v"(x.lane16), "s"(x.rs), "s"(so) : "memory");
}
template <int LO, int HI> DI void dmaR(const Ctx &x, bool live = true) {
  if constexpr (LO < HI) { dmaP<LO>(x, live); dmaR<LO + 4, HI>(x, live); }
}
template <int LO, int HI> DI void dmaRT(const Ctx &x, bool live = true) {
  if constexpr (LO < HI) { dmaP<LO>(x, live); asm volatile("s_waitcnt vmcnt(3)" ::: "memory"); dmaRT<LO + 4, HI>(x, live); }
}
template <int LO, int HI> DI void dmaR6(const Ctx &x, bool live = true) {
  if constexpr (LO < HI) { dmaP<LO>(x, live); asm volatile("s_waitcnt vmcnt(5)" ::: "memory"); dmaR6<LO + 4, HI>(x, live); }
}
DI void sig_set(const Ctx &x, unsigned v, unsigned word = M_SIG) { *(volatile LAS unsigned *)(x.sm + word + (unsigned)x.m * 4u) = v; }
DI void sig_wait(const Ctx &x, unsigned v, unsigned word = M_SIG) {
  for (unsigned sp = 0; sp < (1u << 20); ++sp) {
    const v4u s = *(volatile LAS v4u *)(x.sm + word);
    if (__builtin_amdgcn_readfirstlane(min(min(s.x, s.y), min(s.z, s.w))) >= v) break;
    __builtin_amdgcn_s_sleep(1);
  }
}

#define LDA(dst, voff, soff) \
  asm volatile("buffer_load_dwordx4 %0, %1, %2, %3 offen sc1" : "=v"(dst) : "v"(voff), "s"(x.rs), "s"(soff) : "memory")
#define LDA2(dst, voff, soff, imm) \
  asm volatile("buffer_load_dwordx2 %0, %1, %2, %3 offen offset:%4 sc1" : "=v"(dst) : "v"(voff), "s"(x.rs), "s"(soff), "i"(imm) : "memory")
#define WAITV1(n, r) asm volatile("s_waitcnt vmcnt(%1)" : "+v"(r) : "i"(n) : "memory")
#define WAITV2(n, r, q) asm volatile("s_waitcnt vmcnt(%2)" : "+v"(r), "+v"(q) : "i"(n) : "memory")
#define TIE(r) asm volatile("" : "+v"(r) : : "memory")
DI bool giveup(Ctx &x) {
  if (++x.spins > SPIN_CAP) {
    if (x.lane == 0) __hip_atomic_store(x.ctl + OFF_ABORT / 4, 1u, RLX_AGENT);
    return true;
  }
  __builtin_amdgcn_s_sleep(1);
  return false;
}
DI v4u load_sure(Ctx &x, unsigned voff, unsigned soff) {
  v4u v;
  while (true) {
    asm volatile("buffer_load_dwordx4 %0, %1, %2, %3 offen sc1\n\ts_waitcnt vmcnt(0)" : "=&v"(v) : "v"(voff), "s"(x.rs), "s"(soff) : "memory");
    if (!anybad(max4(v)) || giveup(x)) break;
  }
  return v;
}
DI v2u load_sure2(Ctx &x, unsigned voff, unsigned soff) {
  v2u v;
  while (true) {
    asm volatile("buffer_load_dwordx2 %0, %1, %2, %3 offen sc1\n\ts_waitcnt vmcnt(0)" : "=&v"(v) : "v"(voff), "s"(x.rs), "s"(soff) : "memory");
    if (!anybad(max(v.x, v.y)) || giveup(x)) break;
  }
  return v;
}

#define LDC(dst, voff, soff) \
  asm volatile("buffer_load_dword %0, %1, %2, %3 offen sc1" : "=v"(dst) : "v"(voff), "s"(x.rs), "s"(soff) : "memory")

#define GBAR() do {} while (0)
#define SLEEP(n) sleepn(n)

DI void put_stats(const Ctx &x, v4u u, v4u v, unsigned sbuf) {
  const f4 a = ulo(u), b = uhi(u), c = ulo(v), d = uhi(v);
  const f4 s = (a + b) + (c + d), q = (a * a + b * b) + (c * c + d * d);
  const float s1 = xsum((s.x + s.y) + (s.z + s.w)), s2 = xsum((q.x + q.y) + (q.z + q.w));
  if (x.g == 0) stg2(x.rs, (unsigned)x.tok * 8u, sbuf + (unsigned)(x.T * 8 + x.c) * 128u, v2u{clampf(s1), clampf(s2)});
}
struct Stats { v2u v[2]; };
DI void stats_load(const Ctx &x, Stats &s, unsigned soffT) {
  const unsigned vo = (unsigned)(x.g * 256 + x.tok * 8);
  LDA2(s.v[0], vo, soffT, 0); LDA2(s.v[1], vo, soffT, 128);
}
DI void stats_fin(Ctx &x, Stats &s, unsigned soffT, float &mean, float &var) {
  TIE(s.v[0]); TIE(s.v[1]);
  if (__builtin_expect(anybad(max(max(s.v[0].x, s.v[0].y), max(s.v[1].x, s.v[1].y))), 0)) {
    s.v[0] = load_sure2(x, (unsigned)(x.g * 256 + x.tok * 8), soffT);
    s.v[1] = load_sure2(x, (unsigned)(x.g * 256 + x.tok * 8 + 128), soffT);
  }
  const float s1 = xsum(asf(s.v[0].x) + asf(s.v[1].x)), s2 = xsum(asf(s.v[0].y) + asf(s.v[1].y));
  mean = s1 * (1.0f / 512.0f);
  var = fmaxf(__builtin_fmaf(-mean, mean, s2 * (1.0f / 512.0f)), 0.0f);
}
DI const LAS v4u *lds4(const Ctx &x, unsigned off) { return (const LAS v4u *)(x.sm + off); }
template <int NT, int P> DI void ldaN(const Ctx &x, v4u (&a)[NT]) {
#pragma unroll
  for (int f = 0; f < NT; ++f) {
    const int slot = (P + f) % RING;
    a[f] = *(const LAS v4u *)(x.ab[slot / 60] + (slot % 60) * KB);
  }
}
template <int NT> DI void tieN(v4u (&a)[NT]) {
  if constexpr (NT == 4) asm volatile("" : "+v"(a[0]), "+v"(a[1]), "+v"(a[2]), "+v"(a[3]));
  else if constexpr (NT == 6) asm volatile("" : "+v"(a[0]), "+v"(a[1]), "+v"(a[2]), "+v"(a[3]), "+v"(a[4]), "+v"(a[5]));
  else asm volatile("" : "+v"(a[0]), "+v"(a[1]), "+v"(a[2]), "+v"(a[3]), "+v"(a[4]), "+v"(a[5]), "+v"(a[6]));
}
DI unsigned mx4(unsigned m, v4u v) {
  unsigned r;
  asm("v_max3_u32 %0, %1, %2, %3" : "=v"(r) : "v"(m), "v"(v.x), "v"(v.y));
  asm("v_max3_u32 %0, %1, %2, %3" : "=v"(m) : "v"(r), "v"(v.z), "v"(v.w));
  return m;
}
DI f4 ldcs(const Ctx &x, int tile) { return *(const LAS f4 *)(x.sm + M_CS + (unsigned)tile * 64u + (unsigned)x.g * 16u); }
constexpr int imin(int a, int b) { return a < b ? a : b; }

template <bool SH> DI unsigned vof(const Ctx &x, int k, const unsigned (&vo)[4]) { return SH ? vo[k >> 2] : x.lane16; }
template <int NK> constexpr int depth1() { return 8; }
template <int POS, int NK, bool SH, int K0, int K1, int I> struct KL1 {
  static DI void run(Ctx &x, v4u (&bf)[NK], f4 (&acc)[4], unsigned &mx, v4u (&cur)[4], v4u (&nxt)[4], unsigned so, const unsigned (&vo)[4],
                     unsigned sig) {
    v4u nn[4];
    constexpr int DP = depth1<NK>();
    WAITV1(imin(DP - 1, NK - 1 - I), bf[I]);
    if constexpr (I + DP < NK) LDA(bf[I + DP], vof<SH>(x, I + DP, vo), so + (unsigned)(I + DP) * KB);
    if constexpr (I + DP == NK - 1) { if (sig) sig_set(x, sig); }
    tieN<4>(cur);
    SB();
    if constexpr (I + 2 < K1) ldaN<4, POS + 4 * (I + 2)>(x, nn);
    mx = mx4(mx, bf[I]);
#pragma unroll
    for (int f = 0; f < 4; ++f) acc[f] = mfma32(cur[f], bf[I], acc[f]);
#pragma unroll
    for (int f = 0; f < 4; ++f) {
      __builtin_amdgcn_sched_group_barrier(0x008, 1, 0);
      __builtin_amdgcn_sched_group_barrier(0x100, 1, 0);
      __builtin_amdgcn_sched_group_barrier(0x002, 1, 0);
    }
    SB();
    if constexpr (I + 1 < K1) KL1<POS, NK, SH, K0, K1, I + 1>::run(x, bf, acc, mx, nxt, nn, so, vo, sig);
  }
};
template <int POS, bool SH, int K1, int I> struct KL1R {
  static DI void run(Ctx &x, f4 (&acc)[4], unsigned so, const unsigned (&vo)[4]) {
    const v4u bv = load_sure(x, vof<SH>(x, I, vo), so + (unsigned)I * KB);
    v4u a[4];
    ldaN<4, POS + 4 * I>(x, a);
#pragma unroll
    for (int f = 0; f < 4; ++f) acc[f] = mfma32(a[f], bv, acc[f]);
    if constexpr (I + 1 < K1) KL1R<POS, SH, K1, I + 1>::run(x, acc, so, vo);
  }
};
template <bool ST> DI unsigned canary_poll1(Ctx &x, unsigned so, unsigned cvo, Stats &st, unsigned sst) {
  unsigned cn;
  LDC(cn, cvo, so);
  if constexpr (ST) stats_load(x, st, sst);
  WAITV1(0, cn);
  if constexpr (ST) { TIE(st.v[0]); TIE(st.v[1]); cn = max(cn, max(max(st.v[0].x, st.v[0].y), max(st.v[1].x, st.v[1].y))); }
  return cn;
}
template <int NK, bool SH, bool ST>
DI void pro1(Ctx &x, v4u (&bf)[NK], unsigned so, const unsigned (&vo)[4], unsigned cvo, Stats &st, unsigned sst) {
  while (anybad(canary_poll1<ST>(x, so, cvo, st, sst)) && !giveup(x)) {}
#pragma unroll
  for (int i = 0; i < depth1<NK>(); ++i) LDA(bf[i], vof<SH>(x, i, vo), so + (unsigned)i * KB);
  return;
  unsigned cn;
  LDC(cn, cvo, so);
  if constexpr (ST) stats_load(x, st, sst);
#pragma unroll
  for (int i = 0; i < depth1<NK>(); ++i) LDA(bf[i], vof<SH>(x, i, vo), so + (unsigned)i * KB);
  WAITV1(depth1<NK>(), cn);
  if constexpr (ST) { TIE(st.v[0]); TIE(st.v[1]); cn = max(cn, max(max(st.v[0].x, st.v[0].y), max(st.v[1].x, st.v[1].y))); }
  if (__builtin_expect(anybad(cn), 0)) {
    asm volatile("s_waitcnt vmcnt(0)" ::: "memory");
    while (!giveup(x) && anybad(canary_poll1<ST>(x, so, cvo, st, sst))) {}
#pragma unroll
    for (int i = 0; i < depth1<NK>(); ++i) LDA(bf[i], vof<SH>(x, i, vo), so + (unsigned)i * KB);
  }
}
template <int POS, int NK, bool SH, int K0, int K1>
DI void kloop1(Ctx &x, v4u (&bf)[NK], f4 (&acc)[4], unsigned so, const unsigned (&vo)[4], unsigned sig) {
  f4 save[4];
#pragma unroll
  for (int f = 0; f < 4; ++f) save[f] = acc[f];
  unsigned mx = 0;
  v4u a0[4], a1[4];
  ldaN<4, POS + 4 * K0>(x, a0);
  ldaN<4, POS + 4 * (K0 + 1)>(x, a1);
  KL1<POS, NK, SH, K0, K1, K0>::run(x, bf, acc, mx, a0, a1, so, vo, sig);
  if (__builtin_expect(anybad(mx), 0)) {
#pragma unroll
    for (int f = 0; f < 4; ++f) acc[f] = save[f];
    KL1R<POS, SH, K1, K0>::run(x, acc, so, vo);
  }
}
template <int NT, int POS, int K0, int K1, int I> struct KL2 {
  static DI void run(Ctx &x, v4u (&bf)[16][2], f4 (&acc)[NT][2], unsigned &mx, v4u (&cur)[NT], unsigned so, unsigned sig) {
    v4u nxt[NT];
    WAITV2(2 * imin(3, 15 - I), bf[I][0], bf[I][1]);
    if constexpr (I + 4 < 16) {
      LDA(bf[I + 4][0], x.lane16, so + (unsigned)(I + 4) * KB); LDA(bf[I + 4][1], x.lane16, so + (unsigned)(16 + I + 4) * KB);
    }
    if constexpr (I == (NT == 6 ? 8 : 11)) sig_set(x, sig);
    tieN<NT>(cur);
    SB();
    if constexpr (I + 1 < K1) ldaN<NT, POS + (I + 1) * NT>(x, nxt);
    mx = mx4(mx4(mx, bf[I][0]), bf[I][1]);
#pragma unroll
    for (int f = 0; f < NT; ++f) {
      acc[f][0] = mfma32(cur[f], bf[I][0], acc[f][0]);
      acc[f][1] = mfma32(cur[f], bf[I][1], acc[f][1]);
    }
#pragma unroll
    for (int f = 0; f < NT; ++f) {
      __builtin_amdgcn_sched_group_barrier(0x008, 1, 0);
      __builtin_amdgcn_sched_group_barrier(0x100, 1, 0);
    }
#pragma unroll
    for (int f = 0; f < NT; ++f) {
      __builtin_amdgcn_sched_group_barrier(0x008, 1, 0);
      __builtin_amdgcn_sched_group_barrier(0x002, 1, 0);
    }
    SB();
    if constexpr (I + 1 < K1) KL2<NT, POS, K0, K1, I + 1>::run(x, bf, acc, mx, nxt, so, sig);
  }
};
template <int NT, int POS, int K1, int I> struct KL2R {
  static DI void run(Ctx &x, f4 (&acc)[NT][2], unsigned so) {
    const v4u b0 = load_sure(x, x.lane16, so + (unsigned)I * KB), b1 = load_sure(x, x.lane16, so + (unsigned)(16 + I) * KB);
    v4u a[NT];
    ldaN<NT, POS + I * NT>(x, a);
#pragma unroll
    for (int f = 0; f < NT; ++f) {
      acc[f][0] = mfma32(a[f], b0, acc[f][0]);
      acc[f][1] = mfma32(a[f], b1, acc[f][1]);
    }
    if constexpr (I + 1 < K1) KL2R<NT, POS, K1, I + 1>::run(x, acc, so);
  }
};
DI unsigned stmax(const Stats (&st)[2]) {
  return max(max(max(st[0].v[0].x, st[0].v[0].y), max(st[0].v[1].x, st[0].v[1].y)), max(max(st[1].v[0].x, st[1].v[0].y), max(st[1].v[1].x, st[1].v[1].y)));
}
DI void pro2(Ctx &x, v4u (&bf)[16][2], Stats (&st)[2], unsigned so, unsigned sst) {
  const unsigned cvo = (unsigned)(x.lane & 31) * KB + 1020u;
  unsigned cn;
  while (true) {
    LDC(cn, cvo, so);
    stats_load(x, st[0], sst); stats_load(x, st[1], sst + 1024u);
    WAITV1(0, cn);
    TIE(st[0].v[0]); TIE(st[0].v[1]); TIE(st[1].v[0]); TIE(st[1].v[1]);
    if (!anybad(max(cn, stmax(st))) || giveup(x)) break;
  }
#pragma unroll
  for (int i = 0; i < 4; ++i) { LDA(bf[i][0], x.lane16, so + (unsigned)i * KB); LDA(bf[i][1], x.lane16, so + (unsigned)(16 + i) * KB); }
  return;
  LDC(cn, cvo, so);
  stats_load(x, st[0], sst); stats_load(x, st[1], sst + 1024u);
#pragma unroll
  for (int i = 0; i < 4; ++i) { LDA(bf[i][0], x.lane16, so + (unsigned)i * KB); LDA(bf[i][1], x.lane16, so + (unsigned)(16 + i) * KB); }
  WAITV1(8, cn);
  TIE(st[0].v[0]); TIE(st[0].v[1]); TIE(st[1].v[0]); TIE(st[1].v[1]);
  if (__builtin_expect(anybad(max(cn, stmax(st))), 0)) {
    asm volatile("s_waitcnt vmcnt(0)" ::: "memory");
    while (!giveup(x)) {
      LDC(cn, cvo, so);
      stats_load(x, st[0], sst); stats_load(x, st[1], sst + 1024u);
      WAITV1(0, cn);
      TIE(st[0].v[0]); TIE(st[0].v[1]); TIE(st[1].v[0]); TIE(st[1].v[1]);
      if (!anybad(max(cn, stmax(st)))) break;
    }
#pragma unroll
    for (int i = 0; i < 4; ++i) { LDA(bf[i][0], x.lane16, so + (unsigned)i * KB); LDA(bf[i][1], x.lane16, so + (unsigned)(16 + i) * KB); }
  }
}
template <int NT, int POS, int K0, int K1>
DI void kloop2(Ctx &x, v4u (&bf)[16][2], f4 (&acc)[NT][2], unsigned so, unsigned sig, unsigned &mx) {
  v4u a0[NT];
  ldaN<NT, POS + K0 * NT>(x, a0);
  KL2<NT, POS, K0, K1, K0>::run(x, bf, acc, mx, a0, so, sig);
}
template <int NT, int POS> DI void redo2(Ctx &x, f4 (&acc)[NT][2], unsigned so) {
#pragma unroll
  for (int f = 0; f < NT; ++f) { acc[f][0] = f4{0.f, 0.f, 0.f, 0.f}; acc[f][1] = f4{0.f, 0.f, 0.f, 0.f}; }
  KL2R<NT, POS, 16, 0>::run(x, acc, so);
}

DI void featmap(f4 v, v2u wphi, v2u &plus, v2u &minus) {
  const v2u b = {pk2(v.x, v.y), pk2(v.z, v.w)};
  const f4 p = mfma16(wphi, b, f4{0.f, 0.f, 0.f, 0.f});
  const float mxv = xmax(fmaxf(fmaxf(p.x, p.y), fmaxf(p.z, p.w)));
  const float mnv = -xmax(fmaxf(fmaxf(-p.x, -p.y), fmaxf(-p.z, -p.w)));
  const float L2E = 1.4426950408889634f;
  const float e0 = __builtin_amdgcn_exp2f((p.x - mxv) * L2E), e1 = __builtin_amdgcn_exp2f((p.y - mxv) * L2E);
  const float e2 = __builtin_amdgcn_exp2f((p.z - mxv) * L2E), e3 = __builtin_amdgcn_exp2f((p.w - mxv) * L2E);
  const float n0 = __builtin_amdgcn_exp2f((mnv - p.x) * L2E), n1 = __builtin_amdgcn_exp2f((mnv - p.y) * L2E);
  const float n2 = __builtin_amdgcn_exp2f((mnv - p.z) * L2E), n3 = __builtin_amdgcn_exp2f((mnv - p.w) * L2E);
  const float rp = __builtin_amdgcn_rcpf(xsum((e0 + e1) + (e2 + e3)));
  const float rn = __builtin_amdgcn_rcpf(xsum((n0 + n1) + (n2 + n3)));
  plus = v2u{pk2(e0 * rp, e1 * rp), pk2(e2 * rp, e3 * rp)};
  minus = v2u{pk2(n0 * rn, n1 * rn), pk2(n2 * rn, n3 * rn)};
}
DI v2u trread(const Ctx &x, unsigned off) {
  s4 r = __builtin_amdgcn_ds_read_tr16_b64_v4i16((LAS s4 *)(x.sm + off));
  return __builtin_bit_cast(v2u, r);
}
DI f4 shsum(f4 s) {
#pragma unroll
  for (int o = 4; o < 64; o <<= 1) {
    s.x += __shfl_xor(s.x, o); s.y += __shfl_xor(s.y, o); s.z += __shfl_xor(s.z, o); s.w += __shfl_xor(s.w, o);
  }
  return s;
}

DI void poll_flags(Ctx &x, unsigned off, unsigned tag) {
  for (unsigned sp = 0;; ++sp) {
    unsigned f0, f1, f2, f3;
    const unsigned vo = (unsigned)x.lane * 64u;
    LDC(f0, vo, off); LDC(f1, vo, off + 4096u); LDC(f2, vo, off + 8192u); LDC(f3, vo, off + 12288u);
    asm volatile("s_waitcnt vmcnt(0)" : "+v"(f0), "+v"(f1), "+v"(f2), "+v"(f3)::"memory");
    if (__all(f0 == tag && f1 == tag && f2 == tag && f3 == tag)) break;
    if (sp > SPIN_CAP) {
      if (x.lane == 0) __hip_atomic_store(x.ctl + OFF_ABORT / 4, 3u, RLX_AGENT);
      break;
    }
    __builtin_amdgcn_s_sleep(8);
  }
}
DI unsigned sigv(int L, int ph) { return (unsigned)(L * 8 + ph + 1); }

DI void compute_path(Ctx &x, const Params &p, v4u (&xres)[2], const v2u (&wph)[2], unsigned tag) {
  const unsigned vz[4] = {0u, 0u, 0u, 0u};
  v4u x1r[2], x2r[2];
#pragma unroll 1
  for (int L = 0; L < 2; ++L) {
    const unsigned lb0 = OFF_L0 + (unsigned)L * L_STRIDE;
    v4u phq[2][2];
    GBAR();
    {
      refresh(x);
      FS(0);
      unsigned lb = lb0;
      asm volatile("" : "+s"(lb));
      v4u bf[16][2];
      Stats st[2];
      const unsigned so = lb + O_XB + (unsigned)x.TA * 16u * KB, sst = lb + O_XS + (unsigned)x.TA * 1024u;
      pro2(x, bf, st, so, sst);
      FS(1); FSP(0);
      BAR();
      if (L == 0 && threadIdx.x == 0) __hip_atomic_store(x.ctl + OFF_FLAGS2 / 4 + x.b * 16, tag, RLX_AGENT);
      FS(2);
      f4 acc[6][2];
#pragma unroll
      for (int f = 0; f < 6; ++f) { acc[f][0] = f4{0.f, 0.f, 0.f, 0.f}; acc[f][1] = f4{0.f, 0.f, 0.f, 0.f}; }
      unsigned mx = 0;
      kloop2<6, POS1, 0, 16>(x, bf, acc, so, sigv(L, 0), mx);
      if (__builtin_expect(anybad(mx), 0)) redo2<6, POS1>(x, acc, so);
      FS(3);
      BAR();
      f4 val[2][6];
#pragma unroll
      for (int j = 0; j < 2; ++j) {
        float mean, var;
        stats_fin(x, st[j], sst + (unsigned)j * 1024u, mean, var);
        const float rstd = __builtin_amdgcn_rsqf(var + 1e-5f), mr = mean * rstd;
#pragma unroll
        for (int f = 0; f < 6; ++f) val[j][f] = acc[f][j] * rstd - ldcs(x, f) * mr;
      }
#pragma unroll
      for (int j = 0; j < 2; ++j) {
        const unsigned img = IMG + (unsigned)((2 * x.m + j) * 16 + x.tok) * 32u + (unsigned)x.g * 8u;
#pragma unroll
        for (int h = 0; h < 2; ++h) {
          v2u pl, mi;
          featmap(val[j][2 + h], wph[h], pl, mi);
          *(LAS v2u *)(x.sm + img + (unsigned)(h * 3 + 0) * 4096u) = pl;
          *(LAS v2u *)(x.sm + img + (unsigned)(h * 3 + 1) * 4096u) = mi;
          *(LAS v2u *)(x.sm + img + (unsigned)(h * 3 + 2) * 4096u) = v2u{pk2(val[j][4 + h].x, val[j][4 + h].y), pk2(val[j][4 + h].z, val[j][4 + h].w)};
        }
      }
      FS(4);
      BAR();
      FS(5);
#pragma unroll
      for (int j = 0; j < 2; ++j)
#pragma unroll
        for (int h = 0; h < 2; ++h) {
          v2u pl, mi;
          featmap(val[j][h], wph[h], pl, mi);
          phq[j][h] = v4u{pl.x, pl.y, mi.x, mi.y};
        }
      FS(6);
    }
    GBAR();
    GBAR();
    {
      refresh(x);
      unsigned lb = lb0;
      asm volatile("" : "+s"(lb));
      BAR();
      FS(7);
      v4u ka[2], za[2];
#pragma unroll
      for (int h = 0; h < 2; ++h) {
        ka[h] = *lds4(x, M_KVA + (unsigned)h * 1024u + x.lane16);
        const v2u z0 = *(const LAS v2u *)(x.sm + M_ZA + (unsigned)(h * 2) * 32u + (unsigned)x.g * 8u);
        const v2u z1 = *(const LAS v2u *)(x.sm + M_ZA + (unsigned)(h * 2 + 1) * 32u + (unsigned)x.g * 8u);
        za[h] = v4u{z0.x, z0.y, z1.x, z1.y};
      }
#pragma unroll
      for (int j = 0; j < 2; ++j) {
        f4 o[2];
#pragma unroll
        for (int h = 0; h < 2; ++h) {
          const f4 num = mfma32(ka[h], phq[j][h], f4{0.f, 0.f, 0.f, 0.f});
          const f4 den = mfma32(za[h], phq[j][h], f4{0.f, 0.f, 0.f, 0.f});
          o[h] = num * __builtin_amdgcn_rcpf(den.x + 1e-6f);
        }
        stg(x.rs, x.lane16, lb + O_OB + (unsigned)((x.TA + j) * 16 + x.pp) * KB, pack8(o[0], o[1]));
      }
      FS(8);
    }
    GBAR();
    {
      refresh(x);
      unsigned lb = lb0;
      asm volatile("" : "+s"(lb));
      SLEEP(DLY_2);
      FS(9);
      v4u bf[16];
      const unsigned so = lb + O_OB + (unsigned)x.T * 16u * KB;
      Stats st;
      pro1<16, false, false>(x, bf, so, vz, (unsigned)(x.lane & 15) * KB + 1020u, st, 0u);
      FS(10); FSP(1);
      BAR();
      FS(11);
      f4 acc[4];
#pragma unroll
      for (int f = 0; f < 4; ++f) acc[f] = f4{0.f, 0.f, 0.f, 0.f};
      kloop1<POS2, 16, false, 0, 16>(x, bf, acc, so, vz, sigv(L, 2));
      FS(12);
      x1r[0] = pack8(ulo(xres[0]) + acc[0], uhi(xres[0]) + acc[1]);
      x1r[1] = pack8(ulo(xres[1]) + acc[2], uhi(xres[1]) + acc[3]);
      const unsigned fo = lb + O_X1B + (unsigned)(x.T * 16 + 2 * x.c) * KB;
      stg(x.rs, x.lane16, fo, x1r[0]); stg(x.rs, x.lane16, fo + KB, x1r[1]);
      put_stats(x, x1r[0], x1r[1], lb + O_X1S);
      sig_set(x, sigv(L, 2), M_SIG2);
      FS(13);
    }
    float mean1, rstd1;
    GBAR();
    {
      refresh(x);
      unsigned lb = lb0;
      asm volatile("" : "+s"(lb));
      SLEEP(DLY_3);
      v4u bf[16];
      Stats st;
      const unsigned so = lb + O_X1B + (unsigned)x.T * 16u * KB;
      pro1<16, false, true>(x, bf, so, vz, (unsigned)(x.lane & 15) * KB + 1020u, st, lb + O_X1S + (unsigned)x.T * 1024u);
      FSP(2);
      BAR();
      FS(14);
      f4 acc[4];
#pragma unroll
      for (int f = 0; f < 4; ++f) acc[f] = f4{0.f, 0.f, 0.f, 0.f};
      kloop1<POS3, 16, false, 0, 16>(x, bf, acc, so, vz, sigv(L, 3));
      FS(15);
      float var;
      stats_fin(x, st, lb + O_X1S + (unsigned)x.T * 1024u, mean1, var);
      rstd1 = __builtin_amdgcn_rsqf(var + 1e-5f);
      const float a = rstd1 * __builtin_amdgcn_rsqf(__builtin_fmaf(var * rstd1, rstd1, 1e-5f)), ma = mean1 * a;
      f4 y[4];
#pragma unroll
      for (int f = 0; f < 4; ++f) y[f] = gelu4(acc[f] * a - ldcs(x, 6 + f) * ma);
      const unsigned fo = lb + O_HB + (unsigned)(x.T * 16 + 2 * x.c) * KB;
      stg(x.rs, x.lane16, fo, pack8(y[0], y[1])); stg(x.rs, x.lane16, fo + KB, pack8(y[2], y[3]));
      sig_set(x, sigv(L, 3), M_SIG2);
      FS(16);
    }
    GBAR();
    {
      refresh(x);
      unsigned lb = lb0;
      asm volatile("" : "+s"(lb));
      SLEEP(DLY_4);
      v4u bf[16];
      unsigned vo[4];
#pragma unroll
      for (int q = 0; q < 4; ++q) {
        const int d = q == 0 ? -1 : (q == 1 ? 1 : (q == 2 ? -2 : 2));
        const int src = 16 * x.T + x.tok + d;
        vo[q] = (src >= 0 && src < 2048) ? (unsigned)((src >> 4) * 16 * 1024 + (x.g * 16 + (src & 15)) * 16) : OOB;
      }
      const unsigned so = lb + O_HB;
      unsigned cvo;
      {
        const int s_ = x.lane & 15, nb = x.T + (((s_ >> 2) & 1) ? 1 : -1);
        const int tt = (x.lane & 16) ? nb : x.T;
        cvo = (tt >= 0 && tt < 128) ? (unsigned)(tt * 16 + s_) * KB + 1020u : OOB;
      }
      Stats st;
      pro1<16, true, false>(x, bf, so, vo, cvo, st, 0u);
      FSP(3);
      BAR();
      FS(17);
      f4 acc[4];
#pragma unroll
      for (int f = 0; f < 4; ++f) acc[f] = f4{0.f, 0.f, 0.f, 0.f};
      MARK("P4loop");
      kloop1<POS4, 16, true, 0, 16>(x, bf, acc, so, vo, sigv(L, 4));
      MARK("P4end");
      FS(18);
      const float mr = mean1 * rstd1;
      x2r[0] = pack8(ulo(x1r[0]) * rstd1 - mr + acc[0], uhi(x1r[0]) * rstd1 - mr + acc[1]);
      x2r[1] = pack8(ulo(x1r[1]) * rstd1 - mr + acc[2], uhi(x1r[1]) * rstd1 - mr + acc[3]);
      const unsigned fo = lb + O_X2B + (unsigned)(x.T * 16 + 2 * x.c) * KB;
      stg(x.rs, x.lane16, fo, x2r[0]); stg(x.rs, x.lane16, fo + KB, x2r[1]);
      put_stats(x, x2r[0], x2r[1], lb + O_X2S);
      sig_set(x, sigv(L, 4), M_SIG2);
      FS(19);
    }
    GBAR();
    {
      refresh(x);
      unsigned lb = lb0;
      asm volatile("" : "+s"(lb));
      SLEEP(DLY_5);
      v4u bf[16][2];
      Stats st[2];
      const unsigned so = lb + O_X2B + (unsigned)x.TA * 16u * KB, sst = lb + O_X2S + (unsigned)x.TA * 1024u;
      pro2(x, bf, st, so, sst);
      FSP(4);
      BAR();
      FS(20);
      f4 acc[7][2];
#pragma unroll
      for (int f = 0; f < 7; ++f) { acc[f][0] = f4{0.f, 0.f, 0.f, 0.f}; acc[f][1] = f4{0.f, 0.f, 0.f, 0.f}; }
      unsigned mx = 0;
      MARK("P5loop");
      kloop2<7, POS5, 0, 13>(x, bf, acc, so, sigv(L, 5), mx);
      BAR();
      kloop2<7, POS5, 13, 16>(x, bf, acc, so, sigv(L, 5), mx);
      if (__builtin_expect(anybad(mx), 0)) redo2<7, POS5>(x, acc, so);
      FS(21);
      BAR();
      FS(22);
      MARK("P5epi");
      const int u = x.pp >> 1, odd = x.pp & 1;
#pragma unroll
      for (int j = 0; j < 2; ++j) {
        float mean, var;
        stats_fin(x, st[j], sst + (unsigned)j * 1024u, mean, var);
        const float rstd = __builtin_amdgcn_rsqf(var + 1e-5f), mr = mean * rstd;
        const unsigned hb = lb + O_H3B + (unsigned)((x.TA + j) * 56 + 7 * u) * KB;
#pragma unroll
        for (int r = 0; r < 3; ++r) {
          const f4 y0 = gelu4(acc[2 * r][j] * rstd - ldcs(x, 10 + 2 * r) * mr), y1 = gelu4(acc[2 * r + 1][j] * rstd - ldcs(x, 11 + 2 * r) * mr);
          stg(x.rs, x.lane16, hb + (unsigned)(odd ? 4 + r : r) * KB, pack8(y0, y1));
        }
        const f4 y = gelu4(acc[6][j] * rstd - ldcs(x, 16) * mr);
        stg2(x.rs, x.lane16 + (unsigned)odd * 8u, hb + 3 * KB, v2u{clampw(pk2(y.x, y.y)), clampw(pk2(y.z, y.w))});
      }
      FS(23);
    }
    GBAR();
    {
      refresh(x);
      unsigned lb = lb0;
      asm volatile("" : "+s"(lb));
      SLEEP(DLY_6);
      v4u hf[56];
      const unsigned so = lb + O_H3B + (unsigned)x.T * 56u * KB;
      Stats st;
      pro1<56, false, false>(x, hf, so, vz, (unsigned)min(x.lane, 55) * KB + 1020u, st, 0u);
      FSP(5);
      BAR();
      FS(24);
      f4 acc[4];
#pragma unroll
      for (int f = 0; f < 4; ++f) acc[f] = f4{0.f, 0.f, 0.f, 0.f};
      kloop1<POS6, 56, false, 0, 17>(x, hf, acc, so, vz, 0u);
      BAR();
      FS(25);
      kloop1<POS6, 56, false, 17, 39>(x, hf, acc, so, vz, 0u);
      BAR();
      FS(26);
      kloop1<POS6, 56, false, 39, 56>(x, hf, acc, so, vz, 0u);
      FS(27);
      BAR();
      xres[0] = pack8(ulo(x1r[0]) + (ulo(x2r[0]) + acc[0]), uhi(x1r[0]) + (uhi(x2r[0]) + acc[1]));
      xres[1] = pack8(ulo(x1r[1]) + (ulo(x2r[1]) + acc[2]), uhi(x1r[1]) + (uhi(x2r[1]) + acc[3]));
      if (L == 0) {
        const unsigned fo = lb + L_STRIDE + O_XB + (unsigned)(x.T * 16 + 2 * x.c) * KB;
        stg(x.rs, x.lane16, fo, xres[0]); stg(x.rs, x.lane16, fo + KB, xres[1]);
        put_stats(x, xres[0], xres[1], lb + L_STRIDE + O_XS);
        SLEEP(DLY_1);
      } else {
        GAS unsigned char *op = (GAS unsigned char *)p.out + ((size_t)(16 * x.T + x.tok) * 512 + 64 * x.c + 4 * x.g) * 2;
        *(GAS v2u *)op = v2u{xres[0].x, xres[0].y};
        *(GAS v2u *)(op + 32) = v2u{xres[0].z, xres[0].w};
        *(GAS v2u *)(op + 64) = v2u{xres[1].x, xres[1].y};
        *(GAS v2u *)(op + 96) = v2u{xres[1].z, xres[1].w};
      }
      FS(28); FSP(6);
    }
  }
}

DI void helper_path(Ctx &x, unsigned tag) {
#pragma unroll 1
  for (int L = 0; L < 2; ++L) {
    const unsigned lb0 = OFF_L0 + (unsigned)L * L_STRIDE;
    GBAR();
    {
      refresh(x);
      unsigned lb = lb0;
      asm volatile("" : "+s"(lb));
      FS(0);
      DRAIN_BAR();
      FS(1);
      {
        const unsigned fo = (unsigned)(x.T * 16 + 2 * x.c) * KB, sto = (unsigned)(x.T * 8 + x.c) * 128u;
        poi4(x.rs, x.lane16, lb + O_X1B + fo); poi4(x.rs, x.lane16, lb + O_X1B + fo + KB);
        poi4(x.rs, x.lane16, lb + O_X2B + fo); poi4(x.rs, x.lane16, lb + O_X2B + fo + KB);
        if (x.g == 0) { poi2(x.rs, (unsigned)x.tok * 8u, lb + O_X1S + sto); poi2(x.rs, (unsigned)x.tok * 8u, lb + O_X2S + sto); }
        const int u = x.pp >> 1, odd = x.pp & 1;
#pragma unroll
        for (int j = 0; j < 2; ++j) {
          const unsigned hb = lb + O_H3B + (unsigned)((x.TA + j) * 56 + 7 * u) * KB;
#pragma unroll
          for (int r = 0; r < 3; ++r) poi4(x.rs, x.lane16, hb + (unsigned)(odd ? 4 + r : r) * KB);
          poi2(x.rs, x.lane16 + (unsigned)odd * 8u, hb + 3 * KB);
        }
      }
      if (L != 0) {
        sig_wait(x, sigv(L, 0));
        dmaR<96, 156>(x);
      }
      BAR();
      FS(2);
      BAR();
      FS(3);
      {
        const int h = x.m >> 1, sg = x.m & 1;
        const unsigned lo = (unsigned)(4 * x.g + (x.tok >> 2)) * 32u + (unsigned)(x.tok & 3) * 8u;
        const unsigned ia = IMG + (unsigned)(h * 3 + sg) * 4096u + lo, iv = IMG + (unsigned)(h * 3 + 2) * 4096u + lo;
        f4 kv = {0.f, 0.f, 0.f, 0.f}, z = {0.f, 0.f, 0.f, 0.f};
#pragma unroll
        for (int tb = 0; tb < 4; ++tb) {
          const v2u a0 = trread(x, ia + tb * 1024u), a1 = trread(x, ia + tb * 1024u + 512u);
          const v2u b0 = trread(x, iv + tb * 1024u), b1 = trread(x, iv + tb * 1024u + 512u);
          const v4u a = {a0.x, a0.y, a1.x, a1.y};
          kv = mfma32(a, v4u{b0.x, b0.y, b1.x, b1.y}, kv);
          z = mfma32(a, v4u{0x3F803F80u, 0x3F803F80u, 0x3F803F80u, 0x3F803F80u}, z);
        }
        stg(x.rs, x.lane16, lb + O_KVP + (unsigned)((x.pp * 16 + x.tau) * 4 + x.m) * KB, clamp4(__builtin_bit_cast(v4u, kv)));
        if (x.tok == 0) stg(x.rs, (unsigned)x.g * 16u, lb + O_KVZ + (unsigned)((x.pp * 4 + x.m) * 16 + x.tau) * 64u, clamp4(__builtin_bit_cast(v4u, z)));
      }
      FS(4);
    }
    GBAR();
    {
      refresh(x);
      unsigned lb = lb0;
      asm volatile("" : "+s"(lb));
      if (L == 0) {
        if (x.m == 0) {
          poll_flags(x, OFF_FLAGS2, tag);
          *(volatile LAS unsigned *)(x.sm + M_RDY) = 1u;
        } else {
          for (unsigned sp = 0; sp < (1u << 20); ++sp) {
            if (__builtin_amdgcn_readfirstlane(*(volatile LAS unsigned *)(x.sm + M_RDY))) break;
            __builtin_amdgcn_s_sleep(2);
          }
        }
        v4u cv[3];
#pragma unroll
        for (int jj = 0; jj < 3; ++jj) {
          const int u = min(6 + x.m + 4 * jj, 16);
          cv[jj] = ldg(x.rs, x.lane16, OFF_CSP + (u < 10 ? 96u + (unsigned)(x.c * 4 + u - 6) : 128u + (unsigned)(x.pp * 7 + u - 10)) * KB);
        }
#pragma unroll
        for (int jj = 0; jj < 3; ++jj) {
          const f4 s = shsum(__builtin_bit_cast(f4, cv[jj]));
          const int u = 6 + x.m + 4 * jj;
          if (u < 17 && x.lane < 4) *(LAS f4 *)(x.sm + M_CS + (unsigned)u * 64u + (unsigned)x.lane * 16u) = s;
        }
      }
      SLEEP(DLY_KV);
      v4u pv[16];
      const unsigned so = lb + O_KVP + (unsigned)(x.pp * 64 + x.m) * KB, sz = lb + O_KVZ + (unsigned)(x.pp * 4 + x.m) * KB;
      v4u zv;
      {
        const unsigned cvo = (unsigned)(x.lane & 15) * 4096u + 1020u;
        while (true) {
          unsigned cn;
          LDC(cn, cvo, so);
          LDA(zv, x.lane16, sz);
          WAITV2(0, cn, zv);
          if (!anybad(max(cn, max4(zv))) || giveup(x)) break;
        }
      }
      bool again;
      do {
        asm volatile("" ::: "memory");
#pragma unroll
        for (int i = 0; i < 16; ++i) pv[i] = ldg(x.rs, x.lane16, so + (unsigned)i * 4096u);
        unsigned mx = 0;
#pragma unroll
        for (int i = 0; i < 16; ++i) mx = max(mx, max4(pv[i]));
        again = anybad(mx) && !giveup(x);
      } while (__builtin_expect(again, 0));
      f4 s = __builtin_bit_cast(f4, pv[0]);
#pragma unroll
      for (int i = 1; i < 16; ++i) s += __builtin_bit_cast(f4, pv[i]);
      *(LAS v2u *)(x.sm + M_KVA + (unsigned)(x.m >> 1) * 1024u + x.lane16 + (unsigned)(x.m & 1) * 8u) = v2u{pk2(s.x, s.y), pk2(s.z, s.w)};
      const f4 zs = shsum(__builtin_bit_cast(f4, zv));
      if (x.lane < 4) *(LAS v2u *)(x.sm + M_ZA + (unsigned)x.m * 32u + (unsigned)x.lane * 8u) = v2u{pk2(zs.x, zs.y), pk2(zs.z, zs.w)};
      FS(5); FSP(0);
      GBAR();
      BAR();
      FS(6);
      if (L == 0) dmaR<96, 156>(x);
      dmaR<156, 188>(x);
    }
    GBAR();
    {
      refresh(x);
      unsigned lb = lb0;
      asm volatile("" : "+s"(lb));
      DRAIN_BAR();
      FS(7);
      sig_wait(x, sigv(L, 2));
      dmaR<188, 220>(x);
      sig_wait(x, sigv(L, 2), M_SIG2);
      dmaR<220, 252>(x);
    }
    GBAR();
    {
      refresh(x);
      unsigned lb = lb0;
      asm volatile("" : "+s"(lb));
      DRAIN_BAR();
      FS(8);
      sig_wait(x, sigv(L, 3));
      FS(15);
      dmaR<252, 284>(x);
      sig_wait(x, sigv(L, 3), M_SIG2);
      dmaR<284, 316>(x);
      FS(16);
      if (L == 0) {
        const unsigned fo = lb + L_STRIDE + O_XB + (unsigned)(x.T * 16 + 2 * x.c) * KB;
        poi4(x.rs, x.lane16, fo); poi4(x.rs, x.lane16, fo + KB);
        if (x.g == 0) poi2(x.rs, (unsigned)x.tok * 8u, lb + L_STRIDE + O_XS + (unsigned)(x.T * 8 + x.c) * 128u);
      }
    }
    GBAR();
    {
      refresh(x);
      unsigned lb = lb0;
      asm volatile("" : "+s"(lb));
      DRAIN_BAR();
      FS(9);
      sig_wait(x, sigv(L, 4));
      FS(18);
      dmaR<316, 348>(x);
      sig_wait(x, sigv(L, 4), M_SIG2);
      dmaR<348, 380>(x);
      FS(19);
      if (L == 0) {
        const unsigned fo = lb + L_STRIDE + O_HB + (unsigned)(x.T * 16 + 2 * x.c) * KB;
        poi4(x.rs, x.lane16, fo); poi4(x.rs, x.lane16, fo + KB);
        poi4(x.rs, x.lane16, lb + L_STRIDE + O_OB + (unsigned)(x.TA * 16 + x.pp) * KB);
        poi4(x.rs, x.lane16, lb + L_STRIDE + O_OB + (unsigned)((x.TA + 1) * 16 + x.pp) * KB);
      }
    }
    GBAR();
    {
      refresh(x);
      DRAIN_BAR();
      FS(10);
      dmaRT<380, 400>(x);
      DRAIN_BAR();
      sig_wait(x, sigv(L, 5));
      dmaR<400, 444>(x);
      BAR();
      FS(11);
      dmaR6<444, 556>(x);
    }
    GBAR();
    {
      refresh(x);
      DRAIN_BAR();
      FS(12);
      BAR();
      dmaRT<556, 624>(x);
      DRAIN_BAR();
      FS(13);
      dmaR<624, 712>(x, L == 0);
      BAR();
      dmaR<712, 720>(x, L == 0);
      FS(14);
    }
  }
}

}

__global__ void __launch_bounds__(512, 2) mega_kernel(Params p) {
  extern __shared__ __attribute__((aligned(16))) unsigned char smem_[];
  Ctx x;
  x.sm = (LAS unsigned char *)smem_;
  x.lds0 = (unsigned)(size_t)smem_;
  x.lane = threadIdx.x & 63;
  x.w = __builtin_amdgcn_readfirstlane(threadIdx.x >> 6);
  x.b = blockIdx.x;
  refresh(x);
  const int b = x.b;
  x.spins = 0;
  x.ctl = (gu32 *)p.ws;
  x.rs = __builtin_amdgcn_make_buffer_rsrc((void *)p.ws, 0, WS_BYTES, 0x00020000);
  const bool cw = x.w < 4;

  FSA(0);
  const unsigned tag = __hip_atomic_load(x.ctl + OFF_EPOCH / 4, RLX_AGENT) + 1u;
  v2u xin[4] = {};
  unsigned short wraw[2][4] = {};
  if (cw) {
    const GAS unsigned char *xp = (const GAS unsigned char *)p.x + ((size_t)(16 * x.T + x.tok) * 512 + 64 * x.c + 4 * x.g) * 2;
#pragma unroll
    for (int q = 0; q < 4; ++q) xin[q] = *(const GAS v2u *)(xp + 32 * q);
#pragma unroll
    for (int h = 0; h < 2; ++h) {
      const unsigned short *wp = p.wphi + ((2 * x.pp + h) * 16 + 4 * x.g) * 16 + x.tok;
#pragma unroll
      for (int jj = 0; jj < 4; ++jj) wraw[h][jj] = wp[16 * jj];
    }
  }
  if (threadIdx.x < 9) *(LAS unsigned *)(x.sm + M_SIG + threadIdx.x * 4u) = 0u;
  const int bid = ((x.w + 4) & 7) * 256 + b;
  const unsigned short *src = p.wqkv; int ld = 1536, ra = 0, rb = 0, n0 = 0, kstep = 0, csid = -1; unsigned fr[4] = {0u, 0u, 0u, 0u}, ci[4] = {0u, 0u, 0u, 0u};
  if (bid < 384) {
    kstep = bid & 15; const int nb = bid >> 4; n0 = nb * 64; ra = 32 * kstep; rb = ra + 16;
    const int sec = nb >> 3, h0 = 4 * (nb & 7);
#pragma unroll
    for (int ct = 0; ct < 4; ++ct) {
      const int hh = h0 + ct;
      fr[ct] = W1_F + (unsigned)(((hh >> 1) * 16 + kstep) * 6 + sec * 2 + (hh & 1));
      ci[ct] = (unsigned)((hh >> 1) * 6 + sec * 2 + (hh & 1));
    }
    csid = 0;
  } else if (bid < 768) {
    const int mi = (bid - 384) >> 7, r = (bid - 384) & 127; kstep = r & 15; const int nb = r >> 4;
    src = mi == 0 ? p.wo : (mi == 1 ? p.w1 : p.w2); ld = 512; n0 = nb * 64; ra = 32 * kstep; rb = ra + 16;
#pragma unroll
    for (int ct = 0; ct < 4; ++ct) { fr[ct] = W2_F + (unsigned)(mi * 512 + (nb * 16 + kstep) * 4 + ct); ci[ct] = 96u + (unsigned)(nb * 4 + ct); }
    if (mi == 1) csid = 1;
  } else if (bid < 1216) {
    const int r = bid - 768; kstep = r & 15; const int nb = r >> 4; src = p.w3; ld = 1792; n0 = nb * 64; ra = 32 * kstep; rb = ra + 16;
#pragma unroll
    for (int ct = 0; ct < 4; ++ct) { const int F = nb * 4 + ct; fr[ct] = W5_F + (unsigned)(((F / 7) * 16 + kstep) * 7 + F % 7); ci[ct] = 128u + (unsigned)F; }
    csid = 2;
  } else if (bid < 1664) {
    const int r = bid - 1216; const int nb = r & 7; kstep = r >> 3; src = p.w4; ld = 512; n0 = nb * 64;
    const int u = kstep / 7, q = kstep % 7;
    if (q < 3) { ra = 224 * u + 32 * q; rb = ra + 16; }
    else if (q == 3) { ra = 224 * u + 96; rb = 224 * u + 208; }
    else { ra = 224 * u + 112 + 32 * (q - 4); rb = ra + 16; }
#pragma unroll
    for (int ct = 0; ct < 4; ++ct) fr[ct] = W6_F + (unsigned)((nb * 56 + kstep) * 4 + ct);
  }
  if (bid < 1664) {
    const int ip = x.lane >> 5, uu = (x.lane >> 4) & 1, q = (x.lane >> 2) & 3, c1 = ((x.lane >> 1) & 1) ^ ip, bb = x.lane & 1;
    const int rl = 4 * ip + q, ch = 2 * (2 * uu + c1) + bb;
    const unsigned stg0 = x.lds0 + (unsigned)x.w * 4096u;
#pragma unroll
    for (int u = 0; u < 4; ++u) {
      const int row = (u < 2 ? ra + 8 * u : rb + 8 * (u - 2)) + rl;
      const GAS unsigned char *gp = (const GAS unsigned char *)src + ((size_t)row * ld + n0 + ch * 8) * 2;
      asm volatile("s_mov_b32 m0, %0\n\ts_nop 0\n\tglobal_load_lds_dwordx4 %1, off" ::"s"(stg0 + (unsigned)u * 1024u), "v"(gp) : "memory");
    }
  }
  auto retile = [&]() {
    asm volatile("s_waitcnt vmcnt(0)" ::: "memory");
    const int gq = (x.lane & 15) >> 2, gp_ = x.lane & 3, gi = x.g & 1;
#pragma unroll
    for (int ct = 0; ct < 4; ++ct) {
      const unsigned L = (unsigned)((gi << 5) | ((ct >> 1) << 4) | (gq << 2) | (((ct & 1) ^ gi) << 1) | (gp_ >> 1));
      const unsigned a0 = (unsigned)x.w * 4096u + (unsigned)(x.g >> 1) * 1024u + L * 16u + (unsigned)(gp_ & 1) * 8u;
      const v2u lo = trread(x, a0), hi = trread(x, a0 + 2048u);
      const v4u fragv = {lo.x, lo.y, hi.x, hi.y};
      stg(x.rs, x.lane16, __builtin_amdgcn_readfirstlane(OFF_WT + fr[ct] * KB), fragv);
      if (csid >= 0) {
        const f4 cs = mfma32(fragv, v4u{0x3F803F80u, 0x3F803F80u, 0x3F803F80u, 0x3F803F80u}, f4{0.f, 0.f, 0.f, 0.f});
        const unsigned co = __builtin_amdgcn_readfirstlane(OFF_CSP + (ci[ct] * 16u + (unsigned)kstep) * 64u);
        if (x.tok == 0) stg(x.rs, (unsigned)x.g * 16u, co, __builtin_bit_cast(v4u, cs));
      }
    }
  };
  if (bid < 384) retile();
  FSA(1);
  v4u xres[2];
  v2u wph[2];
  if (cw) {
    xres[0] = clamp4(v4u{xin[0].x, xin[0].y, xin[1].x, xin[1].y});
    xres[1] = clamp4(v4u{xin[2].x, xin[2].y, xin[3].x, xin[3].y});
    const unsigned fo = OFF_L0 + O_XB + (unsigned)(x.T * 16 + 2 * x.c) * KB;
    stg(x.rs, x.lane16, fo, xres[0]); stg(x.rs, x.lane16, fo + KB, xres[1]);
    put_stats(x, xres[0], xres[1], OFF_L0 + O_XS);
#pragma unroll
    for (int h = 0; h < 2; ++h)
      wph[h] = v2u{(unsigned)wraw[h][0] | ((unsigned)wraw[h][1] << 16), (unsigned)wraw[h][2] | ((unsigned)wraw[h][3] << 16)};
  } else {
    xres[0] = xres[1] = v4u{0u, 0u, 0u, 0u};
    wph[0] = wph[1] = v2u{0u, 0u};
  }
  FSA(2);
  asm volatile("s_waitcnt vmcnt(0) lgkmcnt(0)\n\ts_barrier" ::: "memory");
  FSA(3);
  if (threadIdx.x == 0) __hip_atomic_store(x.ctl + OFF_FLAGS / 4 + b * 16, tag, RLX_AGENT);
  if (bid >= 384 && bid < 1664) retile();
  if (!cw) {
    poi4(x.rs, x.lane16, OFF_L0 + O_KVP + (unsigned)((x.pp * 16 + x.tau) * 4 + x.m) * KB);
    if (x.tok == 0) poi4(x.rs, (unsigned)x.g * 16u, OFF_L0 + O_KVZ + (unsigned)((x.pp * 4 + x.m) * 16 + x.tau) * 64u);
    poi4(x.rs, x.lane16, OFF_L0 + O_OB + (unsigned)(x.TA * 16 + x.pp) * KB);
    poi4(x.rs, x.lane16, OFF_L0 + O_OB + (unsigned)((x.TA + 1) * 16 + x.pp) * KB);
    poi4(x.rs, x.lane16, OFF_L0 + O_HB + (unsigned)(x.T * 16 + 2 * x.c) * KB);
    poi4(x.rs, x.lane16, OFF_L0 + O_HB + (unsigned)(x.T * 16 + 2 * x.c + 1) * KB);
    poi4(x.rs, x.lane16, OFF_L0 + L_STRIDE + O_KVP + (unsigned)((x.pp * 16 + x.tau) * 4 + x.m) * KB);
    if (x.tok == 0) poi4(x.rs, (unsigned)x.g * 16u, OFF_L0 + L_STRIDE + O_KVZ + (unsigned)((x.pp * 4 + x.m) * 16 + x.tau) * 64u);
  }
  if (x.w == 4) {
    poll_flags(x, OFF_FLAGS, tag);
    if (b == 0 && x.lane == 0) __hip_atomic_store(x.ctl + OFF_EPOCH / 4, tag, RLX_AGENT);
  }
  BAR();
  FSA(4);
  if (cw) {
    compute_path(x, p, xres, wph, tag);
  } else {
    v4u cv[2];
#pragma unroll
    for (int jj = 0; jj < 2; ++jj) LDA(cv[jj], x.lane16, OFF_CSP + (unsigned)(x.pp * 6 + min(x.m + 4 * jj, 5)) * KB);
    dmaR<POS1, POS2>(x);
#pragma unroll
    for (int jj = 0; jj < 2; ++jj) {
      WAITV1(24, cv[jj]);
      const f4 s = shsum(__builtin_bit_cast(f4, cv[jj]));
      const int u = x.m + 4 * jj;
      if (u < 6 && x.lane < 4) *(LAS f4 *)(x.sm + M_CS + (unsigned)u * 64u + (unsigned)x.lane * 16u) = s;
    }
    helper_path(x, tag);
  }
  asm volatile("s_waitcnt vmcnt(0)" ::: "memory");
}

extern "C" void kernel_launch(void *const *d_in, const int *in_sizes, int n_in, void *d_out, int out_size, void *d_ws, size_t ws_size,
                              hipStream_t stream) {
  Params p;
  p.x = (const unsigned short *)d_in[0];
  p.wqkv = (const unsigned short *)d_in[1];
  p.wphi = (const unsigned short *)d_in[2];
  p.wo = (const unsigned short *)d_in[3];
  p.w1 = (const unsigned short *)d_in[5];
  p.w2 = (const unsigned short *)d_in[6];
  p.w3 = (const unsigned short *)d_in[7];
  p.w4 = (const unsigned short *)d_in[8];
  p.out = (unsigned short *)d_out;
  p.ws = (unsigned char *)d_ws;
  (void)hipFuncSetAttribute((const void *)mega_kernel, hipFuncAttributeMaxDynamicSharedMemorySize, LDS_BYTES);
  hipLaunchKernelGGL(mega_kernel, dim3(256), dim3(512), LDS_BYTES, stream, p);
}
```

```cpp
#include <hip/hip_runtime.h>
#include <stdint.h>

#define GAS __attribute__((address_space(1)))
#define LAS __attribute__((address_space(3)))
#define DI __device__ __forceinline__
typedef unsigned v4u __attribute__((ext_vector_type(4)));
typedef unsigned v2u __attribute__((ext_vector_type(2)));
typedef float f4 __attribute__((ext_vector_type(4)));
typedef float f2 __attribute__((ext_vector_type(2)));
typedef short s8 __attribute__((ext_vector_type(8)));
typedef short s4 __attribute__((ext_vector_type(4)));
typedef __bf16 bf2 __attribute__((ext_vector_type(2)));
typedef GAS unsigned gu32;
typedef __amdgpu_buffer_rsrc_t rsrc_t;
#define RLX_AGENT __ATOMIC_RELAXED, __HIP_MEMORY_SCOPE_AGENT

#define DBG_GBAR 0
#define DBG_NOPOISON 0
#define DBG_STAMP 0
#define SPEC 0
#define WARM 0

namespace {
constexpr unsigned MB = 1u << 20, KB = 1024u;
constexpr unsigned OFF_EPOCH = 0, OFF_ABORT = 256, OFF_GBAR = 512, OFF_FLAGS = 16384, OFF_FLAGS2 = 32768;
constexpr unsigned OFF_WT = 1 * MB;
constexpr unsigned W1_F = 0, W2_F = 1536, W5_F = 3072, W6_F = 4864;
constexpr unsigned OFF_CSP = 8 * MB;
constexpr unsigned OFF_L0 = 9 * MB, L_STRIDE = 24 * MB;
constexpr unsigned O_XB = 0, O_XS = 2 * MB, O_KVP = 2 * MB + 512 * KB, O_KVZ = 3 * MB + 512 * KB, O_OB = 4 * MB, O_X1B = 6 * MB,
                   O_X1S = 8 * MB, O_HB = 9 * MB, O_X2B = 11 * MB, O_X2S = 13 * MB, O_H3B = 14 * MB;
constexpr unsigned WS_BYTES = OFF_L0 + 2 * L_STRIDE;
constexpr unsigned OOB = 0x7F000000u;
constexpr int RING = 156;
constexpr unsigned MISC = RING * KB;
constexpr unsigned M_CS = MISC, M_KVA = MISC + 1088, M_ZA = MISC + 3136, M_SIG = MISC + 3264, M_SIG2 = MISC + 3280, M_RDY = MISC + 3296, M_FLAG = MISC + 3300;
constexpr unsigned IMG = 0;
constexpr unsigned LDS_BYTES = 163840;
constexpr int POS1 = 0, POS2 = 96, POS3 = 160, POS4 = 224, POS5 = 288, POS6 = 400, POSE = 624;
constexpr unsigned SENT = 0xFFFFFFFFu;
constexpr unsigned SPIN_CAP = 1u << 13;
constexpr int AUX_SC1 = 16;
#define DLY_KV 16
#define DLY_1 16
#define DLY_2 16
#define DLY_3 16
#define DLY_4 16
#define DLY_5 16
#define DLY_6 16

struct Params {
  const unsigned short *x, *wqkv, *wphi, *wo, *w1, *w2, *w3, *w4;
  unsigned short *out;
  unsigned char *ws;
};

DI unsigned pk2(float a, float b) { f2 v = {a, b}; bf2 r = __builtin_convertvector(v, bf2); return __builtin_bit_cast(unsigned, r); }
DI float asf(unsigned u) { return __builtin_bit_cast(float, u); }
DI float blo(unsigned u) { return __builtin_bit_cast(float, u << 16); }
DI float bhi(unsigned u) { return __builtin_bit_cast(float, u & 0xFFFF0000u); }
DI unsigned clampw(unsigned u) { return u < 0xFFFFFFFEu ? u : 0xFFFFFFFEu; }
DI unsigned clampf(float f) { return clampw(__builtin_bit_cast(unsigned, f)); }
DI v4u clamp4(v4u v) { return v4u{clampw(v.x), clampw(v.y), clampw(v.z), clampw(v.w)}; }
DI v4u pack8(f4 a, f4 b) { return clamp4(v4u{pk2(a.x, a.y), pk2(a.z, a.w), pk2(b.x, b.y), pk2(b.z, b.w)}); }
DI f4 ulo(v4u v) { return f4{blo(v.x), bhi(v.x), blo(v.y), bhi(v.y)}; }
DI f4 uhi(v4u v) { return f4{blo(v.z), bhi(v.z), blo(v.w), bhi(v.w)}; }
DI f4 mfma32(v4u a, v4u b, f4 c) {
  return __builtin_amdgcn_mfma_f32_16x16x32_bf16(__builtin_bit_cast(s8, a), __builtin_bit_cast(s8, b), c, 0, 0, 0);
}
DI f4 mfma16(v2u a, v2u b, f4 c) {
  return __builtin_amdgcn_mfma_f32_16x16x16bf16_1k(__builtin_bit_cast(s4, a), __builtin_bit_cast(s4, b), c, 0, 0, 0);
}
DI v4u ldg(rsrc_t rs, unsigned voff, unsigned soff) { return __builtin_amdgcn_raw_buffer_load_b128(rs, voff, soff, AUX_SC1); }
DI void stg(rsrc_t rs, unsigned voff, unsigned soff, v4u v) {
  __builtin_amdgcn_raw_buffer_store_b128(v, rs, voff, soff, AUX_SC1);
  asm volatile("s_nop 1" ::"v"(v));
}
DI void stg2(rsrc_t rs, unsigned voff, unsigned soff, v2u v) { __builtin_amdgcn_raw_buffer_store_b64(v, rs, voff, soff, AUX_SC1); }
DI void poi4(rsrc_t rs, unsigned voff, unsigned soff) { if (!DBG_NOPOISON) stg(rs, voff, soff, v4u{0xFFFFFFFFu, 0xFFFFFFFFu, 0xFFFFFFFFu, 0xFFFFFFFFu}); }
DI void poi2(rsrc_t rs, unsigned voff, unsigned soff) { if (!DBG_NOPOISON) stg2(rs, voff, soff, v2u{0xFFFFFFFFu, 0xFFFFFFFFu}); }
DI unsigned max4(v4u v) { return max(max(v.x, v.y), max(v.z, v.w)); }
DI bool anybad(unsigned mx) { return __any(mx == SENT) != 0; }
DI float xsum(float x) {
  unsigned u = __builtin_bit_cast(unsigned, x);
  auto r = __builtin_amdgcn_permlane16_swap(u, u, false, false);
  x = __builtin_bit_cast(float, (unsigned)r[0]) + __builtin_bit_cast(float, (unsigned)r[1]);
  u = __builtin_bit_cast(unsigned, x);
  auto q = __builtin_amdgcn_permlane32_swap(u, u, false, false);
  return __builtin_bit_cast(float, (unsigned)q[0]) + __builtin_bit_cast(float, (unsigned)q[1]);
}
DI float xmax(float x) {
  unsigned u = __builtin_bit_cast(unsigned, x);
  auto r = __builtin_amdgcn_permlane16_swap(u, u, false, false);
  x = fmaxf(__builtin_bit_cast(float, (unsigned)r[0]), __builtin_bit_cast(float, (unsigned)r[1]));
  u = __builtin_bit_cast(unsigned, x);
  auto q = __builtin_amdgcn_permlane32_swap(u, u, false, false);
  return fmaxf(__builtin_bit_cast(float, (unsigned)q[0]), __builtin_bit_cast(float, (unsigned)q[1]));
}
template <bool MAX> DI float red_op(float a, float b) { return MAX ? fmaxf(a, b) : a + b; }
DI unsigned fbits(float f) { return __builtin_bit_cast(unsigned, f); }
template <bool MAX> DI void xred4(float &a, float &b, float &c, float &d) {
  const auto r1 = __builtin_amdgcn_permlane16_swap(fbits(a), fbits(b), false, false);
  const auto r2 = __builtin_amdgcn_permlane16_swap(fbits(c), fbits(d), false, false);
  const float c1 = red_op<MAX>(asf(r1[0]), asf(r1[1])), c2 = red_op<MAX>(asf(r2[0]), asf(r2[1]));
  const auto r3 = __builtin_amdgcn_permlane32_swap(fbits(c1), fbits(c2), false, false);
  const unsigned e = fbits(red_op<MAX>(asf(r3[0]), asf(r3[1])));
  const auto r4 = __builtin_amdgcn_permlane32_swap(e, e, false, false);
  const unsigned f0 = r4[0], f1 = r4[1];
  const auto r5 = __builtin_amdgcn_permlane16_swap(f0, f0, false, false);
  const auto r6 = __builtin_amdgcn_permlane16_swap(f1, f1, false, false);
  a = asf(r5[0]); b = asf(r5[1]); c = asf(r6[0]); d = asf(r6[1]);
}
template <bool MAX> DI void xred2(float &a, float &b) {
  const auto r1 = __builtin_amdgcn_permlane16_swap(fbits(a), fbits(b), false, false);
  const unsigned c = fbits(red_op<MAX>(asf(r1[0]), asf(r1[1])));
  const auto r3 = __builtin_amdgcn_permlane32_swap(c, c, false, false);
  const unsigned e = fbits(red_op<MAX>(asf(r3[0]), asf(r3[1])));
  const auto r5 = __builtin_amdgcn_permlane16_swap(e, e, false, false);
  a = asf(r5[0]); b = asf(r5[1]);
}
DI f2 gelu2(f2 x) {
  const f2 c1 = {-2.302208198f, -2.302208198f}, c2 = {-0.1029432397f, -0.1029432397f}, one = {1.0f, 1.0f};
  const f2 t = x * __builtin_elementwise_fma(x * x, c2, c1);
  const f2 d = one + f2{__builtin_amdgcn_exp2f(t.x), __builtin_amdgcn_exp2f(t.y)};
  return x * f2{__builtin_amdgcn_rcpf(d.x), __builtin_amdgcn_rcpf(d.y)};
}
DI f4 gelu4(f4 y) {
  const f2 a = gelu2(f2{y.x, y.y}), b = gelu2(f2{y.z, y.w});
  return f4{a.x, a.y, b.x, b.y};
}

#define FSL 1
#define FS(i) do {} while (0)
#define FSA(i) do { unsigned long long t_; asm volatile("s_memtime %0\n\ts_waitcnt lgkmcnt(0)" : "=s"(t_) :: "memory"); \
  int ww_ = x.w; asm volatile("" : "+s"(ww_)); \
  unsigned a_ = (ww_ == 0) ? (70 + (i)) * 4u : ((ww_ == 4) ? 400u + (70 + (i)) * 4u : 780u); \
  asm volatile("" : "+s"(a_)); \
  *(LAS unsigned *)(x.sm + M_FLAG + a_) = (unsigned)t_; } while (0)
#define FSP(i) do {} while (0)
#define SB() __builtin_amdgcn_sched_barrier(0)
#define MARK(s) asm volatile("; MARK " s)
#define BAR() asm volatile("s_waitcnt lgkmcnt(0)\n\ts_barrier" ::: "memory")
#define WAITV_BAR(n) asm volatile("s_waitcnt vmcnt(" #n ") lgkmcnt(0)\n\ts_barrier" ::: "memory")
#define DRAIN_BAR() asm volatile("s_waitcnt vmcnt(0) lgkmcnt(0)\n\ts_barrier" ::: "memory")
DI void sleepn(int n) {
  for (int i = 0; i < n; ++i) __builtin_amdgcn_s_sleep(1);
}

struct Ctx {
  rsrc_t rs;
  LAS unsigned char *sm;
  unsigned lds0;
  unsigned lane16;
  LAS unsigned char *ab[3];
  int lane, w, m, g, tok;
  int t, c, tau, pp, b;
  int T, TA;
  unsigned spins;
  gu32 *ctl;
};
DI void refresh(Ctx &x) {
  asm volatile("" : "+s"(x.w), "+s"(x.b), "+v"(x.lane));
  x.lane16 = (unsigned)x.lane * 16u;
  x.g = x.lane >> 4; x.tok = x.lane & 15;
  x.m = x.w & 3;
  const int xc = x.b & 7, j = x.b >> 3;
  x.t = 4 * xc + (j >> 3); x.c = j & 7;
  x.tau = 2 * xc + (j >> 4); x.pp = j & 15;
  x.T = 4 * x.t + x.m;
  x.TA = 8 * x.tau + 2 * x.m;
  x.ab[0] = x.sm + x.lane16;
  unsigned o1 = x.lane16 + 60u * KB, o2 = x.lane16 + 120u * KB;
  asm volatile("" : "+v"(o1), "+v"(o2));
  x.ab[1] = x.sm + o1; x.ab[2] = x.sm + o2;
}

template <int P> DI void dmaP(const Ctx &x, bool live = true) {
  constexpr int Q = P % POSE;
  int w = x.w, b = x.b;
  asm volatile("" : "+s"(w), "+s"(b));
  const int m = w & 3, j = b >> 3;
  unsigned slot = (unsigned)(P % RING) + (unsigned)m;
  unsigned fr;
  if constexpr (Q < POS2) fr = W1_F + (unsigned)((j & 15) * 96 + Q + m);
  else if constexpr (Q < POS5) fr = W2_F + (unsigned)(((Q - POS2) / 64) * 512 + (j & 7) * 64 + (Q - POS2) % 64 + m);
  else if constexpr (Q < POS6) fr = W5_F + (unsigned)((j & 15) * 112 + (Q - POS5) + m);
  else fr = W6_F + (unsigned)((j & 7) * 224 + (Q - POS6) + m);
  const unsigned lds = x.lds0 + slot * KB, so = live ? OFF_WT + fr * KB : OOB;
  asm volatile("s_mov_b32 m0, %0\n\ts_nop 0\n\tbuffer_load_dwordx4 %1, %2, %3 offen sc1 lds" ::"s"(lds), "v"(x.lane16), "s"(x.rs), "s"(so) : "memory");
}
template <int LO, int HI> DI void dmaR(const Ctx &x, bool live = true) {
  if constexpr (LO < HI) { dmaP<LO>(x, live); dmaR<LO + 4, HI>(x, live); }
}
template <int LO, int HI> DI void dmaRT(const Ctx &x, bool live = true) {
  if constexpr (LO < HI) { dmaP<LO>(x, live); asm volatile("s_waitcnt vmcnt(3)" ::: "memory"); dmaRT<LO + 4, HI>(x, live); }
}
template <int LO, int HI> DI void dmaR6(const Ctx &x, bool live = true) {
  if constexpr (LO < HI) { dmaP<LO>(x, live); asm volatile("s_waitcnt vmcnt(5)" ::: "memory"); dmaR6<LO + 4, HI>(x, live); }
}
DI void sig_set(const Ctx &x, unsigned v, unsigned word = M_SIG) { *(volatile LAS unsigned *)(x.sm + word + (unsigned)x.m * 4u) = v; }
DI void sig_wait(const Ctx &x, unsigned v, unsigned word = M_SIG) {
  for (unsigned sp = 0; sp < (1u << 20); ++sp) {
    const v4u s = *(volatile LAS v4u *)(x.sm + word);
    if (__builtin_amdgcn_readfirstlane(min(min(s.x, s.y), min(s.z, s.w))) >= v) break;
    __builtin_amdgcn_s_sleep(1);
  }
}

#define LDA(dst, voff, soff) \
  asm volatile("buffer_load_dwordx4 %0, %1, %2, %3 offen sc1" : "=v"(dst) : "v"(voff), "s"(x.rs), "s"(soff) : "memory")
#define LDA2(dst, voff, soff, imm) \
  asm volatile("buffer_load_dwordx2 %0, %1, %2, %3 offen offset:%4 sc1" : "=v"(dst) : "v"(voff), "s"(x.rs), "s"(soff), "i"(imm) : "memory")
#define WAITV1(n, r) asm volatile("s_waitcnt vmcnt(%1)" : "+v"(r) : "i"(n) : "memory")
#define WAITV2(n, r, q) asm volatile("s_waitcnt vmcnt(%2)" : "+v"(r), "+v"(q) : "i"(n) : "memory")
#define TIE(r) asm volatile("" : "+v"(r) : : "memory")
DI bool giveup(Ctx &x) {
  if (++x.spins > SPIN_CAP) {
    if (x.lane == 0) __hip_atomic_store(x.ctl + OFF_ABORT / 4, 1u, RLX_AGENT);
    return true;
  }
  __builtin_amdgcn_s_sleep(1);
  return false;
}
DI v4u load_sure(Ctx &x, unsigned voff, unsigned soff) {
  v4u v;
  while (true) {
    asm volatile("buffer_load_dwordx4 %0, %1, %2, %3 offen sc1\n\ts_waitcnt vmcnt(0)" : "=&v"(v) : "v"(voff), "s"(x.rs), "s"(soff) : "memory");
    if (!anybad(max4(v)) || giveup(x)) break;
  }
  return v;
}
DI v2u load_sure2(Ctx &x, unsigned voff, unsigned soff) {
  v2u v;
  while (true) {
    asm volatile("buffer_load_dwordx2 %0, %1, %2, %3 offen sc1\n\ts_waitcnt vmcnt(0)" : "=&v"(v) : "v"(voff), "s"(x.rs), "s"(soff) : "memory");
    if (!anybad(max(v.x, v.y)) || giveup(x)) break;
  }
  return v;
}

DI void warm(Ctx &x, unsigned voff, unsigned soff, unsigned soff2) {
}
#define LDC(dst, voff, soff) \
  asm volatile("buffer_load_dword %0, %1, %2, %3 offen sc1" : "=v"(dst) : "v"(voff), "s"(x.rs), "s"(soff) : "memory")

#define GBAR() do {} while (0)
#define SLEEP(n) sleepn(n)

DI void put_stats(const Ctx &x, v4u u, v4u v, unsigned sbuf) {
  const f4 a = ulo(u), b = uhi(u), c = ulo(v), d = uhi(v);
  const f4 s = (a + b) + (c + d), q = (a * a + b * b) + (c * c + d * d);
  float s1 = (s.x + s.y) + (s.z + s.w), s2 = (q.x + q.y) + (q.z + q.w);
  xred2<false>(s1, s2);
  if (x.g == 0) stg2(x.rs, (unsigned)x.tok * 8u, sbuf + (unsigned)(x.T * 8 + x.c) * 128u, v2u{clampf(s1), clampf(s2)});
}
struct Stats { v2u v[2]; };
DI void stats_load(const Ctx &x, Stats &s, unsigned soffT) {
  const unsigned vo = (unsigned)(x.g * 256 + x.tok * 8);
  LDA2(s.v[0], vo, soffT, 0); LDA2(s.v[1], vo, soffT, 128);
}
DI void stats_fin(Ctx &x, Stats &s, unsigned soffT, float &mean, float &var) {
  TIE(s.v[0]); TIE(s.v[1]);
  if (__builtin_expect(anybad(max(max(s.v[0].x, s.v[0].y), max(s.v[1].x, s.v[1].y))), 0)) {
    s.v[0] = load_sure2(x, (unsigned)(x.g * 256 + x.tok * 8), soffT);
    s.v[1] = load_sure2(x, (unsigned)(x.g * 256 + x.tok * 8 + 128), soffT);
  }
  float s1 = asf(s.v[0].x) + asf(s.v[1].x), s2 = asf(s.v[0].y) + asf(s.v[1].y);
  xred2<false>(s1, s2);
  mean = s1 * (1.0f / 512.0f);
  var = fmaxf(__builtin_fmaf(-mean, mean, s2 * (1.0f / 512.0f)), 0.0f);
}
DI const LAS v4u *lds4(const Ctx &x, unsigned off) { return (const LAS v4u *)(x.sm + off); }
template <int NT, int P> DI void ldaN(const Ctx &x, v4u (&a)[NT]) {
#pragma unroll
  for (int f = 0; f < NT; ++f) {
    const int slot = (P + f) % RING;
    a[f] = *(const LAS v4u *)(x.ab[slot / 60] + (slot % 60) * KB);
  }
}
template <int NT> DI void tieN(v4u (&a)[NT]) {
  if constexpr (NT == 4) asm volatile("" : "+v"(a[0]), "+v"(a[1]), "+v"(a[2]), "+v"(a[3]));
  else if constexpr (NT == 6) asm volatile("" : "+v"(a[0]), "+v"(a[1]), "+v"(a[2]), "+v"(a[3]), "+v"(a[4]), "+v"(a[5]));
  else asm volatile("" : "+v"(a[0]), "+v"(a[1]), "+v"(a[2]), "+v"(a[3]), "+v"(a[4]), "+v"(a[5]), "+v"(a[6]));
}
DI unsigned mx4(unsigned m, v4u v) {
  unsigned r;
  asm("v_max3_u32 %0, %1, %2, %3" : "=v"(r) : "v"(m), "v"(v.x), "v"(v.y));
  asm("v_max3_u32 %0, %1, %2, %3" : "=v"(m) : "v"(r), "v"(v.z), "v"(v.w));
  return m;
}
DI f4 ldcs(const Ctx &x, int tile) { return *(const LAS f4 *)(x.sm + M_CS + (unsigned)tile * 64u + (unsigned)x.g * 16u); }
constexpr int imin(int a, int b) { return a < b ? a : b; }

template <bool SH> DI unsigned vof(const Ctx &x, int k, const unsigned (&vo)[4]) { return SH ? vo[k >> 2] : x.lane16; }
template <int NK> constexpr int depth1() { return 8; }
template <int POS, int NK, bool SH, int K0, int K1, int I> struct KL1 {
  static DI void run(Ctx &x, v4u (&bf)[NK], f4 (&acc)[4], unsigned &mx, v4u (&cur)[4], v4u (&nxt)[4], unsigned so, const unsigned (&vo)[4],
                     unsigned sig) {
    v4u nn[4];
    constexpr int DP = depth1<NK>();
    WAITV1(imin(DP - 1, NK - 1 - I), bf[I]);
    if constexpr (I + DP < NK) LDA(bf[I + DP], vof<SH>(x, I + DP, vo), so + (unsigned)(I + DP) * KB);
    if constexpr (I + DP == NK - 1) { if (sig) sig_set(x, sig); }
    tieN<4>(cur);
    SB();
    if constexpr (I + 2 < K1) ldaN<4, POS + 4 * (I + 2)>(x, nn);
    mx = mx4(mx, bf[I]);
#pragma unroll
    for (int f = 0; f < 4; ++f) acc[f] = mfma32(cur[f], bf[I], acc[f]);
#pragma unroll
    for (int f = 0; f < 4; ++f) {
      __builtin_amdgcn_sched_group_barrier(0x008, 1, 0);
      __builtin_amdgcn_sched_group_barrier(0x100, 1, 0);
      __builtin_amdgcn_sched_group_barrier(0x002, 1, 0);
    }
    SB();
    if constexpr (I + 1 < K1) KL1<POS, NK, SH, K0, K1, I + 1>::run(x, bf, acc, mx, nxt, nn, so, vo, sig);
  }
};
template <int POS, bool SH, int K1, int I> struct KL1R {
  static DI void run(Ctx &x, f4 (&acc)[4], unsigned so, const unsigned (&vo)[4]) {
    const v4u bv = load_sure(x, vof<SH>(x, I, vo), so + (unsigned)I * KB);
    v4u a[4];
    ldaN<4, POS + 4 * I>(x, a);
#pragma unroll
    for (int f = 0; f < 4; ++f) acc[f] = mfma32(a[f], bv, acc[f]);
    if constexpr (I + 1 < K1) KL1R<POS, SH, K1, I + 1>::run(x, acc, so, vo);
  }
};
template <bool ST> DI unsigned canary_poll1(Ctx &x, unsigned so, unsigned cvo, Stats &st, unsigned sst) {
  unsigned cn;
  LDC(cn, cvo, so);
  if constexpr (ST) stats_load(x, st, sst);
  WAITV1(0, cn);
  if constexpr (ST) { TIE(st.v[0]); TIE(st.v[1]); cn = max(cn, max(max(st.v[0].x, st.v[0].y), max(st.v[1].x, st.v[1].y))); }
  return cn;
}
template <int NK, bool SH, bool ST>
DI void pro1(Ctx &x, v4u (&bf)[NK], unsigned so, const unsigned (&vo)[4], unsigned cvo, Stats &st, unsigned sst, unsigned &cn) {
  while (anybad(canary_poll1<ST>(x, so, cvo, st, sst)) && !giveup(x)) {}
  cn = 0;
#pragma unroll
  for (int i = 0; i < depth1<NK>(); ++i) LDA(bf[i], vof<SH>(x, i, vo), so + (unsigned)i * KB);
}
template <int NK, bool SH, bool ST>
DI void chk1(Ctx &x, v4u (&bf)[NK], unsigned so, const unsigned (&vo)[4], unsigned cvo, Stats &st, unsigned sst, unsigned cn) {
}
template <int POS, int NK, bool SH, int K0, int K1>
DI void kloop1(Ctx &x, v4u (&bf)[NK], f4 (&acc)[4], unsigned so, const unsigned (&vo)[4], unsigned sig) {
  f4 save[4];
#pragma unroll
  for (int f = 0; f < 4; ++f) save[f] = acc[f];
  unsigned mx = 0;
  v4u a0[4], a1[4];
  ldaN<4, POS + 4 * K0>(x, a0);
  ldaN<4, POS + 4 * (K0 + 1)>(x, a1);
  KL1<POS, NK, SH, K0, K1, K0>::run(x, bf, acc, mx, a0, a1, so, vo, sig);
  if (__builtin_expect(anybad(mx), 0)) {
#pragma unroll
    for (int f = 0; f < 4; ++f) acc[f] = save[f];
    KL1R<POS, SH, K1, K0>::run(x, acc, so, vo);
  }
}
template <int NT, int POS, int K0, int K1, int I> struct KL2 {
  static DI void run(Ctx &x, v4u (&bf)[16][2], f4 (&acc)[NT][2], unsigned &mx, v4u (&cur)[NT], unsigned so, unsigned sig) {
    v4u nxt[NT];
    WAITV2(2 * imin(3, 15 - I), bf[I][0], bf[I][1]);
    if constexpr (I + 4 < 16) {
      LDA(bf[I + 4][0], x.lane16, so + (unsigned)(I + 4) * KB); LDA(bf[I + 4][1], x.lane16, so + (unsigned)(16 + I + 4) * KB);
    }
    if constexpr (I == (NT == 6 ? 8 : 11)) sig_set(x, sig);
    tieN<NT>(cur);
    SB();
    if constexpr (I + 1 < K1) ldaN<NT, POS + (I + 1) * NT>(x, nxt);
    mx = mx4(mx4(mx, bf[I][0]), bf[I][1]);
#pragma unroll
    for (int f = 0; f < NT; ++f) {
      acc[f][0] = mfma32(cur[f], bf[I][0], acc[f][0]);
      acc[f][1] = mfma32(cur[f], bf[I][1], acc[f][1]);
    }
#pragma unroll
    for (int f = 0; f < NT; ++f) {
      __builtin_amdgcn_sched_group_barrier(0x008, 1, 0);
      __builtin_amdgcn_sched_group_barrier(0x100, 1, 0);
    }
#pragma unroll
    for (int f = 0; f < NT; ++f) {
      __builtin_amdgcn_sched_group_barrier(0x008, 1, 0);
      __builtin_amdgcn_sched_group_barrier(0x002, 1, 0);
    }
    SB();
    if constexpr (I + 1 < K1) KL2<NT, POS, K0, K1, I + 1>::run(x, bf, acc, mx, nxt, so, sig);
  }
};
template <int NT, int POS, int K1, int I> struct KL2R {
  static DI void run(Ctx &x, f4 (&acc)[NT][2], unsigned so) {
    const v4u b0 = load_sure(x, x.lane16, so + (unsigned)I * KB), b1 = load_sure(x, x.lane16, so + (unsigned)(16 + I) * KB);
    v4u a[NT];
    ldaN<NT, POS + I * NT>(x, a);
#pragma unroll
    for (int f = 0; f < NT; ++f) {
      acc[f][0] = mfma32(a[f], b0, acc[f][0]);
      acc[f][1] = mfma32(a[f], b1, acc[f][1]);
    }
    if constexpr (I + 1 < K1) KL2R<NT, POS, K1, I + 1>::run(x, acc, so);
  }
};
DI unsigned stmax(const Stats (&st)[2]) {
  return max(max(max(st[0].v[0].x, st[0].v[0].y), max(st[0].v[1].x, st[0].v[1].y)), max(max(st[1].v[0].x, st[1].v[0].y), max(st[1].v[1].x, st[1].v[1].y)));
}
DI void poll2(Ctx &x, Stats (&st)[2], unsigned so, unsigned sst) {
  const unsigned cvo = (unsigned)(x.lane & 31) * KB + 1020u;
  while (true) {
    unsigned cn;
    LDC(cn, cvo, so);
    stats_load(x, st[0], sst); stats_load(x, st[1], sst + 1024u);
    WAITV1(0, cn);
    TIE(st[0].v[0]); TIE(st[0].v[1]); TIE(st[1].v[0]); TIE(st[1].v[1]);
    if (!anybad(max(cn, stmax(st))) || giveup(x)) break;
  }
}
DI void first2(Ctx &x, v4u (&bf)[16][2], unsigned so) {
#pragma unroll
  for (int i = 0; i < 4; ++i) { LDA(bf[i][0], x.lane16, so + (unsigned)i * KB); LDA(bf[i][1], x.lane16, so + (unsigned)(16 + i) * KB); }
}
DI void pro2(Ctx &x, v4u (&bf)[16][2], Stats (&st)[2], unsigned so, unsigned sst, unsigned &cn) {
  poll2(x, st, so, sst);
  cn = 0;
  first2(x, bf, so);
}
DI void chk2(Ctx &x, v4u (&bf)[16][2], Stats (&st)[2], unsigned so, unsigned sst, unsigned cn) {
}
template <int NT, int POS, int K0, int K1>
DI void kloop2(Ctx &x, v4u (&bf)[16][2], f4 (&acc)[NT][2], unsigned so, unsigned sig, unsigned &mx) {
  v4u a0[NT];
  ldaN<NT, POS + K0 * NT>(x, a0);
  KL2<NT, POS, K0, K1, K0>::run(x, bf, acc, mx, a0, so, sig);
}
template <int NT, int POS> DI void redo2(Ctx &x, f4 (&acc)[NT][2], unsigned so) {
#pragma unroll
  for (int f = 0; f < NT; ++f) { acc[f][0] = f4{0.f, 0.f, 0.f, 0.f}; acc[f][1] = f4{0.f, 0.f, 0.f, 0.f}; }
  KL2R<NT, POS, 16, 0>::run(x, acc, so);
}

DI void featmap4(const f4 (&v)[4], const v2u (&wphi)[2], v2u (&plus)[4], v2u (&minus)[4]) {
  const float L2E = 1.4426950408889634f;
  f4 p[4];
#pragma unroll
  for (int i = 0; i < 4; ++i) p[i] = mfma16(wphi[i & 1], v2u{pk2(v[i].x, v[i].y), pk2(v[i].z, v[i].w)}, f4{0.f, 0.f, 0.f, 0.f});
  float mx[4], nm[4];
#pragma unroll
  for (int i = 0; i < 4; ++i) {
    p[i] *= L2E;
    mx[i] = fmaxf(fmaxf(p[i].x, p[i].y), fmaxf(p[i].z, p[i].w));
    nm[i] = -fminf(fminf(p[i].x, p[i].y), fminf(p[i].z, p[i].w));
  }
  xred4<true>(mx[0], mx[1], mx[2], mx[3]);
  xred4<true>(nm[0], nm[1], nm[2], nm[3]);
  f4 e[4], n[4];
  float se[4], sn[4];
#pragma unroll
  for (int i = 0; i < 4; ++i) {
    const f4 a = p[i] - mx[i], b = -p[i] - nm[i];
    e[i] = f4{__builtin_amdgcn_exp2f(a.x), __builtin_amdgcn_exp2f(a.y), __builtin_amdgcn_exp2f(a.z), __builtin_amdgcn_exp2f(a.w)};
    n[i] = f4{__builtin_amdgcn_exp2f(b.x), __builtin_amdgcn_exp2f(b.y), __builtin_amdgcn_exp2f(b.z), __builtin_amdgcn_exp2f(b.w)};
    se[i] = (e[i].x + e[i].y) + (e[i].z + e[i].w);
    sn[i] = (n[i].x + n[i].y) + (n[i].z + n[i].w);
  }
  xred4<false>(se[0], se[1], se[2], se[3]);
  xred4<false>(sn[0], sn[1], sn[2], sn[3]);
#pragma unroll
  for (int i = 0; i < 4; ++i) {
    const f4 a = e[i] * __builtin_amdgcn_rcpf(se[i]), b = n[i] * __builtin_amdgcn_rcpf(sn[i]);
    plus[i] = v2u{pk2(a.x, a.y), pk2(a.z, a.w)};
    minus[i] = v2u{pk2(b.x, b.y), pk2(b.z, b.w)};
  }
}
DI v2u trread(const Ctx &x, unsigned off) {
  s4 r = __builtin_amdgcn_ds_read_tr16_b64_v4i16((LAS s4 *)(x.sm + off));
  return __builtin_bit_cast(v2u, r);
}
DI f4 shsum(f4 s) {
#pragma unroll
  for (int o = 4; o < 64; o <<= 1) {
    s.x += __shfl_xor(s.x, o); s.y += __shfl_xor(s.y, o); s.z += __shfl_xor(s.z, o); s.w += __shfl_xor(s.w, o);
  }
  return s;
}

DI void poll_flags(Ctx &x, unsigned off, unsigned tag) {
  for (unsigned sp = 0;; ++sp) {
    unsigned f0, f1, f2, f3;
    const unsigned vo = (unsigned)x.lane * 64u;
    LDC(f0, vo, off); LDC(f1, vo, off + 4096u); LDC(f2, vo, off + 8192u); LDC(f3, vo, off + 12288u);
    asm volatile("s_waitcnt vmcnt(0)" : "+v"(f0), "+v"(f1), "+v"(f2), "+v"(f3)::"memory");
    if (__all(f0 == tag && f1 == tag && f2 == tag && f3 == tag)) break;
    if (sp > SPIN_CAP) {
      if (x.lane == 0) __hip_atomic_store(x.ctl + OFF_ABORT / 4, 3u, RLX_AGENT);
      break;
    }
    __builtin_amdgcn_s_sleep(8);
  }
}
DI unsigned sigv(int L, int ph) { return (unsigned)(L * 8 + ph + 1); }

DI void compute_path(Ctx &x, const Params &p, v4u (&xres)[2], const v2u (&wph)[2], unsigned tag) {
  const unsigned vz[4] = {0u, 0u, 0u, 0u};
  v4u x1r[2], x2r[2];
#pragma unroll 1
  for (int L = 0; L < 2; ++L) {
    const unsigned lb0 = OFF_L0 + (unsigned)L * L_STRIDE;
    v4u phq[2][2];
    GBAR();
    {
      refresh(x);
      FS(0);
      unsigned lb = lb0;
      asm volatile("" : "+s"(lb));
      v4u bf[16][2];
      Stats st[2];
      const unsigned so = lb + O_XB + (unsigned)x.TA * 16u * KB, sst = lb + O_XS + (unsigned)x.TA * 1024u;
      unsigned cn;
      pro2(x, bf, st, so, sst, cn);
      FS(1); FSP(0);
      BAR();
      if (L == 0 && threadIdx.x == 0) __hip_atomic_store(x.ctl + OFF_FLAGS2 / 4 + x.b * 16, tag, RLX_AGENT);
      chk2(x, bf, st, so, sst, cn);
      FS(2);
      f4 acc[6][2];
#pragma unroll
      for (int f = 0; f < 6; ++f) { acc[f][0] = f4{0.f, 0.f, 0.f, 0.f}; acc[f][1] = f4{0.f, 0.f, 0.f, 0.f}; }
      unsigned mx = 0;
      kloop2<6, POS1, 0, 16>(x, bf, acc, so, sigv(L, 0), mx);
      if (__builtin_expect(anybad(mx), 0)) redo2<6, POS1>(x, acc, so);
      FS(3);
      BAR();
      MARK("P1epi");
      f4 val[2][6];
#pragma unroll
      for (int j = 0; j < 2; ++j) {
        float mean, var;
        stats_fin(x, st[j], sst + (unsigned)j * 1024u, mean, var);
        const float rstd = __builtin_amdgcn_rsqf(var + 1e-5f), mr = mean * rstd;
#pragma unroll
        for (int f = 0; f < 6; ++f) val[j][f] = acc[f][j] * rstd - ldcs(x, f) * mr;
      }
      {
        v2u pl[4], mi[4];
        const f4 kk[4] = {val[0][2], val[0][3], val[1][2], val[1][3]};
        featmap4(kk, wph, pl, mi);
#pragma unroll
        for (int i = 0; i < 4; ++i) {
          const int j = i >> 1, h = i & 1;
          const unsigned img = IMG + (unsigned)((2 * x.m + j) * 16 + x.tok) * 32u + (unsigned)x.g * 8u + (unsigned)(h * 3) * 4096u;
          *(LAS v2u *)(x.sm + img) = pl[i];
          *(LAS v2u *)(x.sm + img + 4096u) = mi[i];
          *(LAS v2u *)(x.sm + img + 8192u) = v2u{pk2(val[j][4 + h].x, val[j][4 + h].y), pk2(val[j][4 + h].z, val[j][4 + h].w)};
        }
      }
      MARK("P1epiend");
      FS(4);
      BAR();
      FS(5);
      {
        v2u pl[4], mi[4];
        TIE(val[0][0]); TIE(val[0][1]); TIE(val[1][0]); TIE(val[1][1]);
        const f4 qq[4] = {val[0][0], val[0][1], val[1][0], val[1][1]};
        featmap4(qq, wph, pl, mi);
#pragma unroll
        for (int i = 0; i < 4; ++i) phq[i >> 1][i & 1] = v4u{pl[i].x, pl[i].y, mi[i].x, mi[i].y};
      }
      FS(6);
    }
    GBAR();
    GBAR();
    {
      refresh(x);
      unsigned lb = lb0;
      asm volatile("" : "+s"(lb));
      BAR();
      FS(7);
      v4u ka[2], za[2];
#pragma unroll
      for (int h = 0; h < 2; ++h) {
        ka[h] = *lds4(x, M_KVA + (unsigned)h * 1024u + x.lane16);
        const v2u z0 = *(const LAS v2u *)(x.sm + M_ZA + (unsigned)(h * 2) * 32u + (unsigned)x.g * 8u);
        const v2u z1 = *(const LAS v2u *)(x.sm + M_ZA + (unsigned)(h * 2 + 1) * 32u + (unsigned)x.g * 8u);
        za[h] = v4u{z0.x, z0.y, z1.x, z1.y};
      }
#pragma unroll
      for (int j = 0; j < 2; ++j) {
        f4 o[2];
#pragma unroll
        for (int h = 0; h < 2; ++h) {
          const f4 num = mfma32(ka[h], phq[j][h], f4{0.f, 0.f, 0.f, 0.f});
          const f4 den = mfma32(za[h], phq[j][h], f4{0.f, 0.f, 0.f, 0.f});
          o[h] = num * __builtin_amdgcn_rcpf(den.x + 1e-6f);
        }
        stg(x.rs, x.lane16, lb + O_OB + (unsigned)((x.TA + j) * 16 + x.pp) * KB, pack8(o[0], o[1]));
      }
      FS(8);
      {
        const unsigned ob = lb + O_OB + (unsigned)(x.TA * 16 + x.pp) * KB;
        warm(x, (unsigned)((x.lane >> 3) & 1) * 16u * KB + (unsigned)(x.lane & 7) * 128u, ob, ob);
      }
    }
    GBAR();
    {
      refresh(x);
      unsigned lb = lb0;
      asm volatile("" : "+s"(lb));
      SLEEP(DLY_2);
      FS(9);
      v4u bf[16];
      const unsigned so = lb + O_OB + (unsigned)x.T * 16u * KB;
      Stats st;
      unsigned cn;
      const unsigned cvo = (unsigned)(x.lane & 15) * KB + 1020u;
      pro1<16, false, false>(x, bf, so, vz, cvo, st, 0u, cn);
      FS(10); FSP(1);
      BAR();
      chk1<16, false, false>(x, bf, so, vz, cvo, st, 0u, cn);
      FS(11);
      f4 acc[4];
#pragma unroll
      for (int f = 0; f < 4; ++f) acc[f] = f4{0.f, 0.f, 0.f, 0.f};
      kloop1<POS2, 16, false, 0, 16>(x, bf, acc, so, vz, sigv(L, 2));
      FS(12);
      x1r[0] = pack8(ulo(xres[0]) + acc[0], uhi(xres[0]) + acc[1]);
      x1r[1] = pack8(ulo(xres[1]) + acc[2], uhi(xres[1]) + acc[3]);
      const unsigned fo = lb + O_X1B + (unsigned)(x.T * 16 + 2 * x.c) * KB;
      stg(x.rs, x.lane16, fo, x1r[0]); stg(x.rs, x.lane16, fo + KB, x1r[1]);
      put_stats(x, x1r[0], x1r[1], lb + O_X1S);
      sig_set(x, sigv(L, 2), M_SIG2);
      FS(13);
      warm(x, (unsigned)(x.lane & 15) * 128u, fo, lb + O_X1S + (unsigned)(x.T * 8 + x.c) * 128u);
    }
    float mean1, rstd1;
    GBAR();
    {
      refresh(x);
      unsigned lb = lb0;
      asm volatile("" : "+s"(lb));
      SLEEP(DLY_3);
      v4u bf[16];
      Stats st;
      const unsigned so = lb + O_X1B + (unsigned)x.T * 16u * KB;
      unsigned cn;
      const unsigned cvo = (unsigned)(x.lane & 15) * KB + 1020u;
      pro1<16, false, true>(x, bf, so, vz, cvo, st, lb + O_X1S + (unsigned)x.T * 1024u, cn);
      FSP(2);
      BAR();
      chk1<16, false, true>(x, bf, so, vz, cvo, st, lb + O_X1S + (unsigned)x.T * 1024u, cn);
      FS(14);
      f4 acc[4];
#pragma unroll
      for (int f = 0; f < 4; ++f) acc[f] = f4{0.f, 0.f, 0.f, 0.f};
      kloop1<POS3, 16, false, 0, 16>(x, bf, acc, so, vz, sigv(L, 3));
      FS(15);
      float var;
      stats_fin(x, st, lb + O_X1S + (unsigned)x.T * 1024u, mean1, var);
      rstd1 = __builtin_amdgcn_rsqf(var + 1e-5f);
      const float a = rstd1 * __builtin_amdgcn_rsqf(__builtin_fmaf(var * rstd1, rstd1, 1e-5f)), ma = mean1 * a;
      f4 y[4];
#pragma unroll
      for (int f = 0; f < 4; ++f) y[f] = gelu4(acc[f] * a - ldcs(x, 6 + f) * ma);
      const unsigned fo = lb + O_HB + (unsigned)(x.T * 16 + 2 * x.c) * KB;
      stg(x.rs, x.lane16, fo, pack8(y[0], y[1])); stg(x.rs, x.lane16, fo + KB, pack8(y[2], y[3]));
      sig_set(x, sigv(L, 3), M_SIG2);
      FS(16);
      warm(x, (unsigned)(x.lane & 15) * 128u, fo, fo);
    }
    GBAR();
    {
      refresh(x);
      unsigned lb = lb0;
      asm volatile("" : "+s"(lb));
      SLEEP(DLY_4);
      v4u bf[16];
      unsigned vo[4];
#pragma unroll
      for (int q = 0; q < 4; ++q) {
        const int d = q == 0 ? -1 : (q == 1 ? 1 : (q == 2 ? -2 : 2));
        const int src = 16 * x.T + x.tok + d;
        vo[q] = (src >= 0 && src < 2048) ? (unsigned)((src >> 4) * 16 * 1024 + (x.g * 16 + (src & 15)) * 16) : OOB;
      }
      const unsigned so = lb + O_HB;
      unsigned cvo;
      {
        const int s_ = x.lane & 15, nb = x.T + (((s_ >> 2) & 1) ? 1 : -1);
        const int tt = (x.lane & 16) ? nb : x.T;
        cvo = (tt >= 0 && tt < 128) ? (unsigned)(tt * 16 + s_) * KB + 1020u : OOB;
      }
      Stats st;
      unsigned cn;
      pro1<16, true, false>(x, bf, so, vo, cvo, st, 0u, cn);
      FSP(3);
      BAR();
      chk1<16, true, false>(x, bf, so, vo, cvo, st, 0u, cn);
      FS(17);
      f4 acc[4];
#pragma unroll
      for (int f = 0; f < 4; ++f) acc[f] = f4{0.f, 0.f, 0.f, 0.f};
      MARK("P4loop");
      kloop1<POS4, 16, true, 0, 16>(x, bf, acc, so, vo, sigv(L, 4));
      MARK("P4end");
      FS(18);
      const float mr = mean1 * rstd1;
      x2r[0] = pack8(ulo(x1r[0]) * rstd1 - mr + acc[0], uhi(x1r[0]) * rstd1 - mr + acc[1]);
      x2r[1] = pack8(ulo(x1r[1]) * rstd1 - mr + acc[2], uhi(x1r[1]) * rstd1 - mr + acc[3]);
      const unsigned fo = lb + O_X2B + (unsigned)(x.T * 16 + 2 * x.c) * KB;
      stg(x.rs, x.lane16, fo, x2r[0]); stg(x.rs, x.lane16, fo + KB, x2r[1]);
      put_stats(x, x2r[0], x2r[1], lb + O_X2S);
      sig_set(x, sigv(L, 4), M_SIG2);
      FS(19);
      warm(x, (unsigned)(x.lane & 15) * 128u, fo, lb + O_X2S + (unsigned)(x.T * 8 + x.c) * 128u);
    }
    GBAR();
    {
      refresh(x);
      unsigned lb = lb0;
      asm volatile("" : "+s"(lb));
      SLEEP(DLY_5);
      v4u bf[16][2];
      Stats st[2];
      const unsigned so = lb + O_X2B + (unsigned)x.TA * 16u * KB, sst = lb + O_X2S + (unsigned)x.TA * 1024u;
      unsigned cn;
      pro2(x, bf, st, so, sst, cn);
      FSP(4);
      BAR();
      chk2(x, bf, st, so, sst, cn);
      FS(20);
      f4 acc[7][2];
#pragma unroll
      for (int f = 0; f < 7; ++f) { acc[f][0] = f4{0.f, 0.f, 0.f, 0.f}; acc[f][1] = f4{0.f, 0.f, 0.f, 0.f}; }
      unsigned mx = 0;
      MARK("P5loop");
      kloop2<7, POS5, 0, 13>(x, bf, acc, so, sigv(L, 5), mx);
      BAR();
      kloop2<7, POS5, 13, 16>(x, bf, acc, so, sigv(L, 5), mx);
      if (__builtin_expect(anybad(mx), 0)) redo2<7, POS5>(x, acc, so);
      FS(21);
      BAR();
      FS(22);
      MARK("P5epi");
      const int u = x.pp >> 1, odd = x.pp & 1;
#pragma unroll
      for (int j = 0; j < 2; ++j) {
        float mean, var;
        stats_fin(x, st[j], sst + (unsigned)j * 1024u, mean, var);
        const float rstd = __builtin_amdgcn_rsqf(var + 1e-5f), mr = mean * rstd;
        const unsigned hb = lb + O_H3B + (unsigned)((x.TA + j) * 56 + 7 * u) * KB;
#pragma unroll
        for (int r = 0; r < 3; ++r) {
          const f4 y0 = gelu4(acc[2 * r][j] * rstd - ldcs(x, 10 + 2 * r) * mr), y1 = gelu4(acc[2 * r + 1][j] * rstd - ldcs(x, 11 + 2 * r) * mr);
          stg(x.rs, x.lane16, hb + (unsigned)(odd ? 4 + r : r) * KB, pack8(y0, y1));
        }
        const f4 y = gelu4(acc[6][j] * rstd - ldcs(x, 16) * mr);
        stg2(x.rs, x.lane16 + (unsigned)odd * 8u, hb + 3 * KB, v2u{clampw(pk2(y.x, y.y)), clampw(pk2(y.z, y.w))});
      }
      FS(23);
      {
        const unsigned hb0 = lb + O_H3B + (unsigned)(x.TA * 56 + 7 * u + (odd ? 3 : 0)) * KB;
        warm(x, (unsigned)(x.lane >> 5) * 56u * KB + (unsigned)(x.lane & 31) * 128u, hb0, hb0);
      }
    }
    GBAR();
    {
      refresh(x);
      unsigned lb = lb0;
      asm volatile("" : "+s"(lb));
      SLEEP(DLY_6);
      v4u hf[56];
      const unsigned so = lb + O_H3B + (unsigned)x.T * 56u * KB;
      Stats st;
      unsigned cn;
      const unsigned cvo = (unsigned)min(x.lane, 55) * KB + 1020u;
      pro1<56, false, false>(x, hf, so, vz, cvo, st, 0u, cn);
      FSP(5);
      BAR();
      chk1<56, false, false>(x, hf, so, vz, cvo, st, 0u, cn);
      FS(24);
      f4 acc[4];
#pragma unroll
      for (int f = 0; f < 4; ++f) acc[f] = f4{0.f, 0.f, 0.f, 0.f};
      kloop1<POS6, 56, false, 0, 17>(x, hf, acc, so, vz, 0u);
      BAR();
      FS(25);
      kloop1<POS6, 56, false, 17, 39>(x, hf, acc, so, vz, 0u);
      BAR();
      FS(26);
      kloop1<POS6, 56, false, 39, 56>(x, hf, acc, so, vz, 0u);
      FS(27);
      BAR();
      xres[0] = pack8(ulo(x1r[0]) + (ulo(x2r[0]) + acc[0]), uhi(x1r[0]) + (uhi(x2r[0]) + acc[1]));
      xres[1] = pack8(ulo(x1r[1]) + (ulo(x2r[1]) + acc[2]), uhi(x1r[1]) + (uhi(x2r[1]) + acc[3]));
      if (L == 0) {
        const unsigned fo = lb + L_STRIDE + O_XB + (unsigned)(x.T * 16 + 2 * x.c) * KB;
        stg(x.rs, x.lane16, fo, xres[0]); stg(x.rs, x.lane16, fo + KB, xres[1]);
        put_stats(x, xres[0], xres[1], lb + L_STRIDE + O_XS);
        warm(x, (unsigned)(x.lane & 15) * 128u, fo, lb + L_STRIDE + O_XS + (unsigned)(x.T * 8 + x.c) * 128u);
        SLEEP(DLY_1);
      } else {
        GAS unsigned char *op = (GAS unsigned char *)p.out + ((size_t)(16 * x.T + x.tok) * 512 + 64 * x.c + 4 * x.g) * 2;
        *(GAS v2u *)op = v2u{xres[0].x, xres[0].y};
        *(GAS v2u *)(op + 32) = v2u{xres[0].z, xres[0].w};
        *(GAS v2u *)(op + 64) = v2u{xres[1].x, xres[1].y};
        *(GAS v2u *)(op + 96) = v2u{xres[1].z, xres[1].w};
      }
      FS(28); FSP(6);
    }
  }
}

DI void poison_h3(Ctx &x, unsigned lb, int j) {
  const int u = x.pp >> 1, odd = x.pp & 1;
  const unsigned hb = lb + O_H3B + (unsigned)((x.TA + j) * 56 + 7 * u) * KB;
#pragma unroll
  for (int r = 0; r < 3; ++r) poi4(x.rs, x.lane16, hb + (unsigned)(odd ? 4 + r : r) * KB);
  poi2(x.rs, x.lane16 + (unsigned)odd * 8u, hb + 3 * KB);
}
DI void helper_path(Ctx &x, unsigned tag) {
#pragma unroll 1
  for (int L = 0; L < 2; ++L) {
    const unsigned lb0 = OFF_L0 + (unsigned)L * L_STRIDE;
    GBAR();
    {
      refresh(x);
      unsigned lb = lb0;
      asm volatile("" : "+s"(lb));
      FS(0);
      DRAIN_BAR();
      FS(1);
      if (L != 0) {
        sig_wait(x, sigv(L, 0));
        dmaR<96, 156>(x);
      }
      BAR();
      {
        const unsigned fo = (unsigned)(x.T * 16 + 2 * x.c) * KB, sto = (unsigned)(x.T * 8 + x.c) * 128u;
        poi4(x.rs, x.lane16, lb + O_X1B + fo); poi4(x.rs, x.lane16, lb + O_X1B + fo + KB);
        poi4(x.rs, x.lane16, lb + O_X2B + fo); poi4(x.rs, x.lane16, lb + O_X2B + fo + KB);
        if (x.g == 0) { poi2(x.rs, (unsigned)x.tok * 8u, lb + O_X1S + sto); poi2(x.rs, (unsigned)x.tok * 8u, lb + O_X2S + sto); }
      }
      FS(2);
      BAR();
      FS(3);
      {
        const int h = x.m >> 1, sg = x.m & 1;
        const unsigned lo = (unsigned)(4 * x.g + (x.tok >> 2)) * 32u + (unsigned)(x.tok & 3) * 8u;
        const unsigned ia = IMG + (unsigned)(h * 3 + sg) * 4096u + lo, iv = IMG + (unsigned)(h * 3 + 2) * 4096u + lo;
        f4 kv = {0.f, 0.f, 0.f, 0.f}, z = {0.f, 0.f, 0.f, 0.f};
#pragma unroll
        for (int tb = 0; tb < 4; ++tb) {
          const v2u a0 = trread(x, ia + tb * 1024u), a1 = trread(x, ia + tb * 1024u + 512u);
          const v2u b0 = trread(x, iv + tb * 1024u), b1 = trread(x, iv + tb * 1024u + 512u);
          const v4u a = {a0.x, a0.y, a1.x, a1.y};
          kv = mfma32(a, v4u{b0.x, b0.y, b1.x, b1.y}, kv);
          z = mfma32(a, v4u{0x3F803F80u, 0x3F803F80u, 0x3F803F80u, 0x3F803F80u}, z);
        }
        stg(x.rs, x.lane16, lb + O_KVP + (unsigned)((x.pp * 16 + x.tau) * 4 + x.m) * KB, clamp4(__builtin_bit_cast(v4u, kv)));
        if (x.tok == 0) stg(x.rs, (unsigned)x.g * 16u, lb + O_KVZ + (unsigned)((x.pp * 4 + x.m) * 16 + x.tau) * 64u, clamp4(__builtin_bit_cast(v4u, z)));
      }
      FS(4);
    }
    GBAR();
    {
      refresh(x);
      unsigned lb = lb0;
      asm volatile("" : "+s"(lb));
      if (L == 0) {
        if (x.m == 0) {
          poll_flags(x, OFF_FLAGS2, tag);
          *(volatile LAS unsigned *)(x.sm + M_RDY) = 1u;
        } else {
          for (unsigned sp = 0; sp < (1u << 20); ++sp) {
            if (__builtin_amdgcn_readfirstlane(*(volatile LAS unsigned *)(x.sm + M_RDY))) break;
            __builtin_amdgcn_s_sleep(2);
          }
        }
        v4u cv[3];
#pragma unroll
        for (int jj = 0; jj < 3; ++jj) {
          const int u = min(6 + x.m + 4 * jj, 16);
          cv[jj] = ldg(x.rs, x.lane16, OFF_CSP + (u < 10 ? 96u + (unsigned)(x.c * 4 + u - 6) : 128u + (unsigned)(x.pp * 7 + u - 10)) * KB);
        }
#pragma unroll
        for (int jj = 0; jj < 3; ++jj) {
          const f4 s = shsum(__builtin_bit_cast(f4, cv[jj]));
          const int u = 6 + x.m + 4 * jj;
          if (u < 17 && x.lane < 4) *(LAS f4 *)(x.sm + M_CS + (unsigned)u * 64u + (unsigned)x.lane * 16u) = s;
        }
      }
      SLEEP(DLY_KV);
      v4u pv[16];
      const unsigned so = lb + O_KVP + (unsigned)(x.pp * 64 + x.m) * KB, sz = lb + O_KVZ + (unsigned)(x.pp * 4 + x.m) * KB;
      v4u zv;
      {
        const unsigned cvo = (unsigned)(x.lane & 15) * 4096u + 1020u;
        while (true) {
          unsigned cn;
          LDC(cn, cvo, so);
          LDA(zv, x.lane16, sz);
          WAITV2(0, cn, zv);
          if (!anybad(max(cn, max4(zv))) || giveup(x)) break;
        }
      }
      bool again;
      do {
        asm volatile("" ::: "memory");
#pragma unroll
        for (int i = 0; i < 16; ++i) pv[i] = ldg(x.rs, x.lane16, so + (unsigned)i * 4096u);
        unsigned mx = 0;
#pragma unroll
        for (int i = 0; i < 16; ++i) mx = max(mx, max4(pv[i]));
        again = anybad(mx) && !giveup(x);
      } while (__builtin_expect(again, 0));
      f4 s = __builtin_bit_cast(f4, pv[0]);
#pragma unroll
      for (int i = 1; i < 16; ++i) s += __builtin_bit_cast(f4, pv[i]);
      *(LAS v2u *)(x.sm + M_KVA + (unsigned)(x.m >> 1) * 1024u + x.lane16 + (unsigned)(x.m & 1) * 8u) = v2u{pk2(s.x, s.y), pk2(s.z, s.w)};
      const f4 zs = shsum(__builtin_bit_cast(f4, zv));
      if (x.lane < 4) *(LAS v2u *)(x.sm + M_ZA + (unsigned)x.m * 32u + (unsigned)x.lane * 8u) = v2u{pk2(zs.x, zs.y), pk2(zs.z, zs.w)};
      FS(5); FSP(0);
      GBAR();
      BAR();
      FS(6);
      if (L == 0) dmaR<96, 156>(x);
      dmaR<156, 188>(x);
    }
    GBAR();
    {
      refresh(x);
      unsigned lb = lb0;
      asm volatile("" : "+s"(lb));
      DRAIN_BAR();
      FS(7);
      sig_wait(x, sigv(L, 2));
      dmaR<188, 220>(x);
      sig_wait(x, sigv(L, 2), M_SIG2);
      dmaR<220, 252>(x);
      poison_h3(x, lb, 0); poison_h3(x, lb, 1);
    }
    GBAR();
    {
      refresh(x);
      unsigned lb = lb0;
      asm volatile("" : "+s"(lb));
      WAITV_BAR(8);
      FS(8);
      sig_wait(x, sigv(L, 3));
      FS(15);
      dmaR<252, 284>(x);
      sig_wait(x, sigv(L, 3), M_SIG2);
      dmaR<284, 316>(x);
      FS(16);
      if (L == 0) {
        const unsigned fo = lb + L_STRIDE + O_XB + (unsigned)(x.T * 16 + 2 * x.c) * KB;
        poi4(x.rs, x.lane16, fo); poi4(x.rs, x.lane16, fo + KB);
        if (x.g == 0) poi2(x.rs, (unsigned)x.tok * 8u, lb + L_STRIDE + O_XS + (unsigned)(x.T * 8 + x.c) * 128u);
      }
    }
    GBAR();
    {
      refresh(x);
      unsigned lb = lb0;
      asm volatile("" : "+s"(lb));
      if (L == 0) WAITV_BAR(3); else WAITV_BAR(0);
      FS(9);
      sig_wait(x, sigv(L, 4));
      FS(18);
      dmaR<316, 348>(x);
      sig_wait(x, sigv(L, 4), M_SIG2);
      dmaR<348, 380>(x);
      FS(19);
      if (L == 0) {
        const unsigned fo = lb + L_STRIDE + O_HB + (unsigned)(x.T * 16 + 2 * x.c) * KB;
        poi4(x.rs, x.lane16, fo); poi4(x.rs, x.lane16, fo + KB);
        poi4(x.rs, x.lane16, lb + L_STRIDE + O_OB + (unsigned)(x.TA * 16 + x.pp) * KB);
        poi4(x.rs, x.lane16, lb + L_STRIDE + O_OB + (unsigned)((x.TA + 1) * 16 + x.pp) * KB);
      }
    }
    GBAR();
    {
      refresh(x);
      if (L == 0) WAITV_BAR(4); else WAITV_BAR(0);
      FS(10);
      dmaRT<380, 400>(x);
      DRAIN_BAR();
      sig_wait(x, sigv(L, 5));
      dmaR<400, 444>(x);
      BAR();
      FS(11);
      dmaR6<444, 556>(x);
    }
    GBAR();
    {
      refresh(x);
      DRAIN_BAR();
      FS(12);
      BAR();
      dmaRT<556, 624>(x);
      DRAIN_BAR();
      FS(13);
      dmaR<624, 712>(x, L == 0);
      BAR();
      dmaR<712, 720>(x, L == 0);
      FS(14);
    }
  }
}

}

__global__ void __launch_bounds__(512, 2) mega_kernel(Params p) {
  extern __shared__ __attribute__((aligned(16))) unsigned char smem_[];
  Ctx x;
  x.sm = (LAS unsigned char *)smem_;
  x.lds0 = (unsigned)(size_t)smem_;
  x.lane = threadIdx.x & 63;
  x.w = __builtin_amdgcn_readfirstlane(threadIdx.x >> 6);
  x.b = blockIdx.x;
  refresh(x);
  const int b = x.b;
  x.spins = 0;
  x.ctl = (gu32 *)p.ws;
  x.rs = __builtin_amdgcn_make_buffer_rsrc((void *)p.ws, 0, WS_BYTES, 0x00020000);
  const bool cw = x.w < 4;

  FSA(0);
  const unsigned tag = __hip_atomic_load(x.ctl + OFF_EPOCH / 4, RLX_AGENT) + 1u;
  v2u xin[4] = {};
  unsigned short wraw[2][4] = {};
  if (cw) {
    const GAS unsigned char *xp = (const GAS unsigned char *)p.x + ((size_t)(16 * x.T + x.tok) * 512 + 64 * x.c + 4 * x.g) * 2;
#pragma unroll
    for (int q = 0; q < 4; ++q) xin[q] = *(const GAS v2u *)(xp + 32 * q);
#pragma unroll
    for (int h = 0; h < 2; ++h) {
      const unsigned short *wp = p.wphi + ((2 * x.pp + h) * 16 + 4 * x.g) * 16 + x.tok;
#pragma unroll
      for (int jj = 0; jj < 4; ++jj) wraw[h][jj] = wp[16 * jj];
    }
  }
  if (threadIdx.x < 9) *(LAS unsigned *)(x.sm + M_SIG + threadIdx.x * 4u) = 0u;
  const int bid = ((x.w + 4) & 7) * 256 + b;
  const unsigned short *src = p.wqkv; int ld = 1536, ra = 0, rb = 0, n0 = 0, kstep = 0, csid = -1; unsigned fr[4] = {0u, 0u, 0u, 0u}, ci[4] = {0u, 0u, 0u, 0u};
  if (bid < 384) {
    kstep = bid & 15; const int nb = bid >> 4; n0 = nb * 64; ra = 32 * kstep; rb = ra + 16;
    const int sec = nb >> 3, h0 = 4 * (nb & 7);
#pragma unroll
    for (int ct = 0; ct < 4; ++ct) {
      const int hh = h0 + ct;
      fr[ct] = W1_F + (unsigned)(((hh >> 1) * 16 + kstep) * 6 + sec * 2 + (hh & 1));
      ci[ct] = (unsigned)((hh >> 1) * 6 + sec * 2 + (hh & 1));
    }
    csid = 0;
  } else if (bid < 768) {
    const int mi = (bid - 384) >> 7, r = (bid - 384) & 127; kstep = r & 15; const int nb = r >> 4;
    src = mi == 0 ? p.wo : (mi == 1 ? p.w1 : p.w2); ld = 512; n0 = nb * 64; ra = 32 * kstep; rb = ra + 16;
#pragma unroll
    for (int ct = 0; ct < 4; ++ct) { fr[ct] = W2_F + (unsigned)(mi * 512 + (nb * 16 + kstep) * 4 + ct); ci[ct] = 96u + (unsigned)(nb * 4 + ct); }
    if (mi == 1) csid = 1;
  } else if (bid < 1216) {
    const int r = bid - 768; kstep = r & 15; const int nb = r >> 4; src = p.w3; ld = 1792; n0 = nb * 64; ra = 32 * kstep; rb = ra + 16;
#pragma unroll
    for (int ct = 0; ct < 4; ++ct) { const int F = nb * 4 + ct; fr[ct] = W5_F + (unsigned)(((F / 7) * 16 + kstep) * 7 + F % 7); ci[ct] = 128u + (unsigned)F; }
    csid = 2;
  } else if (bid < 1664) {
    const int r = bid - 1216; const int nb = r & 7; kstep = r >> 3; src = p.w4; ld = 512; n0 = nb * 64;
    const int u = kstep / 7, q = kstep % 7;
    if (q < 3) { ra = 224 * u + 32 * q; rb = ra + 16; }
    else if (q == 3) { ra = 224 * u + 96; rb = 224 * u + 208; }
    else { ra = 224 * u + 112 + 32 * (q - 4); rb = ra + 16; }
#pragma unroll
    for (int ct = 0; ct < 4; ++ct) fr[ct] = W6_F + (unsigned)((nb * 56 + kstep) * 4 + ct);
  }
  if (bid < 1664) {
    const int ip = x.lane >> 5, uu = (x.lane >> 4) & 1, q = (x.lane >> 2) & 3, c1 = ((x.lane >> 1) & 1) ^ ip, bb = x.lane & 1;
    const int rl = 4 * ip + q, ch = 2 * (2 * uu + c1) + bb;
    const unsigned stg0 = x.lds0 + (unsigned)x.w * 4096u;
#pragma unroll
    for (int u = 0; u < 4; ++u) {
      const int row = (u < 2 ? ra + 8 * u : rb + 8 * (u - 2)) + rl;
      const GAS unsigned char *gp = (const GAS unsigned char *)src + ((size_t)row * ld + n0 + ch * 8) * 2;
      asm volatile("s_mov_b32 m0, %0\n\ts_nop 0\n\tglobal_load_lds_dwordx4 %1, off" ::"s"(stg0 + (unsigned)u * 1024u), "v"(gp) : "memory");
    }
  }
  auto retile = [&]() {
    asm volatile("s_waitcnt vmcnt(0)" ::: "memory");
    const int gq = (x.lane & 15) >> 2, gp_ = x.lane & 3, gi = x.g & 1;
#pragma unroll
    for (int ct = 0; ct < 4; ++ct) {
      const unsigned L = (unsigned)((gi << 5) | ((ct >> 1) << 4) | (gq << 2) | (((ct & 1) ^ gi) << 1) | (gp_ >> 1));
      const unsigned a0 = (unsigned)x.w * 4096u + (unsigned)(x.g >> 1) * 1024u + L * 16u + (unsigned)(gp_ & 1) * 8u;
      const v2u lo = trread(x, a0), hi = trread(x, a0 + 2048u);
      const v4u fragv = {lo.x, lo.y, hi.x, hi.y};
      stg(x.rs, x.lane16, __builtin_amdgcn_readfirstlane(OFF_WT + fr[ct] * KB), fragv);
      if (csid >= 0) {
        const f4 cs = mfma32(fragv, v4u{0x3F803F80u, 0x3F803F80u, 0x3F803F80u, 0x3F803F80u}, f4{0.f, 0.f, 0.f, 0.f});
        const unsigned co = __builtin_amdgcn_readfirstlane(OFF_CSP + (ci[ct] * 16u + (unsigned)kstep) * 64u);
        if (x.tok == 0) stg(x.rs, (unsigned)x.g * 16u, co, __builtin_bit_cast(v4u, cs));
      }
    }
  };
  if (bid < 384) retile();
  FSA(1);
  v4u xres[2];
  v2u wph[2];
  if (cw) {
    xres[0] = clamp4(v4u{xin[0].x, xin[0].y, xin[1].x, xin[1].y});
    xres[1] = clamp4(v4u{xin[2].x, xin[2].y, xin[3].x, xin[3].y});
    const unsigned fo = OFF_L0 + O_XB + (unsigned)(x.T * 16 + 2 * x.c) * KB;
    stg(x.rs, x.lane16, fo, xres[0]); stg(x.rs, x.lane16, fo + KB, xres[1]);
    put_stats(x, xres[0], xres[1], OFF_L0 + O_XS);
#pragma unroll
    for (int h = 0; h < 2; ++h)
      wph[h] = v2u{(unsigned)wraw[h][0] | ((unsigned)wraw[h][1] << 16), (unsigned)wraw[h][2] | ((unsigned)wraw[h][3] << 16)};
  } else {
    xres[0] = xres[1] = v4u{0u, 0u, 0u, 0u};
    wph[0] = wph[1] = v2u{0u, 0u};
  }
  FSA(2);
  asm volatile("s_waitcnt vmcnt(0) lgkmcnt(0)\n\ts_barrier" ::: "memory");
  FSA(3);
  if (threadIdx.x == 0) __hip_atomic_store(x.ctl + OFF_FLAGS / 4 + b * 16, tag, RLX_AGENT);
  if (bid >= 384 && bid < 1664) retile();
  if (!cw) {
    poi4(x.rs, x.lane16, OFF_L0 + O_KVP + (unsigned)((x.pp * 16 + x.tau) * 4 + x.m) * KB);
    if (x.tok == 0) poi4(x.rs, (unsigned)x.g * 16u, OFF_L0 + O_KVZ + (unsigned)((x.pp * 4 + x.m) * 16 + x.tau) * 64u);
    poi4(x.rs, x.lane16, OFF_L0 + O_OB + (unsigned)(x.TA * 16 + x.pp) * KB);
    poi4(x.rs, x.lane16, OFF_L0 + O_OB + (unsigned)((x.TA + 1) * 16 + x.pp) * KB);
    poi4(x.rs, x.lane16, OFF_L0 + O_HB + (unsigned)(x.T * 16 + 2 * x.c) * KB);
    poi4(x.rs, x.lane16, OFF_L0 + O_HB + (unsigned)(x.T * 16 + 2 * x.c + 1) * KB);
    poi4(x.rs, x.lane16, OFF_L0 + L_STRIDE + O_KVP + (unsigned)((x.pp * 16 + x.tau) * 4 + x.m) * KB);
    if (x.tok == 0) poi4(x.rs, (unsigned)x.g * 16u, OFF_L0 + L_STRIDE + O_KVZ + (unsigned)((x.pp * 4 + x.m) * 16 + x.tau) * 64u);
  }
  if (x.w == 4) {
    poll_flags(x, OFF_FLAGS, tag);
    if (b == 0 && x.lane == 0) __hip_atomic_store(x.ctl + OFF_EPOCH / 4, tag, RLX_AGENT);
  }
  BAR();
  FSA(4);
  if (cw) {
    compute_path(x, p, xres, wph, tag);
  } else {
    v4u cv[2];
#pragma unroll
    for (int jj = 0; jj < 2; ++jj) LDA(cv[jj], x.lane16, OFF_CSP + (unsigned)(x.pp * 6 + min(x.m + 4 * jj, 5)) * KB);
    dmaR<POS1, POS2>(x);
#pragma unroll
    for (int jj = 0; jj < 2; ++jj) {
      WAITV1(24, cv[jj]);
      const f4 s = shsum(__builtin_bit_cast(f4, cv[jj]));
      const int u = x.m + 4 * jj;
      if (u < 6 && x.lane < 4) *(LAS f4 *)(x.sm + M_CS + (unsigned)u * 64u + (unsigned)x.lane * 16u) = s;
    }
    helper_path(x, tag);
  }
  asm volatile("s_waitcnt vmcnt(0)" ::: "memory");
}

extern "C" void kernel_launch(void *const *d_in, const int *in_sizes, int n_in, void *d_out, int out_size, void *d_ws, size_t ws_size,
                              hipStream_t stream) {
  Params p;
  p.x = (const unsigned short *)d_in[0];
  p.wqkv = (const unsigned short *)d_in[1];
  p.wphi = (const unsigned short *)d_in[2];
  p.wo = (const unsigned short *)d_in[3];
  p.w1 = (const unsigned short *)d_in[5];
  p.w2 = (const unsigned short *)d_in[6];
  p.w3 = (const unsigned short *)d_in[7];
  p.w4 = (const unsigned short *)d_in[8];
  p.out = (unsigned short *)d_out;
  p.ws = (unsigned char *)d_ws;
  (void)hipFuncSetAttribute((const void *)mega_kernel, hipFuncAttributeMaxDynamicSharedMemorySize, LDS_BYTES);
  hipLaunchKernelGGL(mega_kernel, dim3(256), dim3(512), LDS_BYTES, stream, p);
}
```

```cpp
#include <hip/hip_runtime.h>
#include <stdint.h>

#define GAS __attribute__((address_space(1)))
#define LAS __attribute__((address_space(3)))
#define DI __device__ __forceinline__
typedef unsigned v4u __attribute__((ext_vector_type(4)));
typedef unsigned v2u __attribute__((ext_vector_type(2)));
typedef float f4 __attribute__((ext_vector_type(4)));
typedef float f2 __attribute__((ext_vector_type(2)));
typedef short s8 __attribute__((ext_vector_type(8)));
typedef short s4 __attribute__((ext_vector_type(4)));
typedef __bf16 bf2 __attribute__((ext_vector_type(2)));
typedef GAS unsigned gu32;
typedef __amdgpu_buffer_rsrc_t rsrc_t;
#define RLX_AGENT __ATOMIC_RELAXED, __HIP_MEMORY_SCOPE_AGENT

#define DBG_GBAR 0
#define DBG_NOPOISON 0
#define DBG_STAMP 0
#define SPEC 0
#define PREFETCH 1
#define WARM 0

namespace {
constexpr unsigned MB = 1u << 20, KB = 1024u;
constexpr unsigned OFF_EPOCH = 0, OFF_ABORT = 256, OFF_GBAR = 512, OFF_FLAGS = 16384, OFF_FLAGS2 = 32768;
constexpr unsigned OFF_WT = 1 * MB;
constexpr unsigned W1_F = 0, W2_F = 1536, W5_F = 3072, W6_F = 4864;
constexpr unsigned OFF_CSP = 8 * MB;
constexpr unsigned OFF_L0 = 9 * MB, L_STRIDE = 24 * MB;
constexpr unsigned O_XB = 0, O_XS = 2 * MB, O_KVP = 2 * MB + 512 * KB, O_KVZ = 3 * MB + 512 * KB, O_OB = 4 * MB, O_X1B = 6 * MB,
                   O_X1S = 8 * MB, O_HB = 9 * MB, O_X2B = 11 * MB, O_X2S = 13 * MB, O_H3B = 14 * MB;
constexpr unsigned WS_BYTES = OFF_L0 + 2 * L_STRIDE;
constexpr unsigned OOB = 0x7F000000u;
constexpr int RING = 156;
constexpr unsigned MISC = RING * KB;
constexpr unsigned M_CS = MISC, M_KVA = MISC + 1088, M_ZA = MISC + 3136, M_SIG = MISC + 3264, M_SIG2 = MISC + 3280, M_RDY = MISC + 3296, M_FLAG = MISC + 3300;
constexpr unsigned IMG = 0;
constexpr unsigned LDS_BYTES = 163840;
constexpr int POS1 = 0, POS2 = 96, POS3 = 160, POS4 = 224, POS5 = 288, POS6 = 400, POSE = 624;
constexpr unsigned SENT = 0xFFFFFFFFu;
constexpr unsigned SPIN_CAP = 1u << 13;
constexpr int AUX_SC1 = 16;
#define DLY_KV 16
#define DLY_1 16
#define DLY_2 16
#define DLY_3 16
#define DLY_4 16
#define DLY_5 16
#define DLY_6 16

struct Params {
  const unsigned short *x, *wqkv, *wphi, *wo, *w1, *w2, *w3, *w4;
  unsigned short *out;
  unsigned char *ws;
};

DI unsigned pk2(float a, float b) { f2 v = {a, b}; bf2 r = __builtin_convertvector(v, bf2); return __builtin_bit_cast(unsigned, r); }
DI float asf(unsigned u) { return __builtin_bit_cast(float, u); }
DI float blo(unsigned u) { return __builtin_bit_cast(float, u << 16); }
DI float bhi(unsigned u) { return __builtin_bit_cast(float, u & 0xFFFF0000u); }
DI unsigned clampw(unsigned u) { return u < 0xFFFFFFFEu ? u : 0xFFFFFFFEu; }
DI unsigned clampf(float f) { return clampw(__builtin_bit_cast(unsigned, f)); }
DI v4u clamp4(v4u v) { return v4u{clampw(v.x), clampw(v.y), clampw(v.z), clampw(v.w)}; }
DI v4u pack8(f4 a, f4 b) { return clamp4(v4u{pk2(a.x, a.y), pk2(a.z, a.w), pk2(b.x, b.y), pk2(b.z, b.w)}); }
DI f4 ulo(v4u v) { return f4{blo(v.x), bhi(v.x), blo(v.y), bhi(v.y)}; }
DI f4 uhi(v4u v) { return f4{blo(v.z), bhi(v.z), blo(v.w), bhi(v.w)}; }
DI f4 mfma32(v4u a, v4u b, f4 c) {
  return __builtin_amdgcn_mfma_f32_16x16x32_bf16(__builtin_bit_cast(s8, a), __builtin_bit_cast(s8, b), c, 0, 0, 0);
}
DI f4 mfma16(v2u a, v2u b, f4 c) {
  return __builtin_amdgcn_mfma_f32_16x16x16bf16_1k(__builtin_bit_cast(s4, a), __builtin_bit_cast(s4, b), c, 0, 0, 0);
}
DI v4u ldg(rsrc_t rs, unsigned voff, unsigned soff) { return __builtin_amdgcn_raw_buffer_load_b128(rs, voff, soff, AUX_SC1); }
DI void stg(rsrc_t rs, unsigned voff, unsigned soff, v4u v) {
  __builtin_amdgcn_raw_buffer_store_b128(v, rs, voff, soff, AUX_SC1);
  asm volatile("s_nop 1" ::"v"(v));
}
DI void stg2(rsrc_t rs, unsigned voff, unsigned soff, v2u v) { __builtin_amdgcn_raw_buffer_store_b64(v, rs, voff, soff, AUX_SC1); }
DI void poi4(rsrc_t rs, unsigned voff, unsigned soff) { if (!DBG_NOPOISON) stg(rs, voff, soff, v4u{0xFFFFFFFFu, 0xFFFFFFFFu, 0xFFFFFFFFu, 0xFFFFFFFFu}); }
DI void poi2(rsrc_t rs, unsigned voff, unsigned soff) { if (!DBG_NOPOISON) stg2(rs, voff, soff, v2u{0xFFFFFFFFu, 0xFFFFFFFFu}); }
DI unsigned max4(v4u v) { return max(max(v.x, v.y), max(v.z, v.w)); }
DI bool anybad(unsigned mx) { return __any(mx == SENT) != 0; }
DI float xsum(float x) {
  unsigned u = __builtin_bit_cast(unsigned, x);
  auto r = __builtin_amdgcn_permlane16_swap(u, u, false, false);
  x = __builtin_bit_cast(float, (unsigned)r[0]) + __builtin_bit_cast(float, (unsigned)r[1]);
  u = __builtin_bit_cast(unsigned, x);
  auto q = __builtin_amdgcn_permlane32_swap(u, u, false, false);
  return __builtin_bit_cast(float, (unsigned)q[0]) + __builtin_bit_cast(float, (unsigned)q[1]);
}
DI float xmax(float x) {
  unsigned u = __builtin_bit_cast(unsigned, x);
  auto r = __builtin_amdgcn_permlane16_swap(u, u, false, false);
  x = fmaxf(__builtin_bit_cast(float, (unsigned)r[0]), __builtin_bit_cast(float, (unsigned)r[1]));
  u = __builtin_bit_cast(unsigned, x);
  auto q = __builtin_amdgcn_permlane32_swap(u, u, false, false);
  return fmaxf(__builtin_bit_cast(float, (unsigned)q[0]), __builtin_bit_cast(float, (unsigned)q[1]));
}
template <bool MAX> DI float red_op(float a, float b) { return MAX ? fmaxf(a, b) : a + b; }
DI unsigned fbits(float f) { return __builtin_bit_cast(unsigned, f); }
template <bool MAX> DI void xred4(float &a, float &b, float &c, float &d) {
  const auto r1 = __builtin_amdgcn_permlane16_swap(fbits(a), fbits(b), false, false);
  const auto r2 = __builtin_amdgcn_permlane16_swap(fbits(c), fbits(d), false, false);
  const float c1 = red_op<MAX>(asf(r1[0]), asf(r1[1])), c2 = red_op<MAX>(asf(r2[0]), asf(r2[1]));
  const auto r3 = __builtin_amdgcn_permlane32_swap(fbits(c1), fbits(c2), false, false);
  const unsigned e = fbits(red_op<MAX>(asf(r3[0]), asf(r3[1])));
  const auto r4 = __builtin_amdgcn_permlane32_swap(e, e, false, false);
  const unsigned f0 = r4[0], f1 = r4[1];
  const auto r5 = __builtin_amdgcn_permlane16_swap(f0, f0, false, false);
  const auto r6 = __builtin_amdgcn_permlane16_swap(f1, f1, false, false);
  a = asf(r5[0]); b = asf(r5[1]); c = asf(r6[0]); d = asf(r6[1]);
}
template <bool MAX> DI void xred2(float &a, float &b) {
  const auto r1 = __builtin_amdgcn_permlane16_swap(fbits(a), fbits(b), false, false);
  const unsigned c = fbits(red_op<MAX>(asf(r1[0]), asf(r1[1])));
  const auto r3 = __builtin_amdgcn_permlane32_swap(c, c, false, false);
  const unsigned e = fbits(red_op<MAX>(asf(r3[0]), asf(r3[1])));
  const auto r5 = __builtin_amdgcn_permlane16_swap(e, e, false, false);
  a = asf(r5[0]); b = asf(r5[1]);
}
DI f2 gelu2(f2 x) {
  const f2 c1 = {-2.302208198f, -2.302208198f}, c2 = {-0.1029432397f, -0.1029432397f}, one = {1.0f, 1.0f};
  const f2 t = x * __builtin_elementwise_fma(x * x, c2, c1);
  const f2 d = one + f2{__builtin_amdgcn_exp2f(t.x), __builtin_amdgcn_exp2f(t.y)};
  return x * f2{__builtin_amdgcn_rcpf(d.x), __builtin_amdgcn_rcpf(d.y)};
}
DI f4 gelu4(f4 y) {
  const f2 a = gelu2(f2{y.x, y.y}), b = gelu2(f2{y.z, y.w});
  return f4{a.x, a.y, b.x, b.y};
}

#define FSL 1
#define FS(i) do {} while (0)
#define FSA(i) do { unsigned long long t_; asm volatile("s_memtime %0\n\ts_waitcnt lgkmcnt(0)" : "=s"(t_) :: "memory"); \
  int ww_ = x.w; asm volatile("" : "+s"(ww_)); \
  unsigned a_ = (ww_ == 0) ? (70 + (i)) * 4u : ((ww_ == 4) ? 400u + (70 + (i)) * 4u : 780u); \
  asm volatile("" : "+s"(a_)); \
  *(LAS unsigned *)(x.sm + M_FLAG + a_) = (unsigned)t_; } while (0)
#define FSP(i) do {} while (0)
#define SB() __builtin_amdgcn_sched_barrier(0)
#define MARK(s) asm volatile("; MARK " s)
#define BAR() asm volatile("s_waitcnt lgkmcnt(0)\n\ts_barrier" ::: "memory")
#define WAITV_BAR(n) asm volatile("s_waitcnt vmcnt(" #n ") lgkmcnt(0)\n\ts_barrier" ::: "memory")
#define DRAIN_BAR() asm volatile("s_waitcnt vmcnt(0) lgkmcnt(0)\n\ts_barrier" ::: "memory")
DI void sleepn(int n) {
  for (int i = 0; i < n; ++i) __builtin_amdgcn_s_sleep(1);
}

struct Ctx {
  rsrc_t rs;
  LAS unsigned char *sm;
  unsigned lds0;
  unsigned lane16;
  LAS unsigned char *ab[3];
  int lane, w, m, g, tok;
  int t, c, tau, pp, b;
  int T, TA;
  unsigned spins;
  gu32 *ctl;
};
DI void refresh(Ctx &x) {
  asm volatile("" : "+s"(x.w), "+s"(x.b), "+v"(x.lane));
  x.lane16 = (unsigned)x.lane * 16u;
  x.g = x.lane >> 4; x.tok = x.lane & 15;
  x.m = x.w & 3;
  const int xc = x.b & 7, j = x.b >> 3;
  x.t = 4 * xc + (j >> 3); x.c = j & 7;
  x.tau = 2 * xc + (j >> 4); x.pp = j & 15;
  x.T = 4 * x.t + x.m;
  x.TA = 8 * x.tau + 2 * x.m;
  x.ab[0] = x.sm + x.lane16;
  unsigned o1 = x.lane16 + 60u * KB, o2 = x.lane16 + 120u * KB;
  asm volatile("" : "+v"(o1), "+v"(o2));
  x.ab[1] = x.sm + o1; x.ab[2] = x.sm + o2;
}

template <int P> DI void dmaP(const Ctx &x, bool live = true) {
  constexpr int Q = P % POSE;
  int w = x.w, b = x.b;
  asm volatile("" : "+s"(w), "+s"(b));
  const int m = w & 3, j = b >> 3;
  unsigned slot = (unsigned)(P % RING) + (unsigned)m;
  unsigned fr;
  if constexpr (Q < POS2) fr = W1_F + (unsigned)((j & 15) * 96 + Q + m);
  else if constexpr (Q < POS5) fr = W2_F + (unsigned)(((Q - POS2) / 64) * 512 + (j & 7) * 64 + (Q - POS2) % 64 + m);
  else if constexpr (Q < POS6) fr = W5_F + (unsigned)((j & 15) * 112 + (Q - POS5) + m);
  else fr = W6_F + (unsigned)((j & 7) * 224 + (Q - POS6) + m);
  const unsigned lds = x.lds0 + slot * KB, so = live ? OFF_WT + fr * KB : OOB;
  asm volatile("s_mov_b32 m0, %0\n\ts_nop 0\n\tbuffer_load_dwordx4 %1, %2, %3 offen sc1 lds" ::"s"(lds), "v"(x.lane16), "s"(x.rs), "s"(so) : "memory");
}
template <int LO, int HI> DI void dmaR(const Ctx &x, bool live = true) {
  if constexpr (LO < HI) { dmaP<LO>(x, live); dmaR<LO + 4, HI>(x, live); }
}
template <int LO, int HI> DI void dmaRT(const Ctx &x, bool live = true) {
  if constexpr (LO < HI) { dmaP<LO>(x, live); asm volatile("s_waitcnt vmcnt(3)" ::: "memory"); dmaRT<LO + 4, HI>(x, live); }
}
template <int LO, int HI> DI void dmaR6(const Ctx &x, bool live = true) {
  if constexpr (LO < HI) { dmaP<LO>(x, live); asm volatile("s_waitcnt vmcnt(5)" ::: "memory"); dmaR6<LO + 4, HI>(x, live); }
}
DI void sig_set(const Ctx &x, unsigned v, unsigned word = M_SIG) { *(volatile LAS unsigned *)(x.sm + word + (unsigned)x.m * 4u) = v; }
DI void sig_wait(const Ctx &x, unsigned v, unsigned word = M_SIG) {
  for (unsigned sp = 0; sp < (1u << 20); ++sp) {
    const v4u s = *(volatile LAS v4u *)(x.sm + word);
    if (__builtin_amdgcn_readfirstlane(min(min(s.x, s.y), min(s.z, s.w))) >= v) break;
    __builtin_amdgcn_s_sleep(1);
  }
}

#define LDA(dst, voff, soff) \
  asm volatile("buffer_load_dwordx4 %0, %1, %2, %3 offen sc1" : "=v"(dst) : "v"(voff), "s"(x.rs), "s"(soff) : "memory")
#define LDA2(dst, voff, soff, imm) \
  asm volatile("buffer_load_dwordx2 %0, %1, %2, %3 offen offset:%4 sc1" : "=v"(dst) : "v"(voff), "s"(x.rs), "s"(soff), "i"(imm) : "memory")
#define WAITV1(n, r) asm volatile("s_waitcnt vmcnt(%1)" : "+v"(r) : "i"(n) : "memory")
#define WAITV2(n, r, q) asm volatile("s_waitcnt vmcnt(%2)" : "+v"(r), "+v"(q) : "i"(n) : "memory")
#define TIE(r) asm volatile("" : "+v"(r) : : "memory")
DI bool giveup(Ctx &x) {
  if (++x.spins > SPIN_CAP) {
    if (x.lane == 0) __hip_atomic_store(x.ctl + OFF_ABORT / 4, 1u, RLX_AGENT);
    return true;
  }
  __builtin_amdgcn_s_sleep(1);
  return false;
}
DI v4u load_sure(Ctx &x, unsigned voff, unsigned soff) {
  v4u v;
  while (true) {
    asm volatile("buffer_load_dwordx4 %0, %1, %2, %3 offen sc1\n\ts_waitcnt vmcnt(0)" : "=&v"(v) : "v"(voff), "s"(x.rs), "s"(soff) : "memory");
    if (!anybad(max4(v)) || giveup(x)) break;
  }
  return v;
}
DI v2u load_sure2(Ctx &x, unsigned voff, unsigned soff) {
  v2u v;
  while (true) {
    asm volatile("buffer_load_dwordx2 %0, %1, %2, %3 offen sc1\n\ts_waitcnt vmcnt(0)" : "=&v"(v) : "v"(voff), "s"(x.rs), "s"(soff) : "memory");
    if (!anybad(max(v.x, v.y)) || giveup(x)) break;
  }
  return v;
}

DI void warm(Ctx &x, unsigned voff, unsigned soff, unsigned soff2) {
}
#define LDC(dst, voff, soff) \
  asm volatile("buffer_load_dword %0, %1, %2, %3 offen sc1" : "=v"(dst) : "v"(voff), "s"(x.rs), "s"(soff) : "memory")

#define GBAR() do {} while (0)
#define SLEEP(n) sleepn(n)

DI void put_stats(const Ctx &x, v4u u, v4u v, unsigned sbuf) {
  const f4 a = ulo(u), b = uhi(u), c = ulo(v), d = uhi(v);
  const f4 s = (a + b) + (c + d), q = (a * a + b * b) + (c * c + d * d);
  float s1 = (s.x + s.y) + (s.z + s.w), s2 = (q.x + q.y) + (q.z + q.w);
  xred2<false>(s1, s2);
  if (x.g == 0) stg2(x.rs, (unsigned)x.tok * 8u, sbuf + (unsigned)(x.T * 8 + x.c) * 128u, v2u{clampf(s1), clampf(s2)});
}
struct Stats { v2u v[2]; };
DI void stats_load(const Ctx &x, Stats &s, unsigned soffT) {
  const unsigned vo = (unsigned)(x.g * 256 + x.tok * 8);
  LDA2(s.v[0], vo, soffT, 0); LDA2(s.v[1], vo, soffT, 128);
}
DI void stats_fin(Ctx &x, Stats &s, unsigned soffT, float &mean, float &var) {
  TIE(s.v[0]); TIE(s.v[1]);
  if (__builtin_expect(anybad(max(max(s.v[0].x, s.v[0].y), max(s.v[1].x, s.v[1].y))), 0)) {
    s.v[0] = load_sure2(x, (unsigned)(x.g * 256 + x.tok * 8), soffT);
    s.v[1] = load_sure2(x, (unsigned)(x.g * 256 + x.tok * 8 + 128), soffT);
  }
  float s1 = asf(s.v[0].x) + asf(s.v[1].x), s2 = asf(s.v[0].y) + asf(s.v[1].y);
  xred2<false>(s1, s2);
  mean = s1 * (1.0f / 512.0f);
  var = fmaxf(__builtin_fmaf(-mean, mean, s2 * (1.0f / 512.0f)), 0.0f);
}
DI const LAS v4u *lds4(const Ctx &x, unsigned off) { return (const LAS v4u *)(x.sm + off); }
template <int NT, int P> DI void ldaN(const Ctx &x, v4u (&a)[NT]) {
#pragma unroll
  for (int f = 0; f < NT; ++f) {
    const int slot = (P + f) % RING;
    a[f] = *(const LAS v4u *)(x.ab[slot / 60] + (slot % 60) * KB);
  }
}
template <int NT> DI void tieN(v4u (&a)[NT]) {
  if constexpr (NT == 4) asm volatile("" : "+v"(a[0]), "+v"(a[1]), "+v"(a[2]), "+v"(a[3]));
  else if constexpr (NT == 6) asm volatile("" : "+v"(a[0]), "+v"(a[1]), "+v"(a[2]), "+v"(a[3]), "+v"(a[4]), "+v"(a[5]));
  else asm volatile("" : "+v"(a[0]), "+v"(a[1]), "+v"(a[2]), "+v"(a[3]), "+v"(a[4]), "+v"(a[5]), "+v"(a[6]));
}
DI unsigned mx4(unsigned m, v4u v) {
  unsigned r;
  asm("v_max3_u32 %0, %1, %2, %3" : "=v"(r) : "v"(m), "v"(v.x), "v"(v.y));
  asm("v_max3_u32 %0, %1, %2, %3" : "=v"(m) : "v"(r), "v"(v.z), "v"(v.w));
  return m;
}
DI f4 ldcs(const Ctx &x, int tile) { return *(const LAS f4 *)(x.sm + M_CS + (unsigned)tile * 64u + (unsigned)x.g * 16u); }
constexpr int imin(int a, int b) { return a < b ? a : b; }

template <bool SH> DI unsigned vof(const Ctx &x, int k, const unsigned (&vo)[4]) { return SH ? vo[k >> 2] : x.lane16; }
template <int NK> constexpr int depth1() { return 8; }
template <int POS, int NK, bool SH, int K0, int K1, int I> struct KL1 {
  static DI void run(Ctx &x, v4u (&bf)[NK], f4 (&acc)[4], unsigned &mx, v4u (&cur)[4], v4u (&nxt)[4], unsigned so, const unsigned (&vo)[4],
                     unsigned sig) {
    v4u nn[4];
    constexpr int DP = depth1<NK>();
    WAITV1(imin(DP - 1, NK - 1 - I), bf[I]);
    if constexpr (I + DP < NK) LDA(bf[I + DP], vof<SH>(x, I + DP, vo), so + (unsigned)(I + DP) * KB);
    if constexpr (I + DP == NK - 1) { if (sig) sig_set(x, sig); }
    tieN<4>(cur);
    SB();
    if constexpr (I + 2 < K1) ldaN<4, POS + 4 * (I + 2)>(x, nn);
    mx = mx4(mx, bf[I]);
#pragma unroll
    for (int f = 0; f < 4; ++f) acc[f] = mfma32(cur[f], bf[I], acc[f]);
#pragma unroll
    for (int f = 0; f < 4; ++f) {
      __builtin_amdgcn_sched_group_barrier(0x008, 1, 0);
      __builtin_amdgcn_sched_group_barrier(0x100, 1, 0);
      __builtin_amdgcn_sched_group_barrier(0x002, 1, 0);
    }
    SB();
    if constexpr (I + 1 < K1) KL1<POS, NK, SH, K0, K1, I + 1>::run(x, bf, acc, mx, nxt, nn, so, vo, sig);
  }
};
template <int POS, bool SH, int K1, int I> struct KL1R {
  static DI void run(Ctx &x, f4 (&acc)[4], unsigned so, const unsigned (&vo)[4]) {
    const v4u bv = load_sure(x, vof<SH>(x, I, vo), so + (unsigned)I * KB);
    v4u a[4];
    ldaN<4, POS + 4 * I>(x, a);
#pragma unroll
    for (int f = 0; f < 4; ++f) acc[f] = mfma32(a[f], bv, acc[f]);
    if constexpr (I + 1 < K1) KL1R<POS, SH, K1, I + 1>::run(x, acc, so, vo);
  }
};
template <bool ST> DI unsigned canary_poll1(Ctx &x, unsigned so, unsigned cvo, Stats &st, unsigned sst) {
  unsigned cn;
  LDC(cn, cvo, so);
  if constexpr (ST) stats_load(x, st, sst);
  WAITV1(0, cn);
  if constexpr (ST) { TIE(st.v[0]); TIE(st.v[1]); cn = max(cn, max(max(st.v[0].x, st.v[0].y), max(st.v[1].x, st.v[1].y))); }
  return cn;
}
template <int NK, bool SH, bool ST>
DI void pro1(Ctx &x, v4u (&bf)[NK], unsigned so, const unsigned (&vo)[4], unsigned cvo, Stats &st, unsigned sst, unsigned &cn) {
  while (anybad(canary_poll1<ST>(x, so, cvo, st, sst)) && !giveup(x)) {}
  cn = 0;
#pragma unroll
  for (int i = 0; i < depth1<NK>(); ++i) LDA(bf[i], vof<SH>(x, i, vo), so + (unsigned)i * KB);
}
template <int NK, bool SH, bool ST>
DI void chk1(Ctx &x, v4u (&bf)[NK], unsigned so, const unsigned (&vo)[4], unsigned cvo, Stats &st, unsigned sst, unsigned cn) {
}
template <int POS, int NK, bool SH, int K0, int K1>
DI void kloop1(Ctx &x, v4u (&bf)[NK], f4 (&acc)[4], unsigned so, const unsigned (&vo)[4], unsigned sig) {
  f4 save[4];
#pragma unroll
  for (int f = 0; f < 4; ++f) save[f] = acc[f];
  unsigned mx = 0;
  v4u a0[4], a1[4];
  ldaN<4, POS + 4 * K0>(x, a0);
  ldaN<4, POS + 4 * (K0 + 1)>(x, a1);
  KL1<POS, NK, SH, K0, K1, K0>::run(x, bf, acc, mx, a0, a1, so, vo, sig);
  if (__builtin_expect(anybad(mx), 0)) {
#pragma unroll
    for (int f = 0; f < 4; ++f) acc[f] = save[f];
    KL1R<POS, SH, K1, K0>::run(x, acc, so, vo);
  }
}
template <int NT, int POS, int K0, int K1, int I> struct KL2 {
  static DI void run(Ctx &x, v4u (&bf)[16][2], f4 (&acc)[NT][2], unsigned &mx, v4u (&cur)[NT], unsigned so, unsigned sig) {
    v4u nxt[NT];
    WAITV2(2 * imin(3, 15 - I), bf[I][0], bf[I][1]);
    if constexpr (I + 4 < 16) {
      LDA(bf[I + 4][0], x.lane16, so + (unsigned)(I + 4) * KB); LDA(bf[I + 4][1], x.lane16, so + (unsigned)(16 + I + 4) * KB);
    }
    if constexpr (I == (NT == 6 ? 8 : 11)) sig_set(x, sig);
    tieN<NT>(cur);
    SB();
    if constexpr (I + 1 < K1) ldaN<NT, POS + (I + 1) * NT>(x, nxt);
    mx = mx4(mx4(mx, bf[I][0]), bf[I][1]);
#pragma unroll
    for (int f = 0; f < NT; ++f) {
      acc[f][0] = mfma32(cur[f], bf[I][0], acc[f][0]);
      acc[f][1] = mfma32(cur[f], bf[I][1], acc[f][1]);
    }
#pragma unroll
    for (int f = 0; f < NT; ++f) {
      __builtin_amdgcn_sched_group_barrier(0x008, 1, 0);
      __builtin_amdgcn_sched_group_barrier(0x100, 1, 0);
    }
#pragma unroll
    for (int f = 0; f < NT; ++f) {
      __builtin_amdgcn_sched_group_barrier(0x008, 1, 0);
      __builtin_amdgcn_sched_group_barrier(0x002, 1, 0);
    }
    SB();
    if constexpr (I + 1 < K1) KL2<NT, POS, K0, K1, I + 1>::run(x, bf, acc, mx, nxt, so, sig);
  }
};
template <int NT, int POS, int K1, int I> struct KL2R {
  static DI void run(Ctx &x, f4 (&acc)[NT][2], unsigned so) {
    const v4u b0 = load_sure(x, x.lane16, so + (unsigned)I * KB), b1 = load_sure(x, x.lane16, so + (unsigned)(16 + I) * KB);
    v4u a[NT];
    ldaN<NT, POS + I * NT>(x, a);
#pragma unroll
    for (int f = 0; f < NT; ++f) {
      acc[f][0] = mfma32(a[f], b0, acc[f][0]);
      acc[f][1] = mfma32(a[f], b1, acc[f][1]);
    }
    if constexpr (I + 1 < K1) KL2R<NT, POS, K1, I + 1>::run(x, acc, so);
  }
};
DI unsigned stmax(const Stats (&st)[2]) {
  return max(max(max(st[0].v[0].x, st[0].v[0].y), max(st[0].v[1].x, st[0].v[1].y)), max(max(st[1].v[0].x, st[1].v[0].y), max(st[1].v[1].x, st[1].v[1].y)));
}
DI void poll2(Ctx &x, Stats (&st)[2], unsigned so, unsigned sst) {
  const unsigned cvo = (unsigned)(x.lane & 31) * KB + 1020u;
  while (true) {
    unsigned cn;
    LDC(cn, cvo, so);
    stats_load(x, st[0], sst); stats_load(x, st[1], sst + 1024u);
    WAITV1(0, cn);
    TIE(st[0].v[0]); TIE(st[0].v[1]); TIE(st[1].v[0]); TIE(st[1].v[1]);
    if (!anybad(max(cn, stmax(st))) || giveup(x)) break;
  }
}
DI void first2(Ctx &x, v4u (&bf)[16][2], unsigned so) {
#pragma unroll
  for (int i = 0; i < 4; ++i) { LDA(bf[i][0], x.lane16, so + (unsigned)i * KB); LDA(bf[i][1], x.lane16, so + (unsigned)(16 + i) * KB); }
}
DI void pro2(Ctx &x, v4u (&bf)[16][2], Stats (&st)[2], unsigned so, unsigned sst, unsigned &cn) {
  poll2(x, st, so, sst);
  cn = 0;
  first2(x, bf, so);
}
DI void chk2(Ctx &x, v4u (&bf)[16][2], Stats (&st)[2], unsigned so, unsigned sst, unsigned cn) {
}
template <int NT, int POS, int K0, int K1>
DI void kloop2(Ctx &x, v4u (&bf)[16][2], f4 (&acc)[NT][2], unsigned so, unsigned sig, unsigned &mx) {
  v4u a0[NT];
  ldaN<NT, POS + K0 * NT>(x, a0);
  KL2<NT, POS, K0, K1, K0>::run(x, bf, acc, mx, a0, so, sig);
}
template <int NT, int POS> DI void redo2(Ctx &x, f4 (&acc)[NT][2], unsigned so) {
#pragma unroll
  for (int f = 0; f < NT; ++f) { acc[f][0] = f4{0.f, 0.f, 0.f, 0.f}; acc[f][1] = f4{0.f, 0.f, 0.f, 0.f}; }
  KL2R<NT, POS, 16, 0>::run(x, acc, so);
}

DI void featmap4(const f4 (&v)[4], const v2u (&wphi)[2], v2u (&plus)[4], v2u (&minus)[4]) {
  const float L2E = 1.4426950408889634f;
  f4 p[4];
#pragma unroll
  for (int i = 0; i < 4; ++i) p[i] = mfma16(wphi[i & 1], v2u{pk2(v[i].x, v[i].y), pk2(v[i].z, v[i].w)}, f4{0.f, 0.f, 0.f, 0.f});
  float mx[4], nm[4];
#pragma unroll
  for (int i = 0; i < 4; ++i) {
    p[i] *= L2E;
    mx[i] = fmaxf(fmaxf(p[i].x, p[i].y), fmaxf(p[i].z, p[i].w));
    nm[i] = -fminf(fminf(p[i].x, p[i].y), fminf(p[i].z, p[i].w));
  }
  xred4<true>(mx[0], mx[1], mx[2], mx[3]);
  xred4<true>(nm[0], nm[1], nm[2], nm[3]);
  f4 e[4], n[4];
  float se[4], sn[4];
#pragma unroll
  for (int i = 0; i < 4; ++i) {
    const f4 a = p[i] - mx[i], b = -p[i] - nm[i];
    e[i] = f4{__builtin_amdgcn_exp2f(a.x), __builtin_amdgcn_exp2f(a.y), __builtin_amdgcn_exp2f(a.z), __builtin_amdgcn_exp2f(a.w)};
    n[i] = f4{__builtin_amdgcn_exp2f(b.x), __builtin_amdgcn_exp2f(b.y), __builtin_amdgcn_exp2f(b.z), __builtin_amdgcn_exp2f(b.w)};
    se[i] = (e[i].x + e[i].y) + (e[i].z + e[i].w);
    sn[i] = (n[i].x + n[i].y) + (n[i].z + n[i].w);
  }
  xred4<false>(se[0], se[1], se[2], se[3]);
  xred4<false>(sn[0], sn[1], sn[2], sn[3]);
#pragma unroll
  for (int i = 0; i < 4; ++i) {
    const f4 a = e[i] * __builtin_amdgcn_rcpf(se[i]), b = n[i] * __builtin_amdgcn_rcpf(sn[i]);
    plus[i] = v2u{pk2(a.x, a.y), pk2(a.z, a.w)};
    minus[i] = v2u{pk2(b.x, b.y), pk2(b.z, b.w)};
  }
}
DI v2u trread(const Ctx &x, unsigned off) {
  s4 r = __builtin_amdgcn_ds_read_tr16_b64_v4i16((LAS s4 *)(x.sm + off));
  return __builtin_bit_cast(v2u, r);
}
DI f4 shsum(f4 s) {
#pragma unroll
  for (int o = 4; o < 64; o <<= 1) {
    s.x += __shfl_xor(s.x, o); s.y += __shfl_xor(s.y, o); s.z += __shfl_xor(s.z, o); s.w += __shfl_xor(s.w, o);
  }
  return s;
}

DI void poll_flags(Ctx &x, unsigned off, unsigned tag) {
  for (unsigned sp = 0;; ++sp) {
    unsigned f0, f1, f2, f3;
    const unsigned vo = (unsigned)x.lane * 64u;
    LDC(f0, vo, off); LDC(f1, vo, off + 4096u); LDC(f2, vo, off + 8192u); LDC(f3, vo, off + 12288u);
    asm volatile("s_waitcnt vmcnt(0)" : "+v"(f0), "+v"(f1), "+v"(f2), "+v"(f3)::"memory");
    if (__all(f0 == tag && f1 == tag && f2 == tag && f3 == tag)) break;
    if (sp > SPIN_CAP) {
      if (x.lane == 0) __hip_atomic_store(x.ctl + OFF_ABORT / 4, 3u, RLX_AGENT);
      break;
    }
    __builtin_amdgcn_s_sleep(8);
  }
}
DI unsigned sigv(int L, int ph) { return (unsigned)(L * 8 + ph + 1); }

DI void compute_path(Ctx &x, const Params &p, v4u (&xres)[2], const v2u (&wph)[2], unsigned tag) {
  const unsigned vz[4] = {0u, 0u, 0u, 0u};
  v4u x1r[2], x2r[2];
#pragma unroll 1
  for (int L = 0; L < 2; ++L) {
    const unsigned lb0 = OFF_L0 + (unsigned)L * L_STRIDE;
    v4u phq[2][2];
    GBAR();
    {
      refresh(x);
      FS(0);
      unsigned lb = lb0;
      asm volatile("" : "+s"(lb));
      v4u bf[16][2];
      Stats st[2];
      const unsigned so = lb + O_XB + (unsigned)x.TA * 16u * KB, sst = lb + O_XS + (unsigned)x.TA * 1024u;
      unsigned cn;
      pro2(x, bf, st, so, sst, cn);
      FS(1); FSP(0);
      BAR();
      if (L == 0 && threadIdx.x == 0) __hip_atomic_store(x.ctl + OFF_FLAGS2 / 4 + x.b * 16, tag, RLX_AGENT);
      chk2(x, bf, st, so, sst, cn);
      FS(2);
      f4 acc[6][2];
#pragma unroll
      for (int f = 0; f < 6; ++f) { acc[f][0] = f4{0.f, 0.f, 0.f, 0.f}; acc[f][1] = f4{0.f, 0.f, 0.f, 0.f}; }
      unsigned mx = 0;
      kloop2<6, POS1, 0, 16>(x, bf, acc, so, sigv(L, 0), mx);
      if (__builtin_expect(anybad(mx), 0)) redo2<6, POS1>(x, acc, so);
      FS(3);
      BAR();
      MARK("P1epi");
      f4 val[2][6];
#pragma unroll
      for (int j = 0; j < 2; ++j) {
        float mean, var;
        stats_fin(x, st[j], sst + (unsigned)j * 1024u, mean, var);
        const float rstd = __builtin_amdgcn_rsqf(var + 1e-5f), mr = mean * rstd;
#pragma unroll
        for (int f = 0; f < 6; ++f) val[j][f] = acc[f][j] * rstd - ldcs(x, f) * mr;
      }
      {
        v2u pl[4], mi[4];
        const f4 kk[4] = {val[0][2], val[0][3], val[1][2], val[1][3]};
        featmap4(kk, wph, pl, mi);
#pragma unroll
        for (int i = 0; i < 4; ++i) {
          const int j = i >> 1, h = i & 1;
          const unsigned img = IMG + (unsigned)((2 * x.m + j) * 16 + x.tok) * 32u + (unsigned)x.g * 8u + (unsigned)(h * 3) * 4096u;
          *(LAS v2u *)(x.sm + img) = pl[i];
          *(LAS v2u *)(x.sm + img + 4096u) = mi[i];
          *(LAS v2u *)(x.sm + img + 8192u) = v2u{pk2(val[j][4 + h].x, val[j][4 + h].y), pk2(val[j][4 + h].z, val[j][4 + h].w)};
        }
      }
      MARK("P1epiend");
      FS(4);
      BAR();
      FS(5);
      {
        v2u pl[4], mi[4];
        TIE(val[0][0]); TIE(val[0][1]); TIE(val[1][0]); TIE(val[1][1]);
        const f4 qq[4] = {val[0][0], val[0][1], val[1][0], val[1][1]};
        featmap4(qq, wph, pl, mi);
#pragma unroll
        for (int i = 0; i < 4; ++i) phq[i >> 1][i & 1] = v4u{pl[i].x, pl[i].y, mi[i].x, mi[i].y};
      }
      FS(6);
    }
    GBAR();
    GBAR();
    {
      refresh(x);
      unsigned lb = lb0;
      asm volatile("" : "+s"(lb));
      BAR();
      FS(7);
      v4u ka[2], za[2];
#pragma unroll
      for (int h = 0; h < 2; ++h) {
        ka[h] = *lds4(x, M_KVA + (unsigned)h * 1024u + x.lane16);
        const v2u z0 = *(const LAS v2u *)(x.sm + M_ZA + (unsigned)(h * 2) * 32u + (unsigned)x.g * 8u);
        const v2u z1 = *(const LAS v2u *)(x.sm + M_ZA + (unsigned)(h * 2 + 1) * 32u + (unsigned)x.g * 8u);
        za[h] = v4u{z0.x, z0.y, z1.x, z1.y};
      }
#pragma unroll
      for (int j = 0; j < 2; ++j) {
        f4 o[2];
#pragma unroll
        for (int h = 0; h < 2; ++h) {
          const f4 num = mfma32(ka[h], phq[j][h], f4{0.f, 0.f, 0.f, 0.f});
          const f4 den = mfma32(za[h], phq[j][h], f4{0.f, 0.f, 0.f, 0.f});
          o[h] = num * __builtin_amdgcn_rcpf(den.x + 1e-6f);
        }
        stg(x.rs, x.lane16, lb + O_OB + (unsigned)((x.TA + j) * 16 + x.pp) * KB, pack8(o[0], o[1]));
      }
      FS(8);
      {
        const unsigned ob = lb + O_OB + (unsigned)(x.TA * 16 + x.pp) * KB;
        warm(x, (unsigned)((x.lane >> 3) & 1) * 16u * KB + (unsigned)(x.lane & 7) * 128u, ob, ob);
      }
    }
    GBAR();
    {
      refresh(x);
      unsigned lb = lb0;
      asm volatile("" : "+s"(lb));
      SLEEP(DLY_2);
      FS(9);
      v4u bf[16];
      const unsigned so = lb + O_OB + (unsigned)x.T * 16u * KB;
      Stats st;
      unsigned cn;
      const unsigned cvo = (unsigned)(x.lane & 15) * KB + 1020u;
      pro1<16, false, false>(x, bf, so, vz, cvo, st, 0u, cn);
      FS(10); FSP(1);
      BAR();
      chk1<16, false, false>(x, bf, so, vz, cvo, st, 0u, cn);
      FS(11);
      f4 acc[4];
#pragma unroll
      for (int f = 0; f < 4; ++f) acc[f] = f4{0.f, 0.f, 0.f, 0.f};
      kloop1<POS2, 16, false, 0, 16>(x, bf, acc, so, vz, sigv(L, 2));
      FS(12);
      x1r[0] = pack8(ulo(xres[0]) + acc[0], uhi(xres[0]) + acc[1]);
      x1r[1] = pack8(ulo(xres[1]) + acc[2], uhi(xres[1]) + acc[3]);
      const unsigned fo = lb + O_X1B + (unsigned)(x.T * 16 + 2 * x.c) * KB;
      stg(x.rs, x.lane16, fo, x1r[0]); stg(x.rs, x.lane16, fo + KB, x1r[1]);
      put_stats(x, x1r[0], x1r[1], lb + O_X1S);
      sig_set(x, sigv(L, 2), M_SIG2);
      FS(13);
      warm(x, (unsigned)(x.lane & 15) * 128u, fo, lb + O_X1S + (unsigned)(x.T * 8 + x.c) * 128u);
    }
    float mean1, rstd1;
    GBAR();
    {
      refresh(x);
      unsigned lb = lb0;
      asm volatile("" : "+s"(lb));
      SLEEP(DLY_3);
      v4u bf[16];
      Stats st;
      const unsigned so = lb + O_X1B + (unsigned)x.T * 16u * KB;
      unsigned cn;
      const unsigned cvo = (unsigned)(x.lane & 15) * KB + 1020u;
      pro1<16, false, true>(x, bf, so, vz, cvo, st, lb + O_X1S + (unsigned)x.T * 1024u, cn);
      FSP(2);
      BAR();
      chk1<16, false, true>(x, bf, so, vz, cvo, st, lb + O_X1S + (unsigned)x.T * 1024u, cn);
      FS(14);
      f4 acc[4];
#pragma unroll
      for (int f = 0; f < 4; ++f) acc[f] = f4{0.f, 0.f, 0.f, 0.f};
      kloop1<POS3, 16, false, 0, 16>(x, bf, acc, so, vz, sigv(L, 3));
      FS(15);
      float var;
      stats_fin(x, st, lb + O_X1S + (unsigned)x.T * 1024u, mean1, var);
      rstd1 = __builtin_amdgcn_rsqf(var + 1e-5f);
      const float a = rstd1 * __builtin_amdgcn_rsqf(__builtin_fmaf(var * rstd1, rstd1, 1e-5f)), ma = mean1 * a;
      f4 y[4];
#pragma unroll
      for (int f = 0; f < 4; ++f) y[f] = gelu4(acc[f] * a - ldcs(x, 6 + f) * ma);
      const unsigned fo = lb + O_HB + (unsigned)(x.T * 16 + 2 * x.c) * KB;
      stg(x.rs, x.lane16, fo, pack8(y[0], y[1])); stg(x.rs, x.lane16, fo + KB, pack8(y[2], y[3]));
      sig_set(x, sigv(L, 3), M_SIG2);
      FS(16);
      warm(x, (unsigned)(x.lane & 15) * 128u, fo, fo);
    }
    GBAR();
    {
      refresh(x);
      unsigned lb = lb0;
      asm volatile("" : "+s"(lb));
      SLEEP(DLY_4);
      v4u bf[16];
      unsigned vo[4];
#pragma unroll
      for (int q = 0; q < 4; ++q) {
        const int d = q == 0 ? -1 : (q == 1 ? 1 : (q == 2 ? -2 : 2));
        const int src = 16 * x.T + x.tok + d;
        vo[q] = (src >= 0 && src < 2048) ? (unsigned)((src >> 4) * 16 * 1024 + (x.g * 16 + (src & 15)) * 16) : OOB;
      }
      const unsigned so = lb + O_HB;
      unsigned cvo;
      {
        const int s_ = x.lane & 15, nb = x.T + (((s_ >> 2) & 1) ? 1 : -1);
        const int tt = (x.lane & 16) ? nb : x.T;
        cvo = (tt >= 0 && tt < 128) ? (unsigned)(tt * 16 + s_) * KB + 1020u : OOB;
      }
      Stats st;
      unsigned cn;
      pro1<16, true, false>(x, bf, so, vo, cvo, st, 0u, cn);
      FSP(3);
      BAR();
      chk1<16, true, false>(x, bf, so, vo, cvo, st, 0u, cn);
      FS(17);
      f4 acc[4];
#pragma unroll
      for (int f = 0; f < 4; ++f) acc[f] = f4{0.f, 0.f, 0.f, 0.f};
      MARK("P4loop");
      kloop1<POS4, 16, true, 0, 16>(x, bf, acc, so, vo, sigv(L, 4));
      MARK("P4end");
      FS(18);
      const float mr = mean1 * rstd1;
      x2r[0] = pack8(ulo(x1r[0]) * rstd1 - mr + acc[0], uhi(x1r[0]) * rstd1 - mr + acc[1]);
      x2r[1] = pack8(ulo(x1r[1]) * rstd1 - mr + acc[2], uhi(x1r[1]) * rstd1 - mr + acc[3]);
      const unsigned fo = lb + O_X2B + (unsigned)(x.T * 16 + 2 * x.c) * KB;
      stg(x.rs, x.lane16, fo, x2r[0]); stg(x.rs, x.lane16, fo + KB, x2r[1]);
      put_stats(x, x2r[0], x2r[1], lb + O_X2S);
      sig_set(x, sigv(L, 4), M_SIG2);
      FS(19);
      warm(x, (unsigned)(x.lane & 15) * 128u, fo, lb + O_X2S + (unsigned)(x.T * 8 + x.c) * 128u);
    }
    GBAR();
    {
      refresh(x);
      unsigned lb = lb0;
      asm volatile("" : "+s"(lb));
      SLEEP(DLY_5);
      v4u bf[16][2];
      Stats st[2];
      const unsigned so = lb + O_X2B + (unsigned)x.TA * 16u * KB, sst = lb + O_X2S + (unsigned)x.TA * 1024u;
      unsigned cn;
      pro2(x, bf, st, so, sst, cn);
      FSP(4);
      BAR();
      chk2(x, bf, st, so, sst, cn);
      FS(20);
      f4 acc[7][2];
#pragma unroll
      for (int f = 0; f < 7; ++f) { acc[f][0] = f4{0.f, 0.f, 0.f, 0.f}; acc[f][1] = f4{0.f, 0.f, 0.f, 0.f}; }
      unsigned mx = 0;
      MARK("P5loop");
      kloop2<7, POS5, 0, 13>(x, bf, acc, so, sigv(L, 5), mx);
      BAR();
      kloop2<7, POS5, 13, 16>(x, bf, acc, so, sigv(L, 5), mx);
      if (__builtin_expect(anybad(mx), 0)) redo2<7, POS5>(x, acc, so);
      FS(21);
      BAR();
      FS(22);
      MARK("P5epi");
      const int u = x.pp >> 1, odd = x.pp & 1;
#pragma unroll
      for (int j = 0; j < 2; ++j) {
        float mean, var;
        stats_fin(x, st[j], sst + (unsigned)j * 1024u, mean, var);
        const float rstd = __builtin_amdgcn_rsqf(var + 1e-5f), mr = mean * rstd;
        const unsigned hb = lb + O_H3B + (unsigned)((x.TA + j) * 56 + 7 * u) * KB;
#pragma unroll
        for (int r = 0; r < 3; ++r) {
          const f4 y0 = gelu4(acc[2 * r][j] * rstd - ldcs(x, 10 + 2 * r) * mr), y1 = gelu4(acc[2 * r + 1][j] * rstd - ldcs(x, 11 + 2 * r) * mr);
          stg(x.rs, x.lane16, hb + (unsigned)(odd ? 4 + r : r) * KB, pack8(y0, y1));
        }
        const f4 y = gelu4(acc[6][j] * rstd - ldcs(x, 16) * mr);
        stg2(x.rs, x.lane16 + (unsigned)odd * 8u, hb + 3 * KB, v2u{clampw(pk2(y.x, y.y)), clampw(pk2(y.z, y.w))});
      }
      FS(23);
      {
        const unsigned hb0 = lb + O_H3B + (unsigned)(x.TA * 56 + 7 * u + (odd ? 3 : 0)) * KB;
        warm(x, (unsigned)(x.lane >> 5) * 56u * KB + (unsigned)(x.lane & 31) * 128u, hb0, hb0);
      }
    }
    GBAR();
    {
      refresh(x);
      unsigned lb = lb0;
      asm volatile("" : "+s"(lb));
      SLEEP(DLY_6);
      v4u hf[56];
      const unsigned so = lb + O_H3B + (unsigned)x.T * 56u * KB;
      Stats st;
      unsigned cn;
      const unsigned cvo = (unsigned)min(x.lane, 55) * KB + 1020u;
      pro1<56, false, false>(x, hf, so, vz, cvo, st, 0u, cn);
      FSP(5);
      BAR();
      chk1<56, false, false>(x, hf, so, vz, cvo, st, 0u, cn);
      FS(24);
      f4 acc[4];
#pragma unroll
      for (int f = 0; f < 4; ++f) acc[f] = f4{0.f, 0.f, 0.f, 0.f};
      kloop1<POS6, 56, false, 0, 17>(x, hf, acc, so, vz, 0u);
      BAR();
      FS(25);
      kloop1<POS6, 56, false, 17, 39>(x, hf, acc, so, vz, 0u);
      BAR();
      FS(26);
      kloop1<POS6, 56, false, 39, 56>(x, hf, acc, so, vz, 0u);
      FS(27);
      BAR();
      xres[0] = pack8(ulo(x1r[0]) + (ulo(x2r[0]) + acc[0]), uhi(x1r[0]) + (uhi(x2r[0]) + acc[1]));
      xres[1] = pack8(ulo(x1r[1]) + (ulo(x2r[1]) + acc[2]), uhi(x1r[1]) + (uhi(x2r[1]) + acc[3]));
      if (L == 0) {
        const unsigned fo = lb + L_STRIDE + O_XB + (unsigned)(x.T * 16 + 2 * x.c) * KB;
        stg(x.rs, x.lane16, fo, xres[0]); stg(x.rs, x.lane16, fo + KB, xres[1]);
        put_stats(x, xres[0], xres[1], lb + L_STRIDE + O_XS);
        warm(x, (unsigned)(x.lane & 15) * 128u, fo, lb + L_STRIDE + O_XS + (unsigned)(x.T * 8 + x.c) * 128u);
        SLEEP(DLY_1);
      } else {
        GAS unsigned char *op = (GAS unsigned char *)p.out + ((size_t)(16 * x.T + x.tok) * 512 + 64 * x.c + 4 * x.g) * 2;
        *(GAS v2u *)op = v2u{xres[0].x, xres[0].y};
        *(GAS v2u *)(op + 32) = v2u{xres[0].z, xres[0].w};
        *(GAS v2u *)(op + 64) = v2u{xres[1].x, xres[1].y};
        *(GAS v2u *)(op + 96) = v2u{xres[1].z, xres[1].w};
      }
      FS(28); FSP(6);
    }
  }
}

DI void poison_h3(Ctx &x, unsigned lb, int j) {
  const int u = x.pp >> 1, odd = x.pp & 1;
  const unsigned hb = lb + O_H3B + (unsigned)((x.TA + j) * 56 + 7 * u) * KB;
#pragma unroll
  for (int r = 0; r < 3; ++r) poi4(x.rs, x.lane16, hb + (unsigned)(odd ? 4 + r : r) * KB);
  poi2(x.rs, x.lane16 + (unsigned)odd * 8u, hb + 3 * KB);
}
DI void helper_path(Ctx &x, unsigned tag) {
#pragma unroll 1
  for (int L = 0; L < 2; ++L) {
    const unsigned lb0 = OFF_L0 + (unsigned)L * L_STRIDE;
    GBAR();
    {
      refresh(x);
      unsigned lb = lb0;
      asm volatile("" : "+s"(lb));
      FS(0);
      DRAIN_BAR();
      FS(1);
      if (L != 0) {
        sig_wait(x, sigv(L, 0));
        dmaR<96, 156>(x);
      }
      BAR();
      {
        const unsigned fo = (unsigned)(x.T * 16 + 2 * x.c) * KB, sto = (unsigned)(x.T * 8 + x.c) * 128u;
        poi4(x.rs, x.lane16, lb + O_X1B + fo); poi4(x.rs, x.lane16, lb + O_X1B + fo + KB);
        poi4(x.rs, x.lane16, lb + O_X2B + fo); poi4(x.rs, x.lane16, lb + O_X2B + fo + KB);
        if (x.g == 0) { poi2(x.rs, (unsigned)x.tok * 8u, lb + O_X1S + sto); poi2(x.rs, (unsigned)x.tok * 8u, lb + O_X2S + sto); }
      }
      FS(2);
      BAR();
      FS(3);
      {
        const int h = x.m >> 1, sg = x.m & 1;
        const unsigned lo = (unsigned)(4 * x.g + (x.tok >> 2)) * 32u + (unsigned)(x.tok & 3) * 8u;
        const unsigned ia = IMG + (unsigned)(h * 3 + sg) * 4096u + lo, iv = IMG + (unsigned)(h * 3 + 2) * 4096u + lo;
        f4 kv = {0.f, 0.f, 0.f, 0.f}, z = {0.f, 0.f, 0.f, 0.f};
#pragma unroll
        for (int tb = 0; tb < 4; ++tb) {
          const v2u a0 = trread(x, ia + tb * 1024u), a1 = trread(x, ia + tb * 1024u + 512u);
          const v2u b0 = trread(x, iv + tb * 1024u), b1 = trread(x, iv + tb * 1024u + 512u);
          const v4u a = {a0.x, a0.y, a1.x, a1.y};
          kv = mfma32(a, v4u{b0.x, b0.y, b1.x, b1.y}, kv);
          z = mfma32(a, v4u{0x3F803F80u, 0x3F803F80u, 0x3F803F80u, 0x3F803F80u}, z);
        }
        stg(x.rs, x.lane16, lb + O_KVP + (unsigned)((x.pp * 16 + x.tau) * 4 + x.m) * KB, clamp4(__builtin_bit_cast(v4u, kv)));
        if (x.tok == 0) stg(x.rs, (unsigned)x.g * 16u, lb + O_KVZ + (unsigned)((x.pp * 4 + x.m) * 16 + x.tau) * 64u, clamp4(__builtin_bit_cast(v4u, z)));
      }
      FS(4);
    }
    GBAR();
    {
      refresh(x);
      unsigned lb = lb0;
      asm volatile("" : "+s"(lb));
      if (L == 0) {
        if (x.m == 0) {
          poll_flags(x, OFF_FLAGS2, tag);
          *(volatile LAS unsigned *)(x.sm + M_RDY) = 1u;
        } else {
          for (unsigned sp = 0; sp < (1u << 20); ++sp) {
            if (__builtin_amdgcn_readfirstlane(*(volatile LAS unsigned *)(x.sm + M_RDY))) break;
            __builtin_amdgcn_s_sleep(2);
          }
        }
        v4u cv[3];
#pragma unroll
        for (int jj = 0; jj < 3; ++jj) {
          const int u = min(6 + x.m + 4 * jj, 16);
          cv[jj] = ldg(x.rs, x.lane16, OFF_CSP + (u < 10 ? 96u + (unsigned)(x.c * 4 + u - 6) : 128u + (unsigned)(x.pp * 7 + u - 10)) * KB);
        }
#pragma unroll
        for (int jj = 0; jj < 3; ++jj) {
          const f4 s = shsum(__builtin_bit_cast(f4, cv[jj]));
          const int u = 6 + x.m + 4 * jj;
          if (u < 17 && x.lane < 4) *(LAS f4 *)(x.sm + M_CS + (unsigned)u * 64u + (unsigned)x.lane * 16u) = s;
        }
      }
      SLEEP(DLY_KV);
      v4u pv[16];
      const unsigned so = lb + O_KVP + (unsigned)(x.pp * 64 + x.m) * KB, sz = lb + O_KVZ + (unsigned)(x.pp * 4 + x.m) * KB;
      v4u zv;
      {
        const unsigned cvo = (unsigned)(x.lane & 15) * 4096u + 1020u;
        while (true) {
          unsigned cn;
          LDC(cn, cvo, so);
          LDA(zv, x.lane16, sz);
          WAITV2(0, cn, zv);
          if (!anybad(max(cn, max4(zv))) || giveup(x)) break;
        }
      }
      bool again;
      do {
        asm volatile("" ::: "memory");
#pragma unroll
        for (int i = 0; i < 16; ++i) pv[i] = ldg(x.rs, x.lane16, so + (unsigned)i * 4096u);
        unsigned mx = 0;
#pragma unroll
        for (int i = 0; i < 16; ++i) mx = max(mx, max4(pv[i]));
        again = anybad(mx) && !giveup(x);
      } while (__builtin_expect(again, 0));
      f4 s = __builtin_bit_cast(f4, pv[0]);
#pragma unroll
      for (int i = 1; i < 16; ++i) s += __builtin_bit_cast(f4, pv[i]);
      *(LAS v2u *)(x.sm + M_KVA + (unsigned)(x.m >> 1) * 1024u + x.lane16 + (unsigned)(x.m & 1) * 8u) = v2u{pk2(s.x, s.y), pk2(s.z, s.w)};
      const f4 zs = shsum(__builtin_bit_cast(f4, zv));
      if (x.lane < 4) *(LAS v2u *)(x.sm + M_ZA + (unsigned)x.m * 32u + (unsigned)x.lane * 8u) = v2u{pk2(zs.x, zs.y), pk2(zs.z, zs.w)};
      FS(5); FSP(0);
      GBAR();
      BAR();
      FS(6);
      if (L == 0) dmaR<96, 156>(x);
      dmaR<156, 188>(x);
    }
    GBAR();
    {
      refresh(x);
      unsigned lb = lb0;
      asm volatile("" : "+s"(lb));
      DRAIN_BAR();
      FS(7);
      sig_wait(x, sigv(L, 2));
      dmaR<188, 220>(x);
      sig_wait(x, sigv(L, 2), M_SIG2);
      dmaR<220, 252>(x);
      poison_h3(x, lb, 0); poison_h3(x, lb, 1);
    }
    GBAR();
    {
      refresh(x);
      unsigned lb = lb0;
      asm volatile("" : "+s"(lb));
      WAITV_BAR(8);
      FS(8);
      sig_wait(x, sigv(L, 3));
      FS(15);
      dmaR<252, 284>(x);
      sig_wait(x, sigv(L, 3), M_SIG2);
      dmaR<284, 316>(x);
      FS(16);
      if (L == 0) {
        const unsigned fo = lb + L_STRIDE + O_XB + (unsigned)(x.T * 16 + 2 * x.c) * KB;
        poi4(x.rs, x.lane16, fo); poi4(x.rs, x.lane16, fo + KB);
        if (x.g == 0) poi2(x.rs, (unsigned)x.tok * 8u, lb + L_STRIDE + O_XS + (unsigned)(x.T * 8 + x.c) * 128u);
      }
    }
    GBAR();
    {
      refresh(x);
      unsigned lb = lb0;
      asm volatile("" : "+s"(lb));
      if (L == 0) WAITV_BAR(3); else WAITV_BAR(0);
      FS(9);
      sig_wait(x, sigv(L, 4));
      FS(18);
      dmaR<316, 348>(x);
      sig_wait(x, sigv(L, 4), M_SIG2);
      dmaR<348, 380>(x);
      FS(19);
      if (L == 0) {
        const unsigned fo = lb + L_STRIDE + O_HB + (unsigned)(x.T * 16 + 2 * x.c) * KB;
        poi4(x.rs, x.lane16, fo); poi4(x.rs, x.lane16, fo + KB);
        poi4(x.rs, x.lane16, lb + L_STRIDE + O_OB + (unsigned)(x.TA * 16 + x.pp) * KB);
        poi4(x.rs, x.lane16, lb + L_STRIDE + O_OB + (unsigned)((x.TA + 1) * 16 + x.pp) * KB);
      }
    }
    GBAR();
    {
      refresh(x);
      if (L == 0) WAITV_BAR(4); else WAITV_BAR(0);
      FS(10);
      dmaRT<380, 400>(x);
      DRAIN_BAR();
      sig_wait(x, sigv(L, 5));
      dmaR<400, 444>(x);
      BAR();
      FS(11);
      dmaR6<444, 556>(x);
    }
    GBAR();
    {
      refresh(x);
      DRAIN_BAR();
      FS(12);
      BAR();
      dmaRT<556, 624>(x);
      DRAIN_BAR();
      FS(13);
      dmaR<624, 712>(x, L == 0);
      BAR();
      dmaR<712, 720>(x, L == 0);
      FS(14);
    }
  }
}

}

__global__ void __launch_bounds__(512, 2) mega_kernel(Params p) {
  extern __shared__ __attribute__((aligned(16))) unsigned char smem_[];
  Ctx x;
  x.sm = (LAS unsigned char *)smem_;
  x.lds0 = (unsigned)(size_t)smem_;
  x.lane = threadIdx.x & 63;
  x.w = __builtin_amdgcn_readfirstlane(threadIdx.x >> 6);
  x.b = blockIdx.x;
  refresh(x);
  const int b = x.b;
  x.spins = 0;
  x.ctl = (gu32 *)p.ws;
  x.rs = __builtin_amdgcn_make_buffer_rsrc((void *)p.ws, 0, WS_BYTES, 0x00020000);
  const bool cw = x.w < 4;

  FSA(0);
  const unsigned tag = __hip_atomic_load(x.ctl + OFF_EPOCH / 4, RLX_AGENT) + 1u;
  unsigned pf0 = 0, pf1 = 0;
  if (x.w == 7) {
    unsigned long long pc;
    asm volatile("s_getpc_b64 %0" : "=s"(pc));
    const GAS unsigned char *cp = (const GAS unsigned char *)(pc & ~127ull) + (size_t)(((b >> 3) + 32 * (x.lane & 15)) * 128);
    const GAS unsigned char *wp = (const GAS unsigned char *)p.ws + (size_t)min(x.lane, 28) * (2 * MB) + 1 * MB;
    asm volatile("global_load_dword %0, %2, off\n\tglobal_load_dword %1, %3, off" : "=&v"(pf0), "=&v"(pf1) : "v"(cp), "v"(wp) : "memory");
    asm volatile("s_waitcnt vmcnt(0)" : "+v"(pf0), "+v"(pf1)::"memory");
  }
  v2u xin[4] = {};
  unsigned short wraw[2][4] = {};
  if (cw) {
    const GAS unsigned char *xp = (const GAS unsigned char *)p.x + ((size_t)(16 * x.T + x.tok) * 512 + 64 * x.c + 4 * x.g) * 2;
#pragma unroll
    for (int q = 0; q < 4; ++q) xin[q] = *(const GAS v2u *)(xp + 32 * q);
#pragma unroll
    for (int h = 0; h < 2; ++h) {
      const unsigned short *wp = p.wphi + ((2 * x.pp + h) * 16 + 4 * x.g) * 16 + x.tok;
#pragma unroll
      for (int jj = 0; jj < 4; ++jj) wraw[h][jj] = wp[16 * jj];
    }
  }
  if (threadIdx.x < 9) *(LAS unsigned *)(x.sm + M_SIG + threadIdx.x * 4u) = 0u;
  const int bid = ((x.w + 4) & 7) * 256 + b;
  const unsigned short *src = p.wqkv; int ld = 1536, ra = 0, rb = 0, n0 = 0, kstep = 0, csid = -1; unsigned fr[4] = {0u, 0u, 0u, 0u}, ci[4] = {0u, 0u, 0u, 0u};
  if (bid < 384) {
    kstep = bid & 15; const int nb = bid >> 4; n0 = nb * 64; ra = 32 * kstep; rb = ra + 16;
    const int sec = nb >> 3, h0 = 4 * (nb & 7);
#pragma unroll
    for (int ct = 0; ct < 4; ++ct) {
      const int hh = h0 + ct;
      fr[ct] = W1_F + (unsigned)(((hh >> 1) * 16 + kstep) * 6 + sec * 2 + (hh & 1));
      ci[ct] = (unsigned)((hh >> 1) * 6 + sec * 2 + (hh & 1));
    }
    csid = 0;
  } else if (bid < 768) {
    const int mi = (bid - 384) >> 7, r = (bid - 384) & 127; kstep = r & 15; const int nb = r >> 4;
    src = mi == 0 ? p.wo : (mi == 1 ? p.w1 : p.w2); ld = 512; n0 = nb * 64; ra = 32 * kstep; rb = ra + 16;
#pragma unroll
    for (int ct = 0; ct < 4; ++ct) { fr[ct] = W2_F + (unsigned)(mi * 512 + (nb * 16 + kstep) * 4 + ct); ci[ct] = 96u + (unsigned)(nb * 4 + ct); }
    if (mi == 1) csid = 1;
  } else if (bid < 1216) {
    const int r = bid - 768; kstep = r & 15; const int nb = r >> 4; src = p.w3; ld = 1792; n0 = nb * 64; ra = 32 * kstep; rb = ra + 16;
#pragma unroll
    for (int ct = 0; ct < 4; ++ct) { const int F = nb * 4 + ct; fr[ct] = W5_F + (unsigned)(((F / 7) * 16 + kstep) * 7 + F % 7); ci[ct] = 128u + (unsigned)F; }
    csid = 2;
  } else if (bid < 1664) {
    const int r = bid - 1216; const int nb = r & 7; kstep = r >> 3; src = p.w4; ld = 512; n0 = nb * 64;
    const int u = kstep / 7, q = kstep % 7;
    if (q < 3) { ra = 224 * u + 32 * q; rb = ra + 16; }
    else if (q == 3) { ra = 224 * u + 96; rb = 224 * u + 208; }
    else { ra = 224 * u + 112 + 32 * (q - 4); rb = ra + 16; }
#pragma unroll
    for (int ct = 0; ct < 4; ++ct) fr[ct] = W6_F + (unsigned)((nb * 56 + kstep) * 4 + ct);
  }
  if (bid < 1664) {
    const int ip = x.lane >> 5, uu = (x.lane >> 4) & 1, q = (x.lane >> 2) & 3, c1 = ((x.lane >> 1) & 1) ^ ip, bb = x.lane & 1;
    const int rl = 4 * ip + q, ch = 2 * (2 * uu + c1) + bb;
    const unsigned stg0 = x.lds0 + (unsigned)x.w * 4096u;
#pragma unroll
    for (int u = 0; u < 4; ++u) {
      const int row = (u < 2 ? ra + 8 * u : rb + 8 * (u - 2)) + rl;
      const GAS unsigned char *gp = (const GAS unsigned char *)src + ((size_t)row * ld + n0 + ch * 8) * 2;
      asm volatile("s_mov_b32 m0, %0\n\ts_nop 0\n\tglobal_load_lds_dwordx4 %1, off" ::"s"(stg0 + (unsigned)u * 1024u), "v"(gp) : "memory");
    }
  }
  auto retile = [&]() {
    asm volatile("s_waitcnt vmcnt(0)" ::: "memory");
    const int gq = (x.lane & 15) >> 2, gp_ = x.lane & 3, gi = x.g & 1;
#pragma unroll
    for (int ct = 0; ct < 4; ++ct) {
      const unsigned L = (unsigned)((gi << 5) | ((ct >> 1) << 4) | (gq << 2) | (((ct & 1) ^ gi) << 1) | (gp_ >> 1));
      const unsigned a0 = (unsigned)x.w * 4096u + (unsigned)(x.g >> 1) * 1024u + L * 16u + (unsigned)(gp_ & 1) * 8u;
      const v2u lo = trread(x, a0), hi = trread(x, a0 + 2048u);
      const v4u fragv = {lo.x, lo.y, hi.x, hi.y};
      stg(x.rs, x.lane16, __builtin_amdgcn_readfirstlane(OFF_WT + fr[ct] * KB), fragv);
      if (csid >= 0) {
        const f4 cs = mfma32(fragv, v4u{0x3F803F80u, 0x3F803F80u, 0x3F803F80u, 0x3F803F80u}, f4{0.f, 0.f, 0.f, 0.f});
        const unsigned co = __builtin_amdgcn_readfirstlane(OFF_CSP + (ci[ct] * 16u + (unsigned)kstep) * 64u);
        if (x.tok == 0) stg(x.rs, (unsigned)x.g * 16u, co, __builtin_bit_cast(v4u, cs));
      }
    }
  };
  if (bid < 384) retile();
  FSA(1);
  v4u xres[2];
  v2u wph[2];
  if (cw) {
    xres[0] = clamp4(v4u{xin[0].x, xin[0].y, xin[1].x, xin[1].y});
    xres[1] = clamp4(v4u{xin[2].x, xin[2].y, xin[3].x, xin[3].y});
    const unsigned fo = OFF_L0 + O_XB + (unsigned)(x.T * 16 + 2 * x.c) * KB;
    stg(x.rs, x.lane16, fo, xres[0]); stg(x.rs, x.lane16, fo + KB, xres[1]);
    put_stats(x, xres[0], xres[1], OFF_L0 + O_XS);
#pragma unroll
    for (int h = 0; h < 2; ++h)
      wph[h] = v2u{(unsigned)wraw[h][0] | ((unsigned)wraw[h][1] << 16), (unsigned)wraw[h][2] | ((unsigned)wraw[h][3] << 16)};
  } else {
    xres[0] = xres[1] = v4u{0u, 0u, 0u, 0u};
    wph[0] = wph[1] = v2u{0u, 0u};
  }
  FSA(2);
  asm volatile("s_waitcnt vmcnt(0) lgkmcnt(0)\n\ts_barrier" ::: "memory");
  FSA(3);
  if (threadIdx.x == 0) __hip_atomic_store(x.ctl + OFF_FLAGS / 4 + b * 16, tag, RLX_AGENT);
  if (bid >= 384 && bid < 1664) retile();
  if (!cw) {
    poi4(x.rs, x.lane16, OFF_L0 + O_KVP + (unsigned)((x.pp * 16 + x.tau) * 4 + x.m) * KB);
    if (x.tok == 0) poi4(x.rs, (unsigned)x.g * 16u, OFF_L0 + O_KVZ + (unsigned)((x.pp * 4 + x.m) * 16 + x.tau) * 64u);
    poi4(x.rs, x.lane16, OFF_L0 + O_OB + (unsigned)(x.TA * 16 + x.pp) * KB);
    poi4(x.rs, x.lane16, OFF_L0 + O_OB + (unsigned)((x.TA + 1) * 16 + x.pp) * KB);
    poi4(x.rs, x.lane16, OFF_L0 + O_HB + (unsigned)(x.T * 16 + 2 * x.c) * KB);
    poi4(x.rs, x.lane16, OFF_L0 + O_HB + (unsigned)(x.T * 16 + 2 * x.c + 1) * KB);
    poi4(x.rs, x.lane16, OFF_L0 + L_STRIDE + O_KVP + (unsigned)((x.pp * 16 + x.tau) * 4 + x.m) * KB);
    if (x.tok == 0) poi4(x.rs, (unsigned)x.g * 16u, OFF_L0 + L_STRIDE + O_KVZ + (unsigned)((x.pp * 4 + x.m) * 16 + x.tau) * 64u);
  }
  if (x.w == 4) {
    poll_flags(x, OFF_FLAGS, tag);
    if (b == 0 && x.lane == 0) __hip_atomic_store(x.ctl + OFF_EPOCH / 4, tag, RLX_AGENT);
  }
  BAR();
  FSA(4);
  if (cw) {
    compute_path(x, p, xres, wph, tag);
  } else {
    v4u cv[2];
#pragma unroll
    for (int jj = 0; jj < 2; ++jj) LDA(cv[jj], x.lane16, OFF_CSP + (unsigned)(x.pp * 6 + min(x.m + 4 * jj, 5)) * KB);
    dmaR<POS1, POS2>(x);
#pragma unroll
    for (int jj = 0; jj < 2; ++jj) {
      WAITV1(24, cv[jj]);
      const f4 s = shsum(__builtin_bit_cast(f4, cv[jj]));
      const int u = x.m + 4 * jj;
      if (u < 6 && x.lane < 4) *(LAS f4 *)(x.sm + M_CS + (unsigned)u * 64u + (unsigned)x.lane * 16u) = s;
    }
    helper_path(x, tag);
  }
  asm volatile("s_waitcnt vmcnt(0)" ::: "memory");
}

extern "C" void kernel_launch(void *const *d_in, const int *in_sizes, int n_in, void *d_out, int out_size, void *d_ws, size_t ws_size,
                              hipStream_t stream) {
  Params p;
  p.x = (const unsigned short *)d_in[0];
  p.wqkv = (const unsigned short *)d_in[1];
  p.wphi = (const unsigned short *)d_in[2];
  p.wo = (const unsigned short *)d_in[3];
  p.w1 = (const unsigned short *)d_in[5];
  p.w2 = (const unsigned short *)d_in[6];
  p.w3 = (const unsigned short *)d_in[7];
  p.w4 = (const unsigned short *)d_in[8];
  p.out = (unsigned short *)d_out;
  p.ws = (unsigned char *)d_ws;
  (void)hipFuncSetAttribute((const void *)mega_kernel, hipFuncAttributeMaxDynamicSharedMemorySize, LDS_BYTES);
  hipLaunchKernelGGL(mega_kernel, dim3(256), dim3(512), LDS_BYTES, stream, p);
}
```

```cpp
#include <hip/hip_runtime.h>
#include <stdint.h>

#define GAS __attribute__((address_space(1)))
#define LAS __attribute__((address_space(3)))
#define DI __device__ __forceinline__
typedef unsigned v4u __attribute__((ext_vector_type(4)));
typedef unsigned v2u __attribute__((ext_vector_type(2)));
typedef float f4 __attribute__((ext_vector_type(4)));
typedef float f2 __attribute__((ext_vector_type(2)));
typedef short s8 __attribute__((ext_vector_type(8)));
typedef short s4 __attribute__((ext_vector_type(4)));
typedef __bf16 bf2 __attribute__((ext_vector_type(2)));
typedef GAS unsigned gu32;
typedef __amdgpu_buffer_rsrc_t rsrc_t;
#define RLX_AGENT __ATOMIC_RELAXED, __HIP_MEMORY_SCOPE_AGENT

#define DBG_GBAR 0
#define DBG_NOPOISON 0
#define DBG_STAMP 0
#define SPEC 0
#define PREFETCH 1
#define KV16 1
#define WARM 0

namespace {
constexpr unsigned MB = 1u << 20, KB = 1024u;
constexpr unsigned OFF_EPOCH = 0, OFF_ABORT = 256, OFF_GBAR = 512, OFF_FLAGS = 16384, OFF_FLAGS2 = 32768;
constexpr unsigned OFF_WT = 1 * MB;
constexpr unsigned W1_F = 0, W2_F = 1536, W5_F = 3072, W6_F = 4864;
constexpr unsigned OFF_CSP = 8 * MB;
constexpr unsigned OFF_L0 = 9 * MB, L_STRIDE = 24 * MB;
constexpr unsigned O_XB = 0, O_XS = 2 * MB, O_KVP = 2 * MB + 512 * KB, O_KVZ = 3 * MB + 512 * KB, O_OB = 4 * MB, O_X1B = 6 * MB,
                   O_X1S = 8 * MB, O_HB = 9 * MB, O_X2B = 11 * MB, O_X2S = 13 * MB, O_H3B = 14 * MB;
constexpr unsigned WS_BYTES = OFF_L0 + 2 * L_STRIDE;
constexpr unsigned OOB = 0x7F000000u;
constexpr int RING = 156;
constexpr unsigned MISC = RING * KB;
constexpr unsigned M_CS = MISC, M_KVA = MISC + 1088, M_ZA = MISC + 3136, M_SIG = MISC + 3264, M_SIG2 = MISC + 3280, M_RDY = MISC + 3296, M_FLAG = MISC + 3300;
constexpr unsigned IMG = 0;
constexpr unsigned LDS_BYTES = 163840;
constexpr int POS1 = 0, POS2 = 96, POS3 = 160, POS4 = 224, POS5 = 288, POS6 = 400, POSE = 624;
constexpr unsigned SENT = 0xFFFFFFFFu;
constexpr unsigned SPIN_CAP = 1u << 13;
constexpr int AUX_SC1 = 16;
#define DLY_KV 16
#define DLY_1 16
#define DLY_2 16
#define DLY_3 16
#define DLY_4 16
#define DLY_5 16
#define DLY_6 16

struct Params {
  const unsigned short *x, *wqkv, *wphi, *wo, *w1, *w2, *w3, *w4;
  unsigned short *out;
  unsigned char *ws;
};

DI unsigned pk2(float a, float b) { f2 v = {a, b}; bf2 r = __builtin_convertvector(v, bf2); return __builtin_bit_cast(unsigned, r); }
DI float asf(unsigned u) { return __builtin_bit_cast(float, u); }
DI float blo(unsigned u) { return __builtin_bit_cast(float, u << 16); }
DI float bhi(unsigned u) { return __builtin_bit_cast(float, u & 0xFFFF0000u); }
DI unsigned clampw(unsigned u) { return u < 0xFFFFFFFEu ? u : 0xFFFFFFFEu; }
DI unsigned clampf(float f) { return clampw(__builtin_bit_cast(unsigned, f)); }
DI v4u clamp4(v4u v) { return v4u{clampw(v.x), clampw(v.y), clampw(v.z), clampw(v.w)}; }
DI v4u pack8(f4 a, f4 b) { return clamp4(v4u{pk2(a.x, a.y), pk2(a.z, a.w), pk2(b.x, b.y), pk2(b.z, b.w)}); }
DI f4 ulo(v4u v) { return f4{blo(v.x), bhi(v.x), blo(v.y), bhi(v.y)}; }
DI f4 uhi(v4u v) { return f4{blo(v.z), bhi(v.z), blo(v.w), bhi(v.w)}; }
DI f4 mfma32(v4u a, v4u b, f4 c) {
  return __builtin_amdgcn_mfma_f32_16x16x32_bf16(__builtin_bit_cast(s8, a), __builtin_bit_cast(s8, b), c, 0, 0, 0);
}
DI f4 mfma16(v2u a, v2u b, f4 c) {
  return __builtin_amdgcn_mfma_f32_16x16x16bf16_1k(__builtin_bit_cast(s4, a), __builtin_bit_cast(s4, b), c, 0, 0, 0);
}
DI v4u ldg(rsrc_t rs, unsigned voff, unsigned soff) { return __builtin_amdgcn_raw_buffer_load_b128(rs, voff, soff, AUX_SC1); }
DI void stg(rsrc_t rs, unsigned voff, unsigned soff, v4u v) {
  __builtin_amdgcn_raw_buffer_store_b128(v, rs, voff, soff, AUX_SC1);
  asm volatile("s_nop 1" ::"v"(v));
}
DI void stg2(rsrc_t rs, unsigned voff, unsigned soff, v2u v) { __builtin_amdgcn_raw_buffer_store_b64(v, rs, voff, soff, AUX_SC1); }
DI void poi4(rsrc_t rs, unsigned voff, unsigned soff) { if (!DBG_NOPOISON) stg(rs, voff, soff, v4u{0xFFFFFFFFu, 0xFFFFFFFFu, 0xFFFFFFFFu, 0xFFFFFFFFu}); }
DI void poi2(rsrc_t rs, unsigned voff, unsigned soff) { if (!DBG_NOPOISON) stg2(rs, voff, soff, v2u{0xFFFFFFFFu, 0xFFFFFFFFu}); }
DI unsigned max4(v4u v) { return max(max(v.x, v.y), max(v.z, v.w)); }
DI bool anybad(unsigned mx) { return __any(mx == SENT) != 0; }
DI float xsum(float x) {
  unsigned u = __builtin_bit_cast(unsigned, x);
  auto r = __builtin_amdgcn_permlane16_swap(u, u, false, false);
  x = __builtin_bit_cast(float, (unsigned)r[0]) + __builtin_bit_cast(float, (unsigned)r[1]);
  u = __builtin_bit_cast(unsigned, x);
  auto q = __builtin_amdgcn_permlane32_swap(u, u, false, false);
  return __builtin_bit_cast(float, (unsigned)q[0]) + __builtin_bit_cast(float, (unsigned)q[1]);
}
DI float xmax(float x) {
  unsigned u = __builtin_bit_cast(unsigned, x);
  auto r = __builtin_amdgcn_permlane16_swap(u, u, false, false);
  x = fmaxf(__builtin_bit_cast(float, (unsigned)r[0]), __builtin_bit_cast(float, (unsigned)r[1]));
  u = __builtin_bit_cast(unsigned, x);
  auto q = __builtin_amdgcn_permlane32_swap(u, u, false, false);
  return fmaxf(__builtin_bit_cast(float, (unsigned)q[0]), __builtin_bit_cast(float, (unsigned)q[1]));
}
template <bool MAX> DI float red_op(float a, float b) { return MAX ? fmaxf(a, b) : a + b; }
DI unsigned fbits(float f) { return __builtin_bit_cast(unsigned, f); }
template <bool MAX> DI void xred4(float &a, float &b, float &c, float &d) {
  const auto r1 = __builtin_amdgcn_permlane16_swap(fbits(a), fbits(b), false, false);
  const auto r2 = __builtin_amdgcn_permlane16_swap(fbits(c), fbits(d), false, false);
  const float c1 = red_op<MAX>(asf(r1[0]), asf(r1[1])), c2 = red_op<MAX>(asf(r2[0]), asf(r2[1]));
  const auto r3 = __builtin_amdgcn_permlane32_swap(fbits(c1), fbits(c2), false, false);
  const unsigned e = fbits(red_op<MAX>(asf(r3[0]), asf(r3[1])));
  const auto r4 = __builtin_amdgcn_permlane32_swap(e, e, false, false);
  const unsigned f0 = r4[0], f1 = r4[1];
  const auto r5 = __builtin_amdgcn_permlane16_swap(f0, f0, false, false);
  const auto r6 = __builtin_amdgcn_permlane16_swap(f1, f1, false, false);
  a = asf(r5[0]); b = asf(r5[1]); c = asf(r6[0]); d = asf(r6[1]);
}
template <bool MAX> DI void xred2(float &a, float &b) {
  const auto r1 = __builtin_amdgcn_permlane16_swap(fbits(a), fbits(b), false, false);
  const unsigned c = fbits(red_op<MAX>(asf(r1[0]), asf(r1[1])));
  const auto r3 = __builtin_amdgcn_permlane32_swap(c, c, false, false);
  const unsigned e = fbits(red_op<MAX>(asf(r3[0]), asf(r3[1])));
  const auto r5 = __builtin_amdgcn_permlane16_swap(e, e, false, false);
  a = asf(r5[0]); b = asf(r5[1]);
}
DI f2 gelu2(f2 x) {
  const f2 c1 = {-2.302208198f, -2.302208198f}, c2 = {-0.1029432397f, -0.1029432397f}, one = {1.0f, 1.0f};
  const f2 t = x * __builtin_elementwise_fma(x * x, c2, c1);
  const f2 d = one + f2{__builtin_amdgcn_exp2f(t.x), __builtin_amdgcn_exp2f(t.y)};
  return x * f2{__builtin_amdgcn_rcpf(d.x), __builtin_amdgcn_rcpf(d.y)};
}
DI f4 gelu4(f4 y) {
  const f2 a = gelu2(f2{y.x, y.y}), b = gelu2(f2{y.z, y.w});
  return f4{a.x, a.y, b.x, b.y};
}

#define FSL 1
#define FS(i) do {} while (0)
#define FSA(i) do { unsigned long long t_; asm volatile("s_memtime %0\n\ts_waitcnt lgkmcnt(0)" : "=s"(t_) :: "memory"); \
  int ww_ = x.w; asm volatile("" : "+s"(ww_)); \
  unsigned a_ = (ww_ == 0) ? (70 + (i)) * 4u : ((ww_ == 4) ? 400u + (70 + (i)) * 4u : 780u); \
  asm volatile("" : "+s"(a_)); \
  *(LAS unsigned *)(x.sm + M_FLAG + a_) = (unsigned)t_; } while (0)
#define FSP(i) do {} while (0)
#define SB() __builtin_amdgcn_sched_barrier(0)
#define MARK(s) asm volatile("; MARK " s)
#define BAR() asm volatile("s_waitcnt lgkmcnt(0)\n\ts_barrier" ::: "memory")
#define WAITV_BAR(n) asm volatile("s_waitcnt vmcnt(" #n ") lgkmcnt(0)\n\ts_barrier" ::: "memory")
#define DRAIN_BAR() asm volatile("s_waitcnt vmcnt(0) lgkmcnt(0)\n\ts_barrier" ::: "memory")
DI void sleepn(int n) {
  for (int i = 0; i < n; ++i) __builtin_amdgcn_s_sleep(1);
}

struct Ctx {
  rsrc_t rs;
  LAS unsigned char *sm;
  unsigned lds0;
  unsigned lane16;
  LAS unsigned char *ab[3];
  int lane, w, m, g, tok;
  int t, c, tau, pp, b;
  int T, TA;
  unsigned spins;
  gu32 *ctl;
};
DI void refresh(Ctx &x) {
  asm volatile("" : "+s"(x.w), "+s"(x.b), "+v"(x.lane));
  x.lane16 = (unsigned)x.lane * 16u;
  x.g = x.lane >> 4; x.tok = x.lane & 15;
  x.m = x.w & 3;
  const int xc = x.b & 7, j = x.b >> 3;
  x.t = 4 * xc + (j >> 3); x.c = j & 7;
  x.tau = 2 * xc + (j >> 4); x.pp = j & 15;
  x.T = 4 * x.t + x.m;
  x.TA = 8 * x.tau + 2 * x.m;
  x.ab[0] = x.sm + x.lane16;
  unsigned o1 = x.lane16 + 60u * KB, o2 = x.lane16 + 120u * KB;
  asm volatile("" : "+v"(o1), "+v"(o2));
  x.ab[1] = x.sm + o1; x.ab[2] = x.sm + o2;
}

template <int P> DI void dmaP(const Ctx &x, bool live = true) {
  constexpr int Q = P % POSE;
  int w = x.w, b = x.b;
  asm volatile("" : "+s"(w), "+s"(b));
  const int m = w & 3, j = b >> 3;
  unsigned slot = (unsigned)(P % RING) + (unsigned)m;
  unsigned fr;
  if constexpr (Q < POS2) fr = W1_F + (unsigned)((j & 15) * 96 + Q + m);
  else if constexpr (Q < POS5) fr = W2_F + (unsigned)(((Q - POS2) / 64) * 512 + (j & 7) * 64 + (Q - POS2) % 64 + m);
  else if constexpr (Q < POS6) fr = W5_F + (unsigned)((j & 15) * 112 + (Q - POS5) + m);
  else fr = W6_F + (unsigned)((j & 7) * 224 + (Q - POS6) + m);
  const unsigned lds = x.lds0 + slot * KB, so = live ? OFF_WT + fr * KB : OOB;
  asm volatile("s_mov_b32 m0, %0\n\ts_nop 0\n\tbuffer_load_dwordx4 %1, %2, %3 offen sc1 lds" ::"s"(lds), "v"(x.lane16), "s"(x.rs), "s"(so) : "memory");
}
template <int LO, int HI> DI void dmaR(const Ctx &x, bool live = true) {
  if constexpr (LO < HI) { dmaP<LO>(x, live); dmaR<LO + 4, HI>(x, live); }
}
template <int LO, int HI> DI void dmaRT(const Ctx &x, bool live = true) {
  if constexpr (LO < HI) { dmaP<LO>(x, live); asm volatile("s_waitcnt vmcnt(3)" ::: "memory"); dmaRT<LO + 4, HI>(x, live); }
}
template <int LO, int HI> DI void dmaR6(const Ctx &x, bool live = true) {
  if constexpr (LO < HI) { dmaP<LO>(x, live); asm volatile("s_waitcnt vmcnt(5)" ::: "memory"); dmaR6<LO + 4, HI>(x, live); }
}
DI void sig_set(const Ctx &x, unsigned v, unsigned word = M_SIG) { *(volatile LAS unsigned *)(x.sm + word + (unsigned)x.m * 4u) = v; }
DI void sig_wait(const Ctx &x, unsigned v, unsigned word = M_SIG) {
  for (unsigned sp = 0; sp < (1u << 20); ++sp) {
    const v4u s = *(volatile LAS v4u *)(x.sm + word);
    if (__builtin_amdgcn_readfirstlane(min(min(s.x, s.y), min(s.z, s.w))) >= v) break;
    __builtin_amdgcn_s_sleep(1);
  }
}

#define LDA(dst, voff, soff) \
  asm volatile("buffer_load_dwordx4 %0, %1, %2, %3 offen sc1" : "=v"(dst) : "v"(voff), "s"(x.rs), "s"(soff) : "memory")
#define LDA2(dst, voff, soff, imm) \
  asm volatile("buffer_load_dwordx2 %0, %1, %2, %3 offen offset:%4 sc1" : "=v"(dst) : "v"(voff), "s"(x.rs), "s"(soff), "i"(imm) : "memory")
#define WAITV1(n, r) asm volatile("s_waitcnt vmcnt(%1)" : "+v"(r) : "i"(n) : "memory")
#define WAITV2(n, r, q) asm volatile("s_waitcnt vmcnt(%2)" : "+v"(r), "+v"(q) : "i"(n) : "memory")
#define TIE(r) asm volatile("" : "+v"(r) : : "memory")
DI bool giveup(Ctx &x) {
  if (++x.spins > SPIN_CAP) {
    if (x.lane == 0) __hip_atomic_store(x.ctl + OFF_ABORT / 4, 1u, RLX_AGENT);
    return true;
  }
  __builtin_amdgcn_s_sleep(1);
  return false;
}
DI v4u load_sure(Ctx &x, unsigned voff, unsigned soff) {
  v4u v;
  while (true) {
    asm volatile("buffer_load_dwordx4 %0, %1, %2, %3 offen sc1\n\ts_waitcnt vmcnt(0)" : "=&v"(v) : "v"(voff), "s"(x.rs), "s"(soff) : "memory");
    if (!anybad(max4(v)) || giveup(x)) break;
  }
  return v;
}
DI v2u load_sure2(Ctx &x, unsigned voff, unsigned soff) {
  v2u v;
  while (true) {
    asm volatile("buffer_load_dwordx2 %0, %1, %2, %3 offen sc1\n\ts_waitcnt vmcnt(0)" : "=&v"(v) : "v"(voff), "s"(x.rs), "s"(soff) : "memory");
    if (!anybad(max(v.x, v.y)) || giveup(x)) break;
  }
  return v;
}

DI void warm(Ctx &x, unsigned voff, unsigned soff, unsigned soff2) {
}
#define LDC(dst, voff, soff) \
  asm volatile("buffer_load_dword %0, %1, %2, %3 offen sc1" : "=v"(dst) : "v"(voff), "s"(x.rs), "s"(soff) : "memory")

#define GBAR() do {} while (0)
#define SLEEP(n) sleepn(n)

DI void put_stats(const Ctx &x, v4u u, v4u v, unsigned sbuf) {
  const f4 a = ulo(u), b = uhi(u), c = ulo(v), d = uhi(v);
  const f4 s = (a + b) + (c + d), q = (a * a + b * b) + (c * c + d * d);
  float s1 = (s.x + s.y) + (s.z + s.w), s2 = (q.x + q.y) + (q.z + q.w);
  xred2<false>(s1, s2);
  if (x.g == 0) stg2(x.rs, (unsigned)x.tok * 8u, sbuf + (unsigned)(x.T * 8 + x.c) * 128u, v2u{clampf(s1), clampf(s2)});
}
struct Stats { v2u v[2]; };
DI void stats_load(const Ctx &x, Stats &s, unsigned soffT) {
  const unsigned vo = (unsigned)(x.g * 256 + x.tok * 8);
  LDA2(s.v[0], vo, soffT, 0); LDA2(s.v[1], vo, soffT, 128);
}
DI void stats_fin(Ctx &x, Stats &s, unsigned soffT, float &mean, float &var) {
  TIE(s.v[0]); TIE(s.v[1]);
  if (__builtin_expect(anybad(max(max(s.v[0].x, s.v[0].y), max(s.v[1].x, s.v[1].y))), 0)) {
    s.v[0] = load_sure2(x, (unsigned)(x.g * 256 + x.tok * 8), soffT);
    s.v[1] = load_sure2(x, (unsigned)(x.g * 256 + x.tok * 8 + 128), soffT);
  }
  float s1 = asf(s.v[0].x) + asf(s.v[1].x), s2 = asf(s.v[0].y) + asf(s.v[1].y);
  xred2<false>(s1, s2);
  mean = s1 * (1.0f / 512.0f);
  var = fmaxf(__builtin_fmaf(-mean, mean, s2 * (1.0f / 512.0f)), 0.0f);
}
DI const LAS v4u *lds4(const Ctx &x, unsigned off) { return (const LAS v4u *)(x.sm + off); }
template <int NT, int P> DI void ldaN(const Ctx &x, v4u (&a)[NT]) {
#pragma unroll
  for (int f = 0; f < NT; ++f) {
    const int slot = (P + f) % RING;
    a[f] = *(const LAS v4u *)(x.ab[slot / 60] + (slot % 60) * KB);
  }
}
template <int NT> DI void tieN(v4u (&a)[NT]) {
  if constexpr (NT == 4) asm volatile("" : "+v"(a[0]), "+v"(a[1]), "+v"(a[2]), "+v"(a[3]));
  else if constexpr (NT == 6) asm volatile("" : "+v"(a[0]), "+v"(a[1]), "+v"(a[2]), "+v"(a[3]), "+v"(a[4]), "+v"(a[5]));
  else asm volatile("" : "+v"(a[0]), "+v"(a[1]), "+v"(a[2]), "+v"(a[3]), "+v"(a[4]), "+v"(a[5]), "+v"(a[6]));
}
DI unsigned mx4(unsigned m, v4u v) {
  unsigned r;
  asm("v_max3_u32 %0, %1, %2, %3" : "=v"(r) : "v"(m), "v"(v.x), "v"(v.y));
  asm("v_max3_u32 %0, %1, %2, %3" : "=v"(m) : "v"(r), "v"(v.z), "v"(v.w));
  return m;
}
DI f4 ldcs(const Ctx &x, int tile) { return *(const LAS f4 *)(x.sm + M_CS + (unsigned)tile * 64u + (unsigned)x.g * 16u); }
constexpr int imin(int a, int b) { return a < b ? a : b; }

template <bool SH> DI unsigned vof(const Ctx &x, int k, const unsigned (&vo)[4]) { return SH ? vo[k >> 2] : x.lane16; }
template <int NK> constexpr int depth1() { return 8; }
template <int POS, int NK, bool SH, int K0, int K1, int I> struct KL1 {
  static DI void run(Ctx &x, v4u (&bf)[NK], f4 (&acc)[4], unsigned &mx, v4u (&cur)[4], v4u (&nxt)[4], unsigned so, const unsigned (&vo)[4],
                     unsigned sig) {
    v4u nn[4];
    constexpr int DP = depth1<NK>();
    WAITV1(imin(DP - 1, NK - 1 - I), bf[I]);
    if constexpr (I + DP < NK) LDA(bf[I + DP], vof<SH>(x, I + DP, vo), so + (unsigned)(I + DP) * KB);
    if constexpr (I + DP == NK - 1) { if (sig) sig_set(x, sig); }
    tieN<4>(cur);
    SB();
    if constexpr (I + 2 < K1) ldaN<4, POS + 4 * (I + 2)>(x, nn);
    mx = mx4(mx, bf[I]);
#pragma unroll
    for (int f = 0; f < 4; ++f) acc[f] = mfma32(cur[f], bf[I], acc[f]);
#pragma unroll
    for (int f = 0; f < 4; ++f) {
      __builtin_amdgcn_sched_group_barrier(0x008, 1, 0);
      __builtin_amdgcn_sched_group_barrier(0x100, 1, 0);
      __builtin_amdgcn_sched_group_barrier(0x002, 1, 0);
    }
    SB();
    if constexpr (I + 1 < K1) KL1<POS, NK, SH, K0, K1, I + 1>::run(x, bf, acc, mx, nxt, nn, so, vo, sig);
  }
};
template <int POS, bool SH, int K1, int I> struct KL1R {
  static DI void run(Ctx &x, f4 (&acc)[4], unsigned so, const unsigned (&vo)[4]) {
    const v4u bv = load_sure(x, vof<SH>(x, I, vo), so + (unsigned)I * KB);
    v4u a[4];
    ldaN<4, POS + 4 * I>(x, a);
#pragma unroll
    for (int f = 0; f < 4; ++f) acc[f] = mfma32(a[f], bv, acc[f]);
    if constexpr (I + 1 < K1) KL1R<POS, SH, K1, I + 1>::run(x, acc, so, vo);
  }
};
template <bool ST> DI unsigned canary_poll1(Ctx &x, unsigned so, unsigned cvo, Stats &st, unsigned sst) {
  unsigned cn;
  LDC(cn, cvo, so);
  if constexpr (ST) stats_load(x, st, sst);
  WAITV1(0, cn);
  if constexpr (ST) { TIE(st.v[0]); TIE(st.v[1]); cn = max(cn, max(max(st.v[0].x, st.v[0].y), max(st.v[1].x, st.v[1].y))); }
  return cn;
}
template <int NK, bool SH, bool ST>
DI void pro1(Ctx &x, v4u (&bf)[NK], unsigned so, const unsigned (&vo)[4], unsigned cvo, Stats &st, unsigned sst, unsigned &cn) {
  while (anybad(canary_poll1<ST>(x, so, cvo, st, sst)) && !giveup(x)) {}
  cn = 0;
#pragma unroll
  for (int i = 0; i < depth1<NK>(); ++i) LDA(bf[i], vof<SH>(x, i, vo), so + (unsigned)i * KB);
}
template <int NK, bool SH, bool ST>
DI void chk1(Ctx &x, v4u (&bf)[NK], unsigned so, const unsigned (&vo)[4], unsigned cvo, Stats &st, unsigned sst, unsigned cn) {
}
template <int POS, int NK, bool SH, int K0, int K1>
DI void kloop1(Ctx &x, v4u (&bf)[NK], f4 (&acc)[4], unsigned so, const unsigned (&vo)[4], unsigned sig) {
  f4 save[4];
#pragma unroll
  for (int f = 0; f < 4; ++f) save[f] = acc[f];
  unsigned mx = 0;
  v4u a0[4], a1[4];
  ldaN<4, POS + 4 * K0>(x, a0);
  ldaN<4, POS + 4 * (K0 + 1)>(x, a1);
  KL1<POS, NK, SH, K0, K1, K0>::run(x, bf, acc, mx, a0, a1, so, vo, sig);
  if (__builtin_expect(anybad(mx), 0)) {
#pragma unroll
    for (int f = 0; f < 4; ++f) acc[f] = save[f];
    KL1R<POS, SH, K1, K0>::run(x, acc, so, vo);
  }
}
template <int NT, int POS, int K0, int K1, int I> struct KL2 {
  static DI void run(Ctx &x, v4u (&bf)[16][2], f4 (&acc)[NT][2], unsigned &mx, v4u (&cur)[NT], unsigned so, unsigned sig) {
    v4u nxt[NT];
    WAITV2(2 * imin(3, 15 - I), bf[I][0], bf[I][1]);
    if constexpr (I + 4 < 16) {
      LDA(bf[I + 4][0], x.lane16, so + (unsigned)(I + 4) * KB); LDA(bf[I + 4][1], x.lane16, so + (unsigned)(16 + I + 4) * KB);
    }
    if constexpr (I == (NT == 6 ? 8 : 11)) sig_set(x, sig);
    tieN<NT>(cur);
    SB();
    if constexpr (I + 1 < K1) ldaN<NT, POS + (I + 1) * NT>(x, nxt);
    mx = mx4(mx4(mx, bf[I][0]), bf[I][1]);
#pragma unroll
    for (int f = 0; f < NT; ++f) {
      acc[f][0] = mfma32(cur[f], bf[I][0], acc[f][0]);
      acc[f][1] = mfma32(cur[f], bf[I][1], acc[f][1]);
    }
#pragma unroll
    for (int f = 0; f < NT; ++f) {
      __builtin_amdgcn_sched_group_barrier(0x008, 1, 0);
      __builtin_amdgcn_sched_group_barrier(0x100, 1, 0);
    }
#pragma unroll
    for (int f = 0; f < NT; ++f) {
      __builtin_amdgcn_sched_group_barrier(0x008, 1, 0);
      __builtin_amdgcn_sched_group_barrier(0x002, 1, 0);
    }
    SB();
    if constexpr (I + 1 < K1) KL2<NT, POS, K0, K1, I + 1>::run(x, bf, acc, mx, nxt, so, sig);
  }
};
template <int NT, int POS, int K1, int I> struct KL2R {
  static DI void run(Ctx &x, f4 (&acc)[NT][2], unsigned so) {
    const v4u b0 = load_sure(x, x.lane16, so + (unsigned)I * KB), b1 = load_sure(x, x.lane16, so + (unsigned)(16 + I) * KB);
    v4u a[NT];
    ldaN<NT, POS + I * NT>(x, a);
#pragma unroll
    for (int f = 0; f < NT; ++f) {
      acc[f][0] = mfma32(a[f], b0, acc[f][0]);
      acc[f][1] = mfma32(a[f], b1, acc[f][1]);
    }
    if constexpr (I + 1 < K1) KL2R<NT, POS, K1, I + 1>::run(x, acc, so);
  }
};
DI unsigned stmax(const Stats (&st)[2]) {
  return max(max(max(st[0].v[0].x, st[0].v[0].y), max(st[0].v[1].x, st[0].v[1].y)), max(max(st[1].v[0].x, st[1].v[0].y), max(st[1].v[1].x, st[1].v[1].y)));
}
DI void poll2(Ctx &x, Stats (&st)[2], unsigned so, unsigned sst) {
  const unsigned cvo = (unsigned)(x.lane & 31) * KB + 1020u;
  while (true) {
    unsigned cn;
    LDC(cn, cvo, so);
    stats_load(x, st[0], sst); stats_load(x, st[1], sst + 1024u);
    WAITV1(0, cn);
    TIE(st[0].v[0]); TIE(st[0].v[1]); TIE(st[1].v[0]); TIE(st[1].v[1]);
    if (!anybad(max(cn, stmax(st))) || giveup(x)) break;
  }
}
DI void first2(Ctx &x, v4u (&bf)[16][2], unsigned so) {
#pragma unroll
  for (int i = 0; i < 4; ++i) { LDA(bf[i][0], x.lane16, so + (unsigned)i * KB); LDA(bf[i][1], x.lane16, so + (unsigned)(16 + i) * KB); }
}
DI void pro2(Ctx &x, v4u (&bf)[16][2], Stats (&st)[2], unsigned so, unsigned sst, unsigned &cn) {
  poll2(x, st, so, sst);
  cn = 0;
  first2(x, bf, so);
}
DI void chk2(Ctx &x, v4u (&bf)[16][2], Stats (&st)[2], unsigned so, unsigned sst, unsigned cn) {
}
template <int NT, int POS, int K0, int K1>
DI void kloop2(Ctx &x, v4u (&bf)[16][2], f4 (&acc)[NT][2], unsigned so, unsigned sig, unsigned &mx) {
  v4u a0[NT];
  ldaN<NT, POS + K0 * NT>(x, a0);
  KL2<NT, POS, K0, K1, K0>::run(x, bf, acc, mx, a0, so, sig);
}
template <int NT, int POS> DI void redo2(Ctx &x, f4 (&acc)[NT][2], unsigned so) {
#pragma unroll
  for (int f = 0; f < NT; ++f) { acc[f][0] = f4{0.f, 0.f, 0.f, 0.f}; acc[f][1] = f4{0.f, 0.f, 0.f, 0.f}; }
  KL2R<NT, POS, 16, 0>::run(x, acc, so);
}

DI void featmap4(const f4 (&v)[4], const v2u (&wphi)[2], v2u (&plus)[4], v2u (&minus)[4]) {
  const float L2E = 1.4426950408889634f;
  f4 p[4];
#pragma unroll
  for (int i = 0; i < 4; ++i) p[i] = mfma16(wphi[i & 1], v2u{pk2(v[i].x, v[i].y), pk2(v[i].z, v[i].w)}, f4{0.f, 0.f, 0.f, 0.f});
  float mx[4], nm[4];
#pragma unroll
  for (int i = 0; i < 4; ++i) {
    p[i] *= L2E;
    mx[i] = fmaxf(fmaxf(p[i].x, p[i].y), fmaxf(p[i].z, p[i].w));
    nm[i] = -fminf(fminf(p[i].x, p[i].y), fminf(p[i].z, p[i].w));
  }
  xred4<true>(mx[0], mx[1], mx[2], mx[3]);
  xred4<true>(nm[0], nm[1], nm[2], nm[3]);
  f4 e[4], n[4];
  float se[4], sn[4];
#pragma unroll
  for (int i = 0; i < 4; ++i) {
    const f4 a = p[i] - mx[i], b = -p[i] - nm[i];
    e[i] = f4{__builtin_amdgcn_exp2f(a.x), __builtin_amdgcn_exp2f(a.y), __builtin_amdgcn_exp2f(a.z), __builtin_amdgcn_exp2f(a.w)};
    n[i] = f4{__builtin_amdgcn_exp2f(b.x), __builtin_amdgcn_exp2f(b.y), __builtin_amdgcn_exp2f(b.z), __builtin_amdgcn_exp2f(b.w)};
    se[i] = (e[i].x + e[i].y) + (e[i].z + e[i].w);
    sn[i] = (n[i].x + n[i].y) + (n[i].z + n[i].w);
  }
  xred4<false>(se[0], se[1], se[2], se[3]);
  xred4<false>(sn[0], sn[1], sn[2], sn[3]);
#pragma unroll
  for (int i = 0; i < 4; ++i) {
    const f4 a = e[i] * __builtin_amdgcn_rcpf(se[i]), b = n[i] * __builtin_amdgcn_rcpf(sn[i]);
    plus[i] = v2u{pk2(a.x, a.y), pk2(a.z, a.w)};
    minus[i] = v2u{pk2(b.x, b.y), pk2(b.z, b.w)};
  }
}
DI v2u trread(const Ctx &x, unsigned off) {
  s4 r = __builtin_amdgcn_ds_read_tr16_b64_v4i16((LAS s4 *)(x.sm + off));
  return __builtin_bit_cast(v2u, r);
}
DI f4 shsum(f4 s) {
#pragma unroll
  for (int o = 4; o < 64; o <<= 1) {
    s.x += __shfl_xor(s.x, o); s.y += __shfl_xor(s.y, o); s.z += __shfl_xor(s.z, o); s.w += __shfl_xor(s.w, o);
  }
  return s;
}

DI void poll_flags(Ctx &x, unsigned off, unsigned tag) {
  for (unsigned sp = 0;; ++sp) {
    unsigned f0, f1, f2, f3;
    const unsigned vo = (unsigned)x.lane * 64u;
    LDC(f0, vo, off); LDC(f1, vo, off + 4096u); LDC(f2, vo, off + 8192u); LDC(f3, vo, off + 12288u);
    asm volatile("s_waitcnt vmcnt(0)" : "+v"(f0), "+v"(f1), "+v"(f2), "+v"(f3)::"memory");
    if (__all(f0 == tag && f1 == tag && f2 == tag && f3 == tag)) break;
    if (sp > SPIN_CAP) {
      if (x.lane == 0) __hip_atomic_store(x.ctl + OFF_ABORT / 4, 3u, RLX_AGENT);
      break;
    }
    __builtin_amdgcn_s_sleep(8);
  }
}
DI unsigned sigv(int L, int ph) { return (unsigned)(L * 8 + ph + 1); }

DI void compute_path(Ctx &x, const Params &p, v4u (&xres)[2], const v2u (&wph)[2], unsigned tag) {
  const unsigned vz[4] = {0u, 0u, 0u, 0u};
  v4u x1r[2], x2r[2];
#pragma unroll 1
  for (int L = 0; L < 2; ++L) {
    const unsigned lb0 = OFF_L0 + (unsigned)L * L_STRIDE;
    v4u phq[2][2];
    GBAR();
    {
      refresh(x);
      FS(0);
      unsigned lb = lb0;
      asm volatile("" : "+s"(lb));
      v4u bf[16][2];
      Stats st[2];
      const unsigned so = lb + O_XB + (unsigned)x.TA * 16u * KB, sst = lb + O_XS + (unsigned)x.TA * 1024u;
      unsigned cn;
      pro2(x, bf, st, so, sst, cn);
      FS(1); FSP(0);
      BAR();
      if (L == 0 && threadIdx.x == 0) __hip_atomic_store(x.ctl + OFF_FLAGS2 / 4 + x.b * 16, tag, RLX_AGENT);
      chk2(x, bf, st, so, sst, cn);
      FS(2);
      f4 acc[6][2];
#pragma unroll
      for (int f = 0; f < 6; ++f) { acc[f][0] = f4{0.f, 0.f, 0.f, 0.f}; acc[f][1] = f4{0.f, 0.f, 0.f, 0.f}; }
      unsigned mx = 0;
      kloop2<6, POS1, 0, 16>(x, bf, acc, so, sigv(L, 0), mx);
      if (__builtin_expect(anybad(mx), 0)) redo2<6, POS1>(x, acc, so);
      FS(3);
      BAR();
      MARK("P1epi");
      f4 val[2][6];
#pragma unroll
      for (int j = 0; j < 2; ++j) {
        float mean, var;
        stats_fin(x, st[j], sst + (unsigned)j * 1024u, mean, var);
        const float rstd = __builtin_amdgcn_rsqf(var + 1e-5f), mr = mean * rstd;
#pragma unroll
        for (int f = 0; f < 6; ++f) val[j][f] = acc[f][j] * rstd - ldcs(x, f) * mr;
      }
      {
        v2u pl[4], mi[4];
        const f4 kk[4] = {val[0][2], val[0][3], val[1][2], val[1][3]};
        featmap4(kk, wph, pl, mi);
#pragma unroll
        for (int i = 0; i < 4; ++i) {
          const int j = i >> 1, h = i & 1;
          const unsigned img = IMG + (unsigned)((2 * x.m + j) * 16 + x.tok) * 32u + (unsigned)x.g * 8u + (unsigned)(h * 3) * 4096u;
          *(LAS v2u *)(x.sm + img) = pl[i];
          *(LAS v2u *)(x.sm + img + 4096u) = mi[i];
          *(LAS v2u *)(x.sm + img + 8192u) = v2u{pk2(val[j][4 + h].x, val[j][4 + h].y), pk2(val[j][4 + h].z, val[j][4 + h].w)};
        }
      }
      MARK("P1epiend");
      FS(4);
      BAR();
      FS(5);
      {
        v2u pl[4], mi[4];
        TIE(val[0][0]); TIE(val[0][1]); TIE(val[1][0]); TIE(val[1][1]);
        const f4 qq[4] = {val[0][0], val[0][1], val[1][0], val[1][1]};
        featmap4(qq, wph, pl, mi);
#pragma unroll
        for (int i = 0; i < 4; ++i) phq[i >> 1][i & 1] = v4u{pl[i].x, pl[i].y, mi[i].x, mi[i].y};
      }
      FS(6);
    }
    GBAR();
    GBAR();
    {
      refresh(x);
      unsigned lb = lb0;
      asm volatile("" : "+s"(lb));
      BAR();
      FS(7);
      v4u ka[2], za[2];
#pragma unroll
      for (int h = 0; h < 2; ++h) {
        ka[h] = *lds4(x, M_KVA + (unsigned)h * 1024u + x.lane16);
        const v2u z0 = *(const LAS v2u *)(x.sm + M_ZA + (unsigned)(h * 2) * 32u + (unsigned)x.g * 8u);
        const v2u z1 = *(const LAS v2u *)(x.sm + M_ZA + (unsigned)(h * 2 + 1) * 32u + (unsigned)x.g * 8u);
        za[h] = v4u{z0.x, z0.y, z1.x, z1.y};
      }
#pragma unroll
      for (int j = 0; j < 2; ++j) {
        f4 o[2];
#pragma unroll
        for (int h = 0; h < 2; ++h) {
          const f4 num = mfma32(ka[h], phq[j][h], f4{0.f, 0.f, 0.f, 0.f});
          const f4 den = mfma32(za[h], phq[j][h], f4{0.f, 0.f, 0.f, 0.f});
          o[h] = num * __builtin_amdgcn_rcpf(den.x + 1e-6f);
        }
        stg(x.rs, x.lane16, lb + O_OB + (unsigned)((x.TA + j) * 16 + x.pp) * KB, pack8(o[0], o[1]));
      }
      FS(8);
      {
        const unsigned ob = lb + O_OB + (unsigned)(x.TA * 16 + x.pp) * KB;
        warm(x, (unsigned)((x.lane >> 3) & 1) * 16u * KB + (unsigned)(x.lane & 7) * 128u, ob, ob);
      }
    }
    GBAR();
    {
      refresh(x);
      unsigned lb = lb0;
      asm volatile("" : "+s"(lb));
      SLEEP(DLY_2);
      FS(9);
      v4u bf[16];
      const unsigned so = lb + O_OB + (unsigned)x.T * 16u * KB;
      Stats st;
      unsigned cn;
      const unsigned cvo = (unsigned)(x.lane & 15) * KB + 1020u;
      pro1<16, false, false>(x, bf, so, vz, cvo, st, 0u, cn);
      FS(10); FSP(1);
      BAR();
      chk1<16, false, false>(x, bf, so, vz, cvo, st, 0u, cn);
      FS(11);
      f4 acc[4];
#pragma unroll
      for (int f = 0; f < 4; ++f) acc[f] = f4{0.f, 0.f, 0.f, 0.f};
      kloop1<POS2, 16, false, 0, 16>(x, bf, acc, so, vz, sigv(L, 2));
      FS(12);
      x1r[0] = pack8(ulo(xres[0]) + acc[0], uhi(xres[0]) + acc[1]);
      x1r[1] = pack8(ulo(xres[1]) + acc[2], uhi(xres[1]) + acc[3]);
      const unsigned fo = lb + O_X1B + (unsigned)(x.T * 16 + 2 * x.c) * KB;
      stg(x.rs, x.lane16, fo, x1r[0]); stg(x.rs, x.lane16, fo + KB, x1r[1]);
      put_stats(x, x1r[0], x1r[1], lb + O_X1S);
      sig_set(x, sigv(L, 2), M_SIG2);
      FS(13);
      warm(x, (unsigned)(x.lane & 15) * 128u, fo, lb + O_X1S + (unsigned)(x.T * 8 + x.c) * 128u);
    }
    float mean1, rstd1;
    GBAR();
    {
      refresh(x);
      unsigned lb = lb0;
      asm volatile("" : "+s"(lb));
      SLEEP(DLY_3);
      v4u bf[16];
      Stats st;
      const unsigned so = lb + O_X1B + (unsigned)x.T * 16u * KB;
      unsigned cn;
      const unsigned cvo = (unsigned)(x.lane & 15) * KB + 1020u;
      pro1<16, false, true>(x, bf, so, vz, cvo, st, lb + O_X1S + (unsigned)x.T * 1024u, cn);
      FSP(2);
      BAR();
      chk1<16, false, true>(x, bf, so, vz, cvo, st, lb + O_X1S + (unsigned)x.T * 1024u, cn);
      FS(14);
      f4 acc[4];
#pragma unroll
      for (int f = 0; f < 4; ++f) acc[f] = f4{0.f, 0.f, 0.f, 0.f};
      kloop1<POS3, 16, false, 0, 16>(x, bf, acc, so, vz, sigv(L, 3));
      FS(15);
      float var;
      stats_fin(x, st, lb + O_X1S + (unsigned)x.T * 1024u, mean1, var);
      rstd1 = __builtin_amdgcn_rsqf(var + 1e-5f);
      const float a = rstd1 * __builtin_amdgcn_rsqf(__builtin_fmaf(var * rstd1, rstd1, 1e-5f)), ma = mean1 * a;
      f4 y[4];
#pragma unroll
      for (int f = 0; f < 4; ++f) y[f] = gelu4(acc[f] * a - ldcs(x, 6 + f) * ma);
      const unsigned fo = lb + O_HB + (unsigned)(x.T * 16 + 2 * x.c) * KB;
      stg(x.rs, x.lane16, fo, pack8(y[0], y[1])); stg(x.rs, x.lane16, fo + KB, pack8(y[2], y[3]));
      sig_set(x, sigv(L, 3), M_SIG2);
      FS(16);
      warm(x, (unsigned)(x.lane & 15) * 128u, fo, fo);
    }
    GBAR();
    {
      refresh(x);
      unsigned lb = lb0;
      asm volatile("" : "+s"(lb));
      SLEEP(DLY_4);
      v4u bf[16];
      unsigned vo[4];
#pragma unroll
      for (int q = 0; q < 4; ++q) {
        const int d = q == 0 ? -1 : (q == 1 ? 1 : (q == 2 ? -2 : 2));
        const int src = 16 * x.T + x.tok + d;
        vo[q] = (src >= 0 && src < 2048) ? (unsigned)((src >> 4) * 16 * 1024 + (x.g * 16 + (src & 15)) * 16) : OOB;
      }
      const unsigned so = lb + O_HB;
      unsigned cvo;
      {
        const int s_ = x.lane & 15, nb = x.T + (((s_ >> 2) & 1) ? 1 : -1);
        const int tt = (x.lane & 16) ? nb : x.T;
        cvo = (tt >= 0 && tt < 128) ? (unsigned)(tt * 16 + s_) * KB + 1020u : OOB;
      }
      Stats st;
      unsigned cn;
      pro1<16, true, false>(x, bf, so, vo, cvo, st, 0u, cn);
      FSP(3);
      BAR();
      chk1<16, true, false>(x, bf, so, vo, cvo, st, 0u, cn);
      FS(17);
      f4 acc[4];
#pragma unroll
      for (int f = 0; f < 4; ++f) acc[f] = f4{0.f, 0.f, 0.f, 0.f};
      MARK("P4loop");
      kloop1<POS4, 16, true, 0, 16>(x, bf, acc, so, vo, sigv(L, 4));
      MARK("P4end");
      FS(18);
      const float mr = mean1 * rstd1;
      x2r[0] = pack8(ulo(x1r[0]) * rstd1 - mr + acc[0], uhi(x1r[0]) * rstd1 - mr + acc[1]);
      x2r[1] = pack8(ulo(x1r[1]) * rstd1 - mr + acc[2], uhi(x1r[1]) * rstd1 - mr + acc[3]);
      const unsigned fo = lb + O_X2B + (unsigned)(x.T * 16 + 2 * x.c) * KB;
      stg(x.rs, x.lane16, fo, x2r[0]); stg(x.rs, x.lane16, fo + KB, x2r[1]);
      put_stats(x, x2r[0], x2r[1], lb + O_X2S);
      sig_set(x, sigv(L, 4), M_SIG2);
      FS(19);
      warm(x, (unsigned)(x.lane & 15) * 128u, fo, lb + O_X2S + (unsigned)(x.T * 8 + x.c) * 128u);
    }
    GBAR();
    {
      refresh(x);
      unsigned lb = lb0;
      asm volatile("" : "+s"(lb));
      SLEEP(DLY_5);
      v4u bf[16][2];
      Stats st[2];
      const unsigned so = lb + O_X2B + (unsigned)x.TA * 16u * KB, sst = lb + O_X2S + (unsigned)x.TA * 1024u;
      unsigned cn;
      pro2(x, bf, st, so, sst, cn);
      FSP(4);
      BAR();
      chk2(x, bf, st, so, sst, cn);
      FS(20);
      f4 acc[7][2];
#pragma unroll
      for (int f = 0; f < 7; ++f) { acc[f][0] = f4{0.f, 0.f, 0.f, 0.f}; acc[f][1] = f4{0.f, 0.f, 0.f, 0.f}; }
      unsigned mx = 0;
      MARK("P5loop");
      kloop2<7, POS5, 0, 13>(x, bf, acc, so, sigv(L, 5), mx);
      BAR();
      kloop2<7, POS5, 13, 16>(x, bf, acc, so, sigv(L, 5), mx);
      if (__builtin_expect(anybad(mx), 0)) redo2<7, POS5>(x, acc, so);
      FS(21);
      BAR();
      FS(22);
      MARK("P5epi");
      const int u = x.pp >> 1, odd = x.pp & 1;
#pragma unroll
      for (int j = 0; j < 2; ++j) {
        float mean, var;
        stats_fin(x, st[j], sst + (unsigned)j * 1024u, mean, var);
        const float rstd = __builtin_amdgcn_rsqf(var + 1e-5f), mr = mean * rstd;
        const unsigned hb = lb + O_H3B + (unsigned)((x.TA + j) * 56 + 7 * u) * KB;
#pragma unroll
        for (int r = 0; r < 3; ++r) {
          const f4 y0 = gelu4(acc[2 * r][j] * rstd - ldcs(x, 10 + 2 * r) * mr), y1 = gelu4(acc[2 * r + 1][j] * rstd - ldcs(x, 11 + 2 * r) * mr);
          stg(x.rs, x.lane16, hb + (unsigned)(odd ? 4 + r : r) * KB, pack8(y0, y1));
        }
        const f4 y = gelu4(acc[6][j] * rstd - ldcs(x, 16) * mr);
        stg2(x.rs, x.lane16 + (unsigned)odd * 8u, hb + 3 * KB, v2u{clampw(pk2(y.x, y.y)), clampw(pk2(y.z, y.w))});
      }
      FS(23);
      {
        const unsigned hb0 = lb + O_H3B + (unsigned)(x.TA * 56 + 7 * u + (odd ? 3 : 0)) * KB;
        warm(x, (unsigned)(x.lane >> 5) * 56u * KB + (unsigned)(x.lane & 31) * 128u, hb0, hb0);
      }
    }
    GBAR();
    {
      refresh(x);
      unsigned lb = lb0;
      asm volatile("" : "+s"(lb));
      SLEEP(DLY_6);
      v4u hf[56];
      const unsigned so = lb + O_H3B + (unsigned)x.T * 56u * KB;
      Stats st;
      unsigned cn;
      const unsigned cvo = (unsigned)min(x.lane, 55) * KB + 1020u;
      pro1<56, false, false>(x, hf, so, vz, cvo, st, 0u, cn);
      FSP(5);
      BAR();
      chk1<56, false, false>(x, hf, so, vz, cvo, st, 0u, cn);
      FS(24);
      f4 acc[4];
#pragma unroll
      for (int f = 0; f < 4; ++f) acc[f] = f4{0.f, 0.f, 0.f, 0.f};
      kloop1<POS6, 56, false, 0, 17>(x, hf, acc, so, vz, 0u);
      BAR();
      FS(25);
      kloop1<POS6, 56, false, 17, 39>(x, hf, acc, so, vz, 0u);
      BAR();
      FS(26);
      kloop1<POS6, 56, false, 39, 56>(x, hf, acc, so, vz, 0u);
      FS(27);
      BAR();
      xres[0] = pack8(ulo(x1r[0]) + (ulo(x2r[0]) + acc[0]), uhi(x1r[0]) + (uhi(x2r[0]) + acc[1]));
      xres[1] = pack8(ulo(x1r[1]) + (ulo(x2r[1]) + acc[2]), uhi(x1r[1]) + (uhi(x2r[1]) + acc[3]));
      if (L == 0) {
        const unsigned fo = lb + L_STRIDE + O_XB + (unsigned)(x.T * 16 + 2 * x.c) * KB;
        stg(x.rs, x.lane16, fo, xres[0]); stg(x.rs, x.lane16, fo + KB, xres[1]);
        put_stats(x, xres[0], xres[1], lb + L_STRIDE + O_XS);
        warm(x, (unsigned)(x.lane & 15) * 128u, fo, lb + L_STRIDE + O_XS + (unsigned)(x.T * 8 + x.c) * 128u);
        SLEEP(DLY_1);
      } else {
        GAS unsigned char *op = (GAS unsigned char *)p.out + ((size_t)(16 * x.T + x.tok) * 512 + 64 * x.c + 4 * x.g) * 2;
        *(GAS v2u *)op = v2u{xres[0].x, xres[0].y};
        *(GAS v2u *)(op + 32) = v2u{xres[0].z, xres[0].w};
        *(GAS v2u *)(op + 64) = v2u{xres[1].x, xres[1].y};
        *(GAS v2u *)(op + 96) = v2u{xres[1].z, xres[1].w};
      }
      FS(28); FSP(6);
    }
  }
}

DI void poison_h3(Ctx &x, unsigned lb, int j) {
  const int u = x.pp >> 1, odd = x.pp & 1;
  const unsigned hb = lb + O_H3B + (unsigned)((x.TA + j) * 56 + 7 * u) * KB;
#pragma unroll
  for (int r = 0; r < 3; ++r) poi4(x.rs, x.lane16, hb + (unsigned)(odd ? 4 + r : r) * KB);
  poi2(x.rs, x.lane16 + (unsigned)odd * 8u, hb + 3 * KB);
}
DI void helper_path(Ctx &x, unsigned tag) {
#pragma unroll 1
  for (int L = 0; L < 2; ++L) {
    const unsigned lb0 = OFF_L0 + (unsigned)L * L_STRIDE;
    GBAR();
    {
      refresh(x);
      unsigned lb = lb0;
      asm volatile("" : "+s"(lb));
      FS(0);
      DRAIN_BAR();
      FS(1);
      if (L != 0) {
        sig_wait(x, sigv(L, 0));
        dmaR<96, 156>(x);
      }
      BAR();
      {
        const unsigned fo = (unsigned)(x.T * 16 + 2 * x.c) * KB, sto = (unsigned)(x.T * 8 + x.c) * 128u;
        poi4(x.rs, x.lane16, lb + O_X1B + fo); poi4(x.rs, x.lane16, lb + O_X1B + fo + KB);
        poi4(x.rs, x.lane16, lb + O_X2B + fo); poi4(x.rs, x.lane16, lb + O_X2B + fo + KB);
        if (x.g == 0) { poi2(x.rs, (unsigned)x.tok * 8u, lb + O_X1S + sto); poi2(x.rs, (unsigned)x.tok * 8u, lb + O_X2S + sto); }
      }
      FS(2);
      BAR();
      FS(3);
      {
        const int h = x.m >> 1, sg = x.m & 1;
        const unsigned lo = (unsigned)(4 * x.g + (x.tok >> 2)) * 32u + (unsigned)(x.tok & 3) * 8u;
        const unsigned ia = IMG + (unsigned)(h * 3 + sg) * 4096u + lo, iv = IMG + (unsigned)(h * 3 + 2) * 4096u + lo;
        f4 kv = {0.f, 0.f, 0.f, 0.f}, z = {0.f, 0.f, 0.f, 0.f};
#pragma unroll
        for (int tb = 0; tb < 4; ++tb) {
          const v2u a0 = trread(x, ia + tb * 1024u), a1 = trread(x, ia + tb * 1024u + 512u);
          const v2u b0 = trread(x, iv + tb * 1024u), b1 = trread(x, iv + tb * 1024u + 512u);
          const v4u a = {a0.x, a0.y, a1.x, a1.y};
          kv = mfma32(a, v4u{b0.x, b0.y, b1.x, b1.y}, kv);
          z = mfma32(a, v4u{0x3F803F80u, 0x3F803F80u, 0x3F803F80u, 0x3F803F80u}, z);
        }
        stg2(x.rs, (unsigned)x.lane * 8u, lb + O_KVP + (unsigned)((x.pp * 16 + x.tau) * 4 + x.m) * KB, v2u{clampw(pk2(kv.x, kv.y)), clampw(pk2(kv.z, kv.w))});
        if (x.tok == 0) stg(x.rs, (unsigned)x.g * 16u, lb + O_KVZ + (unsigned)((x.pp * 4 + x.m) * 16 + x.tau) * 64u, clamp4(__builtin_bit_cast(v4u, z)));
      }
      FS(4);
    }
    GBAR();
    {
      refresh(x);
      unsigned lb = lb0;
      asm volatile("" : "+s"(lb));
      if (L == 0) {
        if (x.m == 0) {
          poll_flags(x, OFF_FLAGS2, tag);
          *(volatile LAS unsigned *)(x.sm + M_RDY) = 1u;
        } else {
          for (unsigned sp = 0; sp < (1u << 20); ++sp) {
            if (__builtin_amdgcn_readfirstlane(*(volatile LAS unsigned *)(x.sm + M_RDY))) break;
            __builtin_amdgcn_s_sleep(2);
          }
        }
        v4u cv[3];
#pragma unroll
        for (int jj = 0; jj < 3; ++jj) {
          const int u = min(6 + x.m + 4 * jj, 16);
          cv[jj] = ldg(x.rs, x.lane16, OFF_CSP + (u < 10 ? 96u + (unsigned)(x.c * 4 + u - 6) : 128u + (unsigned)(x.pp * 7 + u - 10)) * KB);
        }
#pragma unroll
        for (int jj = 0; jj < 3; ++jj) {
          const f4 s = shsum(__builtin_bit_cast(f4, cv[jj]));
          const int u = 6 + x.m + 4 * jj;
          if (u < 17 && x.lane < 4) *(LAS f4 *)(x.sm + M_CS + (unsigned)u * 64u + (unsigned)x.lane * 16u) = s;
        }
      }
      SLEEP(DLY_KV);
      v4u pv[16];
      const unsigned so = lb + O_KVP + (unsigned)(x.pp * 64 + x.m) * KB, sz = lb + O_KVZ + (unsigned)(x.pp * 4 + x.m) * KB;
      v4u zv;
      {
        const unsigned cvo = (unsigned)(x.lane & 15) * 4096u + (KV16 ? 508u : 1020u);
        while (true) {
          unsigned cn;
          LDC(cn, cvo, so);
          LDA(zv, x.lane16, sz);
          WAITV2(0, cn, zv);
          if (!anybad(max(cn, max4(zv))) || giveup(x)) break;
        }
      }
      bool again;
      f4 s;
      v2u pw[16];
      do {
        asm volatile("" ::: "memory");
#pragma unroll
        for (int i = 0; i < 16; ++i) pw[i] = __builtin_amdgcn_raw_buffer_load_b64(x.rs, (unsigned)x.lane * 8u, so + (unsigned)i * 4096u, AUX_SC1);
        unsigned mx = 0;
#pragma unroll
        for (int i = 0; i < 16; ++i) mx = max(mx, max(pw[i].x, pw[i].y));
        again = anybad(mx) && !giveup(x);
      } while (__builtin_expect(again, 0));
      s = ulo(v4u{pw[0].x, pw[0].y, 0u, 0u});
#pragma unroll
      for (int i = 1; i < 16; ++i) s += ulo(v4u{pw[i].x, pw[i].y, 0u, 0u});
      *(LAS v2u *)(x.sm + M_KVA + (unsigned)(x.m >> 1) * 1024u + x.lane16 + (unsigned)(x.m & 1) * 8u) = v2u{pk2(s.x, s.y), pk2(s.z, s.w)};
      const f4 zs = shsum(__builtin_bit_cast(f4, zv));
      if (x.lane < 4) *(LAS v2u *)(x.sm + M_ZA + (unsigned)x.m * 32u + (unsigned)x.lane * 8u) = v2u{pk2(zs.x, zs.y), pk2(zs.z, zs.w)};
      FS(5); FSP(0);
      GBAR();
      BAR();
      FS(6);
      if (L == 0) dmaR<96, 156>(x);
      dmaR<156, 188>(x);
    }
    GBAR();
    {
      refresh(x);
      unsigned lb = lb0;
      asm volatile("" : "+s"(lb));
      DRAIN_BAR();
      FS(7);
      sig_wait(x, sigv(L, 2));
      dmaR<188, 220>(x);
      sig_wait(x, sigv(L, 2), M_SIG2);
      dmaR<220, 252>(x);
      poison_h3(x, lb, 0); poison_h3(x, lb, 1);
    }
    GBAR();
    {
      refresh(x);
      unsigned lb = lb0;
      asm volatile("" : "+s"(lb));
      WAITV_BAR(8);
      FS(8);
      sig_wait(x, sigv(L, 3));
      FS(15);
      dmaR<252, 284>(x);
      sig_wait(x, sigv(L, 3), M_SIG2);
      dmaR<284, 316>(x);
      FS(16);
      if (L == 0) {
        const unsigned fo = lb + L_STRIDE + O_XB + (unsigned)(x.T * 16 + 2 * x.c) * KB;
        poi4(x.rs, x.lane16, fo); poi4(x.rs, x.lane16, fo + KB);
        if (x.g == 0) poi2(x.rs, (unsigned)x.tok * 8u, lb + L_STRIDE + O_XS + (unsigned)(x.T * 8 + x.c) * 128u);
      }
    }
    GBAR();
    {
      refresh(x);
      unsigned lb = lb0;
      asm volatile("" : "+s"(lb));
      if (L == 0) WAITV_BAR(3); else WAITV_BAR(0);
      FS(9);
      sig_wait(x, sigv(L, 4));
      FS(18);
      dmaR<316, 348>(x);
      sig_wait(x, sigv(L, 4), M_SIG2);
      dmaR<348, 380>(x);
      FS(19);
      if (L == 0) {
        const unsigned fo = lb + L_STRIDE + O_HB + (unsigned)(x.T * 16 + 2 * x.c) * KB;
        poi4(x.rs, x.lane16, fo); poi4(x.rs, x.lane16, fo + KB);
        poi4(x.rs, x.lane16, lb + L_STRIDE + O_OB + (unsigned)(x.TA * 16 + x.pp) * KB);
        poi4(x.rs, x.lane16, lb + L_STRIDE + O_OB + (unsigned)((x.TA + 1) * 16 + x.pp) * KB);
      }
    }
    GBAR();
    {
      refresh(x);
      if (L == 0) WAITV_BAR(4); else WAITV_BAR(0);
      FS(10);
      dmaRT<380, 400>(x);
      DRAIN_BAR();
      sig_wait(x, sigv(L, 5));
      dmaR<400, 444>(x);
      BAR();
      FS(11);
      dmaR6<444, 556>(x);
    }
    GBAR();
    {
      refresh(x);
      DRAIN_BAR();
      FS(12);
      BAR();
      dmaRT<556, 624>(x);
      DRAIN_BAR();
      FS(13);
      dmaR<624, 712>(x, L == 0);
      BAR();
      dmaR<712, 720>(x, L == 0);
      FS(14);
    }
  }
}

}

__global__ void __launch_bounds__(512, 2) mega_kernel(Params p) {
  extern __shared__ __attribute__((aligned(16))) unsigned char smem_[];
  Ctx x;
  x.sm = (LAS unsigned char *)smem_;
  x.lds0 = (unsigned)(size_t)smem_;
  x.lane = threadIdx.x & 63;
  x.w = __builtin_amdgcn_readfirstlane(threadIdx.x >> 6);
  x.b = blockIdx.x;
  refresh(x);
  const int b = x.b;
  x.spins = 0;
  x.ctl = (gu32 *)p.ws;
  x.rs = __builtin_amdgcn_make_buffer_rsrc((void *)p.ws, 0, WS_BYTES, 0x00020000);
  const bool cw = x.w < 4;

  FSA(0);
  const unsigned tag = __hip_atomic_load(x.ctl + OFF_EPOCH / 4, RLX_AGENT) + 1u;
  unsigned pf0 = 0, pf1 = 0;
  if (x.w == 7) {
    unsigned long long pc;
    asm volatile("s_getpc_b64 %0" : "=s"(pc));
    const GAS unsigned char *cp = (const GAS unsigned char *)(pc & ~127ull) + (size_t)(((b >> 3) + 32 * (x.lane & 15)) * 128);
    const GAS unsigned char *wp = (const GAS unsigned char *)p.ws + (size_t)min(x.lane, 28) * (2 * MB) + 1 * MB;
    if (x.lane >= 32) {
      const int q = x.lane & 7;
      const unsigned short *ip = q == 0 ? p.x : q == 1 ? p.wqkv : q == 2 ? p.wo : q == 3 ? p.w1 : q == 4 ? p.w2 : q == 5 ? p.w3 : q == 6 ? p.w4 : p.x;
      const size_t half = (q == 2 || q == 3 || q == 4) ? 256 * KB : 1 * MB;
      wp = (const GAS unsigned char *)ip + ((x.lane & 8) ? half : 0);
    }
    asm volatile("global_load_dword %0, %2, off\n\tglobal_load_dword %1, %3, off" : "=&v"(pf0), "=&v"(pf1) : "v"(cp), "v"(wp) : "memory");
    asm volatile("s_waitcnt vmcnt(0)" : "+v"(pf0), "+v"(pf1)::"memory");
  }
  v2u xin[4] = {};
  unsigned short wraw[2][4] = {};
  if (cw) {
    const GAS unsigned char *xp = (const GAS unsigned char *)p.x + ((size_t)(16 * x.T + x.tok) * 512 + 64 * x.c + 4 * x.g) * 2;
#pragma unroll
    for (int q = 0; q < 4; ++q) xin[q] = *(const GAS v2u *)(xp + 32 * q);
#pragma unroll
    for (int h = 0; h < 2; ++h) {
      const unsigned short *wp = p.wphi + ((2 * x.pp + h) * 16 + 4 * x.g) * 16 + x.tok;
#pragma unroll
      for (int jj = 0; jj < 4; ++jj) wraw[h][jj] = wp[16 * jj];
    }
  }
  if (threadIdx.x < 9) *(LAS unsigned *)(x.sm + M_SIG + threadIdx.x * 4u) = 0u;
  const int bid = ((x.w + 4) & 7) * 256 + b;
  const unsigned short *src = p.wqkv; int ld = 1536, ra = 0, rb = 0, n0 = 0, kstep = 0, csid = -1; unsigned fr[4] = {0u, 0u, 0u, 0u}, ci[4] = {0u, 0u, 0u, 0u};
  if (bid < 384) {
    kstep = bid & 15; const int nb = bid >> 4; n0 = nb * 64; ra = 32 * kstep; rb = ra + 16;
    const int sec = nb >> 3, h0 = 4 * (nb & 7);
#pragma unroll
    for (int ct = 0; ct < 4; ++ct) {
      const int hh = h0 + ct;
      fr[ct] = W1_F + (unsigned)(((hh >> 1) * 16 + kstep) * 6 + sec * 2 + (hh & 1));
      ci[ct] = (unsigned)((hh >> 1) * 6 + sec * 2 + (hh & 1));
    }
    csid = 0;
  } else if (bid < 768) {
    const int mi = (bid - 384) >> 7, r = (bid - 384) & 127; kstep = r & 15; const int nb = r >> 4;
    src = mi == 0 ? p.wo : (mi == 1 ? p.w1 : p.w2); ld = 512; n0 = nb * 64; ra = 32 * kstep; rb = ra + 16;
#pragma unroll
    for (int ct = 0; ct < 4; ++ct) { fr[ct] = W2_F + (unsigned)(mi * 512 + (nb * 16 + kstep) * 4 + ct); ci[ct] = 96u + (unsigned)(nb * 4 + ct); }
    if (mi == 1) csid = 1;
  } else if (bid < 1216) {
    const int r = bid - 768; kstep = r & 15; const int nb = r >> 4; src = p.w3; ld = 1792; n0 = nb * 64; ra = 32 * kstep; rb = ra + 16;
#pragma unroll
    for (int ct = 0; ct < 4; ++ct) { const int F = nb * 4 + ct; fr[ct] = W5_F + (unsigned)(((F / 7) * 16 + kstep) * 7 + F % 7); ci[ct] = 128u + (unsigned)F; }
    csid = 2;
  } else if (bid < 1664) {
    const int r = bid - 1216; const int nb = r & 7; kstep = r >> 3; src = p.w4; ld = 512; n0 = nb * 64;
    const int u = kstep / 7, q = kstep % 7;
    if (q < 3) { ra = 224 * u + 32 * q; rb = ra + 16; }
    else if (q == 3) { ra = 224 * u + 96; rb = 224 * u + 208; }
    else { ra = 224 * u + 112 + 32 * (q - 4); rb = ra + 16; }
#pragma unroll
    for (int ct = 0; ct < 4; ++ct) fr[ct] = W6_F + (unsigned)((nb * 56 + kstep) * 4 + ct);
  }
  if (bid < 1664) {
    const int ip = x.lane >> 5, uu = (x.lane >> 4) & 1, q = (x.lane >> 2) & 3, c1 = ((x.lane >> 1) & 1) ^ ip, bb = x.lane & 1;
    const int rl = 4 * ip + q, ch = 2 * (2 * uu + c1) + bb;
    const unsigned stg0 = x.lds0 + (unsigned)x.w * 4096u;
#pragma unroll
    for (int u = 0; u < 4; ++u) {
      const int row = (u < 2 ? ra + 8 * u : rb + 8 * (u - 2)) + rl;
      const GAS unsigned char *gp = (const GAS unsigned char *)src + ((size_t)row * ld + n0 + ch * 8) * 2;
      asm volatile("s_mov_b32 m0, %0\n\ts_nop 0\n\tglobal_load_lds_dwordx4 %1, off" ::"s"(stg0 + (unsigned)u * 1024u), "v"(gp) : "memory");
    }
  }
  auto retile = [&]() {
    asm volatile("s_waitcnt vmcnt(0)" ::: "memory");
    const int gq = (x.lane & 15) >> 2, gp_ = x.lane & 3, gi = x.g & 1;
#pragma unroll
    for (int ct = 0; ct < 4; ++ct) {
      const unsigned L = (unsigned)((gi << 5) | ((ct >> 1) << 4) | (gq << 2) | (((ct & 1) ^ gi) << 1) | (gp_ >> 1));
      const unsigned a0 = (unsigned)x.w * 4096u + (unsigned)(x.g >> 1) * 1024u + L * 16u + (unsigned)(gp_ & 1) * 8u;
      const v2u lo = trread(x, a0), hi = trread(x, a0 + 2048u);
      const v4u fragv = {lo.x, lo.y, hi.x, hi.y};
      stg(x.rs, x.lane16, __builtin_amdgcn_readfirstlane(OFF_WT + fr[ct] * KB), fragv);
      if (csid >= 0) {
        const f4 cs = mfma32(fragv, v4u{0x3F803F80u, 0x3F803F80u, 0x3F803F80u, 0x3F803F80u}, f4{0.f, 0.f, 0.f, 0.f});
        const unsigned co = __builtin_amdgcn_readfirstlane(OFF_CSP + (ci[ct] * 16u + (unsigned)kstep) * 64u);
        if (x.tok == 0) stg(x.rs, (unsigned)x.g * 16u, co, __builtin_bit_cast(v4u, cs));
      }
    }
  };
  if (bid < 384) retile();
  FSA(1);
  v4u xres[2];
  v2u wph[2];
  if (cw) {
    xres[0] = clamp4(v4u{xin[0].x, xin[0].y, xin[1].x, xin[1].y});
    xres[1] = clamp4(v4u{xin[2].x, xin[2].y, xin[3].x, xin[3].y});
    const unsigned fo = OFF_L0 + O_XB + (unsigned)(x.T * 16 + 2 * x.c) * KB;
    stg(x.rs, x.lane16, fo, xres[0]); stg(x.rs, x.lane16, fo + KB, xres[1]);
    put_stats(x, xres[0], xres[1], OFF_L0 + O_XS);
#pragma unroll
    for (int h = 0; h < 2; ++h)
      wph[h] = v2u{(unsigned)wraw[h][0] | ((unsigned)wraw[h][1] << 16), (unsigned)wraw[h][2] | ((unsigned)wraw[h][3] << 16)};
  } else {
    xres[0] = xres[1] = v4u{0u, 0u, 0u, 0u};
    wph[0] = wph[1] = v2u{0u, 0u};
  }
  FSA(2);
  asm volatile("s_waitcnt vmcnt(0) lgkmcnt(0)\n\ts_barrier" ::: "memory");
  FSA(3);
  if (threadIdx.x == 0) __hip_atomic_store(x.ctl + OFF_FLAGS / 4 + b * 16, tag, RLX_AGENT);
  if (bid >= 384 && bid < 1664) retile();
  if (!cw) {
    poi4(x.rs, x.lane16, OFF_L0 + O_KVP + (unsigned)((x.pp * 16 + x.tau) * 4 + x.m) * KB);
    if (x.tok == 0) poi4(x.rs, (unsigned)x.g * 16u, OFF_L0 + O_KVZ + (unsigned)((x.pp * 4 + x.m) * 16 + x.tau) * 64u);
    poi4(x.rs, x.lane16, OFF_L0 + O_OB + (unsigned)(x.TA * 16 + x.pp) * KB);
    poi4(x.rs, x.lane16, OFF_L0 + O_OB + (unsigned)((x.TA + 1) * 16 + x.pp) * KB);
    poi4(x.rs, x.lane16, OFF_L0 + O_HB + (unsigned)(x.T * 16 + 2 * x.c) * KB);
    poi4(x.rs, x.lane16, OFF_L0 + O_HB + (unsigned)(x.T * 16 + 2 * x.c + 1) * KB);
    poi4(x.rs, x.lane16, OFF_L0 + L_STRIDE + O_KVP + (unsigned)((x.pp * 16 + x.tau) * 4 + x.m) * KB);
    if (x.tok == 0) poi4(x.rs, (unsigned)x.g * 16u, OFF_L0 + L_STRIDE + O_KVZ + (unsigned)((x.pp * 4 + x.m) * 16 + x.tau) * 64u);
  }
  if (x.w == 4) {
    poll_flags(x, OFF_FLAGS, tag);
    if (b == 0 && x.lane == 0) __hip_atomic_store(x.ctl + OFF_EPOCH / 4, tag, RLX_AGENT);
  }
  BAR();
  FSA(4);
  if (cw) {
    compute_path(x, p, xres, wph, tag);
  } else {
    v4u cv[2];
#pragma unroll
    for (int jj = 0; jj < 2; ++jj) LDA(cv[jj], x.lane16, OFF_CSP + (unsigned)(x.pp * 6 + min(x.m + 4 * jj, 5)) * KB);
    dmaR<POS1, POS2>(x);
#pragma unroll
    for (int jj = 0; jj < 2; ++jj) {
      WAITV1(24, cv[jj]);
      const f4 s = shsum(__builtin_bit_cast(f4, cv[jj]));
      const int u = x.m + 4 * jj;
      if (u < 6 && x.lane < 4) *(LAS f4 *)(x.sm + M_CS + (unsigned)u * 64u + (unsigned)x.lane * 16u) = s;
    }
    helper_path(x, tag);
  }
  asm volatile("s_waitcnt vmcnt(0)" ::: "memory");
}

extern "C" void kernel_launch(void *const *d_in, const int *in_sizes, int n_in, void *d_out, int out_size, void *d_ws, size_t ws_size,
                              hipStream_t stream) {
  Params p;
  p.x = (const unsigned short *)d_in[0];
  p.wqkv = (const unsigned short *)d_in[1];
  p.wphi = (const unsigned short *)d_in[2];
  p.wo = (const unsigned short *)d_in[3];
  p.w1 = (const unsigned short *)d_in[5];
  p.w2 = (const unsigned short *)d_in[6];
  p.w3 = (const unsigned short *)d_in[7];
  p.w4 = (const unsigned short *)d_in[8];
  p.out = (unsigned short *)d_out;
  p.ws = (unsigned char *)d_ws;
  (void)hipFuncSetAttribute((const void *)mega_kernel, hipFuncAttributeMaxDynamicSharedMemorySize, LDS_BYTES);
  hipLaunchKernelGGL(mega_kernel, dim3(256), dim3(512), LDS_BYTES, stream, p);
}
```

```cpp
#include <hip/hip_runtime.h>
#include <stdint.h>

#define GAS __attribute__((address_space(1)))
#define LAS __attribute__((address_space(3)))
#define DI __device__ __forceinline__
typedef unsigned v4u __attribute__((ext_vector_type(4)));
typedef unsigned v2u __attribute__((ext_vector_type(2)));
typedef float f4 __attribute__((ext_vector_type(4)));
typedef float f2 __attribute__((ext_vector_type(2)));
typedef short s8 __attribute__((ext_vector_type(8)));
typedef short s4 __attribute__((ext_vector_type(4)));
typedef __bf16 bf2 __attribute__((ext_vector_type(2)));
typedef GAS unsigned gu32;
typedef __amdgpu_buffer_rsrc_t rsrc_t;
#define RLX_AGENT __ATOMIC_RELAXED, __HIP_MEMORY_SCOPE_AGENT

#define DBG_GBAR 0
#define DBG_NOPOISON 0
#define DBG_STAMP 0
#define SPEC 0
#define PREFETCH 1
#define KV16 1
#define WARM 0

namespace {
constexpr unsigned MB = 1u << 20, KB = 1024u;
constexpr unsigned OFF_EPOCH = 0, OFF_ABORT = 256, OFF_GBAR = 512, OFF_FLAGS = 16384, OFF_FLAGS2 = 32768;
constexpr unsigned OFF_WT = 1 * MB;
constexpr unsigned W1_F = 0, W2_F = 1536, W5_F = 3072, W6_F = 4864;
constexpr unsigned OFF_CSP = 8 * MB;
constexpr unsigned OFF_L0 = 9 * MB, L_STRIDE = 24 * MB;
constexpr unsigned O_XB = 0, O_XS = 2 * MB, O_KVP = 2 * MB + 512 * KB, O_KVZ = 3 * MB + 512 * KB, O_OB = 4 * MB, O_X1B = 6 * MB,
                   O_X1S = 8 * MB, O_HB = 9 * MB, O_X2B = 11 * MB, O_X2S = 13 * MB, O_H3B = 14 * MB;
constexpr unsigned WS_BYTES = OFF_L0 + 2 * L_STRIDE;
constexpr unsigned OOB = 0x7F000000u;
constexpr int RING = 156;
constexpr unsigned MISC = RING * KB;
constexpr unsigned M_CS = MISC, M_KVA = MISC + 1088, M_ZA = MISC + 3136, M_SIG = MISC + 3264, M_SIG2 = MISC + 3280, M_RDY = MISC + 3296, M_FLAG = MISC + 3300;
constexpr unsigned IMG = 0;
constexpr unsigned LDS_BYTES = 163840;
constexpr int POS1 = 0, POS2 = 96, POS3 = 160, POS4 = 224, POS5 = 288, POS6 = 400, POSE = 624;
constexpr unsigned SENT = 0xFFFFFFFFu;
constexpr unsigned SPIN_CAP = 1u << 13;
constexpr int AUX_SC1 = 16;
#define DLY_KV 14
#define DLY_1 19
#define DLY_2 19
#define DLY_3 19
#define DLY_4 19
#define DLY_5 19
#define DLY_6 19

struct Params {
  const unsigned short *x, *wqkv, *wphi, *wo, *w1, *w2, *w3, *w4;
  unsigned short *out;
  unsigned char *ws;
};

DI unsigned pk2(float a, float b) { f2 v = {a, b}; bf2 r = __builtin_convertvector(v, bf2); return __builtin_bit_cast(unsigned, r); }
DI float asf(unsigned u) { return __builtin_bit_cast(float, u); }
DI float blo(unsigned u) { return __builtin_bit_cast(float, u << 16); }
DI float bhi(unsigned u) { return __builtin_bit_cast(float, u & 0xFFFF0000u); }
DI unsigned clampw(unsigned u) { return u < 0xFFFFFFFEu ? u : 0xFFFFFFFEu; }
DI unsigned clampf(float f) { return clampw(__builtin_bit_cast(unsigned, f)); }
DI v4u clamp4(v4u v) { return v4u{clampw(v.x), clampw(v.y), clampw(v.z), clampw(v.w)}; }
DI v4u pack8(f4 a, f4 b) { return clamp4(v4u{pk2(a.x, a.y), pk2(a.z, a.w), pk2(b.x, b.y), pk2(b.z, b.w)}); }
DI f4 ulo(v4u v) { return f4{blo(v.x), bhi(v.x), blo(v.y), bhi(v.y)}; }
DI f4 uhi(v4u v) { return f4{blo(v.z), bhi(v.z), blo(v.w), bhi(v.w)}; }
DI f4 mfma32(v4u a, v4u b, f4 c) {
  return __builtin_amdgcn_mfma_f32_16x16x32_bf16(__builtin_bit_cast(s8, a), __builtin_bit_cast(s8, b), c, 0, 0, 0);
}
DI f4 mfma16(v2u a, v2u b, f4 c) {
  return __builtin_amdgcn_mfma_f32_16x16x16bf16_1k(__builtin_bit_cast(s4, a), __builtin_bit_cast(s4, b), c, 0, 0, 0);
}
DI v4u ldg(rsrc_t rs, unsigned voff, unsigned soff) { return __builtin_amdgcn_raw_buffer_load_b128(rs, voff, soff, AUX_SC1); }
DI void stg(rsrc_t rs, unsigned voff, unsigned soff, v4u v) {
  __builtin_amdgcn_raw_buffer_store_b128(v, rs, voff, soff, AUX_SC1);
  asm volatile("s_nop 1" ::"v"(v));
}
DI void stg2(rsrc_t rs, unsigned voff, unsigned soff, v2u v) { __builtin_amdgcn_raw_buffer_store_b64(v, rs, voff, soff, AUX_SC1); }
DI void poi4(rsrc_t rs, unsigned voff, unsigned soff) { if (!DBG_NOPOISON) stg(rs, voff, soff, v4u{0xFFFFFFFFu, 0xFFFFFFFFu, 0xFFFFFFFFu, 0xFFFFFFFFu}); }
DI void poi2(rsrc_t rs, unsigned voff, unsigned soff) { if (!DBG_NOPOISON) stg2(rs, voff, soff, v2u{0xFFFFFFFFu, 0xFFFFFFFFu}); }
DI unsigned max4(v4u v) { return max(max(v.x, v.y), max(v.z, v.w)); }
DI bool anybad(unsigned mx) { return __any(mx == SENT) != 0; }
DI float xsum(float x) {
  unsigned u = __builtin_bit_cast(unsigned, x);
  auto r = __builtin_amdgcn_permlane16_swap(u, u, false, false);
  x = __builtin_bit_cast(float, (unsigned)r[0]) + __builtin_bit_cast(float, (unsigned)r[1]);
  u = __builtin_bit_cast(unsigned, x);
  auto q = __builtin_amdgcn_permlane32_swap(u, u, false, false);
  return __builtin_bit_cast(float, (unsigned)q[0]) + __builtin_bit_cast(float, (unsigned)q[1]);
}
DI float xmax(float x) {
  unsigned u = __builtin_bit_cast(unsigned, x);
  auto r = __builtin_amdgcn_permlane16_swap(u, u, false, false);
  x = fmaxf(__builtin_bit_cast(float, (unsigned)r[0]), __builtin_bit_cast(float, (unsigned)r[1]));
  u = __builtin_bit_cast(unsigned, x);
  auto q = __builtin_amdgcn_permlane32_swap(u, u, false, false);
  return fmaxf(__builtin_bit_cast(float, (unsigned)q[0]), __builtin_bit_cast(float, (unsigned)q[1]));
}
template <bool MAX> DI float red_op(float a, float b) { return MAX ? fmaxf(a, b) : a + b; }
DI unsigned fbits(float f) { return __builtin_bit_cast(unsigned, f); }
template <bool MAX> DI void xred4(float &a, float &b, float &c, float &d) {
  const auto r1 = __builtin_amdgcn_permlane16_swap(fbits(a), fbits(b), false, false);
  const auto r2 = __builtin_amdgcn_permlane16_swap(fbits(c), fbits(d), false, false);
  const float c1 = red_op<MAX>(asf(r1[0]), asf(r1[1])), c2 = red_op<MAX>(asf(r2[0]), asf(r2[1]));
  const auto r3 = __builtin_amdgcn_permlane32_swap(fbits(c1), fbits(c2), false, false);
  const unsigned e = fbits(red_op<MAX>(asf(r3[0]), asf(r3[1])));
  const auto r4 = __builtin_amdgcn_permlane32_swap(e, e, false, false);
  const unsigned f0 = r4[0], f1 = r4[1];
  const auto r5 = __builtin_amdgcn_permlane16_swap(f0, f0, false, false);
  const auto r6 = __builtin_amdgcn_permlane16_swap(f1, f1, false, false);
  a = asf(r5[0]); b = asf(r5[1]); c = asf(r6[0]); d = asf(r6[1]);
}
template <bool MAX> DI void xred2(float &a, float &b) {
  const auto r1 = __builtin_amdgcn_permlane16_swap(fbits(a), fbits(b), false, false);
  const unsigned c = fbits(red_op<MAX>(asf(r1[0]), asf(r1[1])));
  const auto r3 = __builtin_amdgcn_permlane32_swap(c, c, false, false);
  const unsigned e = fbits(red_op<MAX>(asf(r3[0]), asf(r3[1])));
  const auto r5 = __builtin_amdgcn_permlane16_swap(e, e, false, false);
  a = asf(r5[0]); b = asf(r5[1]);
}
DI f2 gelu2(f2 x) {
  const f2 c1 = {-2.302208198f, -2.302208198f}, c2 = {-0.1029432397f, -0.1029432397f}, one = {1.0f, 1.0f};
  const f2 t = x * __builtin_elementwise_fma(x * x, c2, c1);
  const f2 d = one + f2{__builtin_amdgcn_exp2f(t.x), __builtin_amdgcn_exp2f(t.y)};
  return x * f2{__builtin_amdgcn_rcpf(d.x), __builtin_amdgcn_rcpf(d.y)};
}
DI f4 gelu4(f4 y) {
  const f2 a = gelu2(f2{y.x, y.y}), b = gelu2(f2{y.z, y.w});
  return f4{a.x, a.y, b.x, b.y};
}

#define FSL 1
#define FS(i) do {} while (0)
#define FSA(i) do { unsigned long long t_; asm volatile("s_memtime %0\n\ts_waitcnt lgkmcnt(0)" : "=s"(t_) :: "memory"); \
  int ww_ = x.w; asm volatile("" : "+s"(ww_)); \
  unsigned a_ = (ww_ == 0) ? (70 + (i)) * 4u : ((ww_ == 4) ? 400u + (70 + (i)) * 4u : 780u); \
  asm volatile("" : "+s"(a_)); \
  *(LAS unsigned *)(x.sm + M_FLAG + a_) = (unsigned)t_; } while (0)
#define FSP(i) do {} while (0)
#define SB() __builtin_amdgcn_sched_barrier(0)
#define MARK(s) asm volatile("; MARK " s)
#define BAR() asm volatile("s_waitcnt lgkmcnt(0)\n\ts_barrier" ::: "memory")
#define WAITV_BAR(n) asm volatile("s_waitcnt vmcnt(" #n ") lgkmcnt(0)\n\ts_barrier" ::: "memory")
#define DRAIN_BAR() asm volatile("s_waitcnt vmcnt(0) lgkmcnt(0)\n\ts_barrier" ::: "memory")
DI void sleepn(int n) {
  for (int i = 0; i < n; ++i) __builtin_amdgcn_s_sleep(1);
}

struct Ctx {
  rsrc_t rs;
  LAS unsigned char *sm;
  unsigned lds0;
  unsigned lane16;
  LAS unsigned char *ab[3];
  int lane, w, m, g, tok;
  int t, c, tau, pp, b;
  int T, TA;
  unsigned spins;
  gu32 *ctl;
};
DI void refresh(Ctx &x) {
  asm volatile("" : "+s"(x.w), "+s"(x.b), "+v"(x.lane));
  x.lane16 = (unsigned)x.lane * 16u;
  x.g = x.lane >> 4; x.tok = x.lane & 15;
  x.m = x.w & 3;
  const int xc = x.b & 7, j = x.b >> 3;
  x.t = 4 * xc + (j >> 3); x.c = j & 7;
  x.tau = 2 * xc + (j >> 4); x.pp = j & 15;
  x.T = 4 * x.t + x.m;
  x.TA = 8 * x.tau + 2 * x.m;
  x.ab[0] = x.sm + x.lane16;
  unsigned o1 = x.lane16 + 60u * KB, o2 = x.lane16 + 120u * KB;
  asm volatile("" : "+v"(o1), "+v"(o2));
  x.ab[1] = x.sm + o1; x.ab[2] = x.sm + o2;
}

template <int P> DI void dmaP(const Ctx &x, bool live = true) {
  constexpr int Q = P % POSE;
  int w = x.w, b = x.b;
  asm volatile("" : "+s"(w), "+s"(b));
  const int m = w & 3, j = b >> 3;
  unsigned slot = (unsigned)(P % RING) + (unsigned)m;
  unsigned fr;
  if constexpr (Q < POS2) fr = W1_F + (unsigned)((j & 15) * 96 + Q + m);
  else if constexpr (Q < POS5) fr = W2_F + (unsigned)(((Q - POS2) / 64) * 512 + (j & 7) * 64 + (Q - POS2) % 64 + m);
  else if constexpr (Q < POS6) fr = W5_F + (unsigned)((j & 15) * 112 + (Q - POS5) + m);
  else fr = W6_F + (unsigned)((j & 7) * 224 + (Q - POS6) + m);
  const unsigned lds = x.lds0 + slot * KB, so = live ? OFF_WT + fr * KB : OOB;
  asm volatile("s_mov_b32 m0, %0\n\ts_nop 0\n\tbuffer_load_dwordx4 %1, %2, %3 offen sc1 lds" ::"s"(lds), "v"(x.lane16), "s"(x.rs), "s"(so) : "memory");
}
template <int LO, int HI> DI void dmaR(const Ctx &x, bool live = true) {
  if constexpr (LO < HI) { dmaP<LO>(x, live); dmaR<LO + 4, HI>(x, live); }
}
template <int LO, int HI> DI void dmaRT(const Ctx &x, bool live = true) {
  if constexpr (LO < HI) { dmaP<LO>(x, live); asm volatile("s_waitcnt vmcnt(3)" ::: "memory"); dmaRT<LO + 4, HI>(x, live); }
}
template <int LO, int HI> DI void dmaR6(const Ctx &x, bool live = true) {
  if constexpr (LO < HI) { dmaP<LO>(x, live); asm volatile("s_waitcnt vmcnt(5)" ::: "memory"); dmaR6<LO + 4, HI>(x, live); }
}
DI void sig_set(const Ctx &x, unsigned v, unsigned word = M_SIG) { *(volatile LAS unsigned *)(x.sm + word + (unsigned)x.m * 4u) = v; }
DI void sig_wait(const Ctx &x, unsigned v, unsigned word = M_SIG) {
  for (unsigned sp = 0; sp < (1u << 20); ++sp) {
    const v4u s = *(volatile LAS v4u *)(x.sm + word);
    if (__builtin_amdgcn_readfirstlane(min(min(s.x, s.y), min(s.z, s.w))) >= v) break;
    __builtin_amdgcn_s_sleep(1);
  }
}

#define LDA(dst, voff, soff) \
  asm volatile("buffer_load_dwordx4 %0, %1, %2, %3 offen sc1" : "=v"(dst) : "v"(voff), "s"(x.rs), "s"(soff) : "memory")
#define LDA2(dst, voff, soff, imm) \
  asm volatile("buffer_load_dwordx2 %0, %1, %2, %3 offen offset:%4 sc1" : "=v"(dst) : "v"(voff), "s"(x.rs), "s"(soff), "i"(imm) : "memory")
#define WAITV1(n, r) asm volatile("s_waitcnt vmcnt(%1)" : "+v"(r) : "i"(n) : "memory")
#define WAITV2(n, r, q) asm volatile("s_waitcnt vmcnt(%2)" : "+v"(r), "+v"(q) : "i"(n) : "memory")
#define TIE(r) asm volatile("" : "+v"(r) : : "memory")
DI bool giveup(Ctx &x) {
  if (++x.spins > SPIN_CAP) {
    if (x.lane == 0) __hip_atomic_store(x.ctl + OFF_ABORT / 4, 1u, RLX_AGENT);
    return true;
  }
  __builtin_amdgcn_s_sleep(1);
  return false;
}
DI v4u load_sure(Ctx &x, unsigned voff, unsigned soff) {
  v4u v;
  while (true) {
    asm volatile("buffer_load_dwordx4 %0, %1, %2, %3 offen sc1\n\ts_waitcnt vmcnt(0)" : "=&v"(v) : "v"(voff), "s"(x.rs), "s"(soff) : "memory");
    if (!anybad(max4(v)) || giveup(x)) break;
  }
  return v;
}
DI v2u load_sure2(Ctx &x, unsigned voff, unsigned soff) {
  v2u v;
  while (true) {
    asm volatile("buffer_load_dwordx2 %0, %1, %2, %3 offen sc1\n\ts_waitcnt vmcnt(0)" : "=&v"(v) : "v"(voff), "s"(x.rs), "s"(soff) : "memory");
    if (!anybad(max(v.x, v.y)) || giveup(x)) break;
  }
  return v;
}

DI void warm(Ctx &x, unsigned voff, unsigned soff, unsigned soff2) {
}
#define LDC(dst, voff, soff) \
  asm volatile("buffer_load_dword %0, %1, %2, %3 offen sc1" : "=v"(dst) : "v"(voff), "s"(x.rs), "s"(soff) : "memory")

#define GBAR() do {} while (0)
#define SLEEP(n) sleepn(n)

DI void put_stats(const Ctx &x, v4u u, v4u v, unsigned sbuf) {
  const f4 a = ulo(u), b = uhi(u), c = ulo(v), d = uhi(v);
  const f4 s = (a + b) + (c + d), q = (a * a + b * b) + (c * c + d * d);
  float s1 = (s.x + s.y) + (s.z + s.w), s2 = (q.x + q.y) + (q.z + q.w);
  xred2<false>(s1, s2);
  if (x.g == 0) stg2(x.rs, (unsigned)x.tok * 8u, sbuf + (unsigned)(x.T * 8 + x.c) * 128u, v2u{clampf(s1), clampf(s2)});
}
struct Stats { v2u v[2]; };
DI void stats_load(const Ctx &x, Stats &s, unsigned soffT) {
  const unsigned vo = (unsigned)(x.g * 256 + x.tok * 8);
  LDA2(s.v[0], vo, soffT, 0); LDA2(s.v[1], vo, soffT, 128);
}
DI void stats_fin(Ctx &x, Stats &s, unsigned soffT, float &mean, float &var) {
  TIE(s.v[0]); TIE(s.v[1]);
  if (__builtin_expect(anybad(max(max(s.v[0].x, s.v[0].y), max(s.v[1].x, s.v[1].y))), 0)) {
    s.v[0] = load_sure2(x, (unsigned)(x.g * 256 + x.tok * 8), soffT);
    s.v[1] = load_sure2(x, (unsigned)(x.g * 256 + x.tok * 8 + 128), soffT);
  }
  float s1 = asf(s.v[0].x) + asf(s.v[1].x), s2 = asf(s.v[0].y) + asf(s.v[1].y);
  xred2<false>(s1, s2);
  mean = s1 * (1.0f / 512.0f);
  var = fmaxf(__builtin_fmaf(-mean, mean, s2 * (1.0f / 512.0f)), 0.0f);
}
DI const LAS v4u *lds4(const Ctx &x, unsigned off) { return (const LAS v4u *)(x.sm + off); }
template <int NT, int P> DI void ldaN(const Ctx &x, v4u (&a)[NT]) {
#pragma unroll
  for (int f = 0; f < NT; ++f) {
    const int slot = (P + f) % RING;
    a[f] = *(const LAS v4u *)(x.ab[slot / 60] + (slot % 60) * KB);
  }
}
template <int NT> DI void tieN(v4u (&a)[NT]) {
  if constexpr (NT == 4) asm volatile("" : "+v"(a[0]), "+v"(a[1]), "+v"(a[2]), "+v"(a[3]));
  else if constexpr (NT == 6) asm volatile("" : "+v"(a[0]), "+v"(a[1]), "+v"(a[2]), "+v"(a[3]), "+v"(a[4]), "+v"(a[5]));
  else asm volatile("" : "+v"(a[0]), "+v"(a[1]), "+v"(a[2]), "+v"(a[3]), "+v"(a[4]), "+v"(a[5]), "+v"(a[6]));
}
DI unsigned mx4(unsigned m, v4u v) {
  unsigned r;
  asm("v_max3_u32 %0, %1, %2, %3" : "=v"(r) : "v"(m), "v"(v.x), "v"(v.y));
  asm("v_max3_u32 %0, %1, %2, %3" : "=v"(m) : "v"(r), "v"(v.z), "v"(v.w));
  return m;
}
DI f4 ldcs(const Ctx &x, int tile) { return *(const LAS f4 *)(x.sm + M_CS + (unsigned)tile * 64u + (unsigned)x.g * 16u); }
constexpr int imin(int a, int b) { return a < b ? a : b; }

template <bool SH> DI unsigned vof(const Ctx &x, int k, const unsigned (&vo)[4]) { return SH ? vo[k >> 2] : x.lane16; }
template <int NK> constexpr int depth1() { return 8; }
template <int POS, int NK, bool SH, int K0, int K1, int I> struct KL1 {
  static DI void run(Ctx &x, v4u (&bf)[NK], f4 (&acc)[4], unsigned &mx, v4u (&cur)[4], v4u (&nxt)[4], unsigned so, const unsigned (&vo)[4],
                     unsigned sig) {
    v4u nn[4];
    constexpr int DP = depth1<NK>();
    WAITV1(imin(DP - 1, NK - 1 - I), bf[I]);
    tieN<4>(cur);
    SB();
    acc[0] = mfma32(cur[0], bf[I], acc[0]);
    SB();
    if constexpr (I + DP < NK) LDA(bf[I + DP], vof<SH>(x, I + DP, vo), so + (unsigned)(I + DP) * KB);
    if constexpr (I + DP == NK - 1) { if (sig) sig_set(x, sig); }
    SB();
    if constexpr (I + 2 < K1) ldaN<4, POS + 4 * (I + 2)>(x, nn);
    mx = mx4(mx, bf[I]);
#pragma unroll
    for (int f = 1; f < 4; ++f) acc[f] = mfma32(cur[f], bf[I], acc[f]);
    __builtin_amdgcn_sched_group_barrier(0x100, 1, 0);
#pragma unroll
    for (int f = 1; f < 4; ++f) {
      __builtin_amdgcn_sched_group_barrier(0x008, 1, 0);
      __builtin_amdgcn_sched_group_barrier(0x100, 1, 0);
      __builtin_amdgcn_sched_group_barrier(0x002, 1, 0);
    }
    SB();
    if constexpr (I + 1 < K1) KL1<POS, NK, SH, K0, K1, I + 1>::run(x, bf, acc, mx, nxt, nn, so, vo, sig);
  }
};
template <int POS, bool SH, int K1, int I> struct KL1R {
  static DI void run(Ctx &x, f4 (&acc)[4], unsigned so, const unsigned (&vo)[4]) {
    const v4u bv = load_sure(x, vof<SH>(x, I, vo), so + (unsigned)I * KB);
    v4u a[4];
    ldaN<4, POS + 4 * I>(x, a);
#pragma unroll
    for (int f = 0; f < 4; ++f) acc[f] = mfma32(a[f], bv, acc[f]);
    if constexpr (I + 1 < K1) KL1R<POS, SH, K1, I + 1>::run(x, acc, so, vo);
  }
};
template <bool ST> DI unsigned canary_poll1(Ctx &x, unsigned so, unsigned cvo, Stats &st, unsigned sst) {
  unsigned cn;
  LDC(cn, cvo, so);
  if constexpr (ST) stats_load(x, st, sst);
  WAITV1(0, cn);
  if constexpr (ST) { TIE(st.v[0]); TIE(st.v[1]); cn = max(cn, max(max(st.v[0].x, st.v[0].y), max(st.v[1].x, st.v[1].y))); }
  return cn;
}
template <int NK, bool SH, bool ST>
DI void pro1(Ctx &x, v4u (&bf)[NK], unsigned so, const unsigned (&vo)[4], unsigned cvo, Stats &st, unsigned sst, unsigned &cn) {
  while (anybad(canary_poll1<ST>(x, so, cvo, st, sst)) && !giveup(x)) {}
  cn = 0;
#pragma unroll
  for (int i = 0; i < depth1<NK>(); ++i) LDA(bf[i], vof<SH>(x, i, vo), so + (unsigned)i * KB);
}
template <int NK, bool SH, bool ST>
DI void chk1(Ctx &x, v4u (&bf)[NK], unsigned so, const unsigned (&vo)[4], unsigned cvo, Stats &st, unsigned sst, unsigned cn) {
}
template <int POS, int NK, bool SH, int K0, int K1>
DI void kloop1(Ctx &x, v4u (&bf)[NK], f4 (&acc)[4], unsigned so, const unsigned (&vo)[4], unsigned sig) {
  f4 save[4];
#pragma unroll
  for (int f = 0; f < 4; ++f) save[f] = acc[f];
  unsigned mx = 0;
  v4u a0[4], a1[4];
  ldaN<4, POS + 4 * K0>(x, a0);
  ldaN<4, POS + 4 * (K0 + 1)>(x, a1);
  KL1<POS, NK, SH, K0, K1, K0>::run(x, bf, acc, mx, a0, a1, so, vo, sig);
  if (__builtin_expect(anybad(mx), 0)) {
#pragma unroll
    for (int f = 0; f < 4; ++f) acc[f] = save[f];
    KL1R<POS, SH, K1, K0>::run(x, acc, so, vo);
  }
}
template <int NT, int POS, int K0, int K1, int I> struct KL2 {
  static DI void run(Ctx &x, v4u (&bf)[16][2], f4 (&acc)[NT][2], unsigned &mx, v4u (&cur)[NT], unsigned so, unsigned sig) {
    v4u nxt[NT];
    WAITV2(2 * imin(3, 15 - I), bf[I][0], bf[I][1]);
    tieN<NT>(cur);
    SB();
    acc[0][0] = mfma32(cur[0], bf[I][0], acc[0][0]);
    acc[0][1] = mfma32(cur[0], bf[I][1], acc[0][1]);
    SB();
    if constexpr (I + 4 < 16) LDA(bf[I + 4][0], x.lane16, so + (unsigned)(I + 4) * KB);
    SB();
    acc[1][0] = mfma32(cur[1], bf[I][0], acc[1][0]);
    acc[1][1] = mfma32(cur[1], bf[I][1], acc[1][1]);
    SB();
    if constexpr (I + 4 < 16) LDA(bf[I + 4][1], x.lane16, so + (unsigned)(16 + I + 4) * KB);
    if constexpr (I == (NT == 6 ? 6 : 11)) sig_set(x, sig);
    SB();
    if constexpr (I + 1 < K1) ldaN<NT, POS + (I + 1) * NT>(x, nxt);
    mx = mx4(mx4(mx, bf[I][0]), bf[I][1]);
#pragma unroll
    for (int f = 2; f < NT; ++f) {
      acc[f][0] = mfma32(cur[f], bf[I][0], acc[f][0]);
      acc[f][1] = mfma32(cur[f], bf[I][1], acc[f][1]);
    }
#pragma unroll
    for (int f = 0; f < NT; ++f) {
      __builtin_amdgcn_sched_group_barrier(0x100, 1, 0);
      __builtin_amdgcn_sched_group_barrier(0x008, 1, 0);
    }
#pragma unroll
    for (int f = 0; f < NT - 4; ++f) {
      __builtin_amdgcn_sched_group_barrier(0x008, 1, 0);
      __builtin_amdgcn_sched_group_barrier(0x002, 2, 0);
    }
    SB();
    if constexpr (I + 1 < K1) KL2<NT, POS, K0, K1, I + 1>::run(x, bf, acc, mx, nxt, so, sig);
  }
};
template <int NT, int POS, int K1, int I> struct KL2R {
  static DI void run(Ctx &x, f4 (&acc)[NT][2], unsigned so) {
    const v4u b0 = load_sure(x, x.lane16, so + (unsigned)I * KB), b1 = load_sure(x, x.lane16, so + (unsigned)(16 + I) * KB);
    v4u a[NT];
    ldaN<NT, POS + I * NT>(x, a);
#pragma unroll
    for (int f = 0; f < NT; ++f) {
      acc[f][0] = mfma32(a[f], b0, acc[f][0]);
      acc[f][1] = mfma32(a[f], b1, acc[f][1]);
    }
    if constexpr (I + 1 < K1) KL2R<NT, POS, K1, I + 1>::run(x, acc, so);
  }
};
DI unsigned stmax(const Stats (&st)[2]) {
  return max(max(max(st[0].v[0].x, st[0].v[0].y), max(st[0].v[1].x, st[0].v[1].y)), max(max(st[1].v[0].x, st[1].v[0].y), max(st[1].v[1].x, st[1].v[1].y)));
}
DI void poll2(Ctx &x, Stats (&st)[2], unsigned so, unsigned sst) {
  const unsigned cvo = (unsigned)(x.lane & 31) * KB + 1020u;
  while (true) {
    unsigned cn;
    LDC(cn, cvo, so);
    stats_load(x, st[0], sst); stats_load(x, st[1], sst + 1024u);
    WAITV1(0, cn);
    TIE(st[0].v[0]); TIE(st[0].v[1]); TIE(st[1].v[0]); TIE(st[1].v[1]);
    if (!anybad(max(cn, stmax(st))) || giveup(x)) break;
  }
}
DI void first2(Ctx &x, v4u (&bf)[16][2], unsigned so) {
#pragma unroll
  for (int i = 0; i < 4; ++i) { LDA(bf[i][0], x.lane16, so + (unsigned)i * KB); LDA(bf[i][1], x.lane16, so + (unsigned)(16 + i) * KB); }
}
DI void pro2(Ctx &x, v4u (&bf)[16][2], Stats (&st)[2], unsigned so, unsigned sst, unsigned &cn) {
  poll2(x, st, so, sst);
  cn = 0;
  first2(x, bf, so);
}
DI void chk2(Ctx &x, v4u (&bf)[16][2], Stats (&st)[2], unsigned so, unsigned sst, unsigned cn) {
}
template <int NT, int POS, int K0, int K1>
DI void kloop2(Ctx &x, v4u (&bf)[16][2], f4 (&acc)[NT][2], unsigned so, unsigned sig, unsigned &mx) {
  v4u a0[NT];
  ldaN<NT, POS + K0 * NT>(x, a0);
  KL2<NT, POS, K0, K1, K0>::run(x, bf, acc, mx, a0, so, sig);
}
template <int NT, int POS> DI void redo2(Ctx &x, f4 (&acc)[NT][2], unsigned so) {
#pragma unroll
  for (int f = 0; f < NT; ++f) { acc[f][0] = f4{0.f, 0.f, 0.f, 0.f}; acc[f][1] = f4{0.f, 0.f, 0.f, 0.f}; }
  KL2R<NT, POS, 16, 0>::run(x, acc, so);
}

DI void featmap4(const f4 (&v)[4], const v2u (&wphi)[2], v2u (&plus)[4], v2u (&minus)[4]) {
  const float L2E = 1.4426950408889634f;
  f4 p[4];
#pragma unroll
  for (int i = 0; i < 4; ++i) p[i] = mfma16(wphi[i & 1], v2u{pk2(v[i].x, v[i].y), pk2(v[i].z, v[i].w)}, f4{0.f, 0.f, 0.f, 0.f});
  float mx[4], nm[4];
#pragma unroll
  for (int i = 0; i < 4; ++i) {
    p[i] *= L2E;
    mx[i] = fmaxf(fmaxf(p[i].x, p[i].y), fmaxf(p[i].z, p[i].w));
    nm[i] = -fminf(fminf(p[i].x, p[i].y), fminf(p[i].z, p[i].w));
  }
  xred4<true>(mx[0], mx[1], mx[2], mx[3]);
  xred4<true>(nm[0], nm[1], nm[2], nm[3]);
  f4 e[4], n[4];
  float se[4], sn[4];
#pragma unroll
  for (int i = 0; i < 4; ++i) {
    const f4 a = p[i] - mx[i], b = -p[i] - nm[i];
    e[i] = f4{__builtin_amdgcn_exp2f(a.x), __builtin_amdgcn_exp2f(a.y), __builtin_amdgcn_exp2f(a.z), __builtin_amdgcn_exp2f(a.w)};
    n[i] = f4{__builtin_amdgcn_exp2f(b.x), __builtin_amdgcn_exp2f(b.y), __builtin_amdgcn_exp2f(b.z), __builtin_amdgcn_exp2f(b.w)};
    se[i] = (e[i].x + e[i].y) + (e[i].z + e[i].w);
    sn[i] = (n[i].x + n[i].y) + (n[i].z + n[i].w);
  }
  xred4<false>(se[0], se[1], se[2], se[3]);
  xred4<false>(sn[0], sn[1], sn[2], sn[3]);
#pragma unroll
  for (int i = 0; i < 4; ++i) {
    const f4 a = e[i] * __builtin_amdgcn_rcpf(se[i]), b = n[i] * __builtin_amdgcn_rcpf(sn[i]);
    plus[i] = v2u{pk2(a.x, a.y), pk2(a.z, a.w)};
    minus[i] = v2u{pk2(b.x, b.y), pk2(b.z, b.w)};
  }
}
DI v2u trread(const Ctx &x, unsigned off) {
  s4 r = __builtin_amdgcn_ds_read_tr16_b64_v4i16((LAS s4 *)(x.sm + off));
  return __builtin_bit_cast(v2u, r);
}
DI f4 shsum(f4 s) {
#pragma unroll
  for (int o = 4; o < 64; o <<= 1) {
    s.x += __shfl_xor(s.x, o); s.y += __shfl_xor(s.y, o); s.z += __shfl_xor(s.z, o); s.w += __shfl_xor(s.w, o);
  }
  return s;
}

DI void poll_flags(Ctx &x, unsigned off, unsigned tag) {
  for (unsigned sp = 0;; ++sp) {
    unsigned f0, f1, f2, f3;
    const unsigned vo = (unsigned)x.lane * 64u;
    LDC(f0, vo, off); LDC(f1, vo, off + 4096u); LDC(f2, vo, off + 8192u); LDC(f3, vo, off + 12288u);
    asm volatile("s_waitcnt vmcnt(0)" : "+v"(f0), "+v"(f1), "+v"(f2), "+v"(f3)::"memory");
    if (__all(f0 == tag && f1 == tag && f2 == tag && f3 == tag)) break;
    if (sp > SPIN_CAP) {
      if (x.lane == 0) __hip_atomic_store(x.ctl + OFF_ABORT / 4, 3u, RLX_AGENT);
      break;
    }
    __builtin_amdgcn_s_sleep(8);
  }
}
DI unsigned sigv(int L, int ph) { return (unsigned)(L * 8 + ph + 1); }

DI void compute_path(Ctx &x, const Params &p, v4u (&xres)[2], const v2u (&wph)[2], unsigned tag) {
  const unsigned vz[4] = {0u, 0u, 0u, 0u};
  v4u x1r[2], x2r[2];
#pragma unroll 1
  for (int L = 0; L < 2; ++L) {
    const unsigned lb0 = OFF_L0 + (unsigned)L * L_STRIDE;
    v4u phq[2][2];
    GBAR();
    {
      refresh(x);
      FS(0);
      unsigned lb = lb0;
      asm volatile("" : "+s"(lb));
      v4u bf[16][2];
      Stats st[2];
      const unsigned so = lb + O_XB + (unsigned)x.TA * 16u * KB, sst = lb + O_XS + (unsigned)x.TA * 1024u;
      unsigned cn;
      pro2(x, bf, st, so, sst, cn);
      FS(1); FSP(0);
      BAR();
      if (L == 0 && threadIdx.x == 0) __hip_atomic_store(x.ctl + OFF_FLAGS2 / 4 + x.b * 16, tag, RLX_AGENT);
      chk2(x, bf, st, so, sst, cn);
      FS(2);
      f4 acc[6][2];
#pragma unroll
      for (int f = 0; f < 6; ++f) { acc[f][0] = f4{0.f, 0.f, 0.f, 0.f}; acc[f][1] = f4{0.f, 0.f, 0.f, 0.f}; }
      unsigned mx = 0;
      kloop2<6, POS1, 0, 16>(x, bf, acc, so, sigv(L, 0), mx);
      if (__builtin_expect(anybad(mx), 0)) redo2<6, POS1>(x, acc, so);
      FS(3);
      BAR();
      MARK("P1epi");
      f4 val[2][6];
#pragma unroll
      for (int j = 0; j < 2; ++j) {
        float mean, var;
        stats_fin(x, st[j], sst + (unsigned)j * 1024u, mean, var);
        const float rstd = __builtin_amdgcn_rsqf(var + 1e-5f), mr = mean * rstd;
#pragma unroll
        for (int f = 0; f < 6; ++f) val[j][f] = acc[f][j] * rstd - ldcs(x, f) * mr;
      }
      {
        v2u pl[4], mi[4];
        const f4 kk[4] = {val[0][2], val[0][3], val[1][2], val[1][3]};
        featmap4(kk, wph, pl, mi);
#pragma unroll
        for (int i = 0; i < 4; ++i) {
          const int j = i >> 1, h = i & 1;
          const unsigned img = IMG + (unsigned)((2 * x.m + j) * 16 + x.tok) * 32u + (unsigned)x.g * 8u + (unsigned)(h * 3) * 4096u;
          *(LAS v2u *)(x.sm + img) = pl[i];
          *(LAS v2u *)(x.sm + img + 4096u) = mi[i];
          *(LAS v2u *)(x.sm + img + 8192u) = v2u{pk2(val[j][4 + h].x, val[j][4 + h].y), pk2(val[j][4 + h].z, val[j][4 + h].w)};
        }
      }
      MARK("P1epiend");
      FS(4);
      BAR();
      FS(5);
      {
        v2u pl[4], mi[4];
        TIE(val[0][0]); TIE(val[0][1]); TIE(val[1][0]); TIE(val[1][1]);
        const f4 qq[4] = {val[0][0], val[0][1], val[1][0], val[1][1]};
        featmap4(qq, wph, pl, mi);
#pragma unroll
        for (int i = 0; i < 4; ++i) phq[i >> 1][i & 1] = v4u{pl[i].x, pl[i].y, mi[i].x, mi[i].y};
      }
      FS(6);
    }
    GBAR();
    GBAR();
    {
      refresh(x);
      unsigned lb = lb0;
      asm volatile("" : "+s"(lb));
      BAR();
      FS(7);
      v4u ka[2], za[2];
#pragma unroll
      for (int h = 0; h < 2; ++h) {
        ka[h] = *lds4(x, M_KVA + (unsigned)h * 1024u + x.lane16);
        const v2u z0 = *(const LAS v2u *)(x.sm + M_ZA + (unsigned)(h * 2) * 32u + (unsigned)x.g * 8u);
        const v2u z1 = *(const LAS v2u *)(x.sm + M_ZA + (unsigned)(h * 2 + 1) * 32u + (unsigned)x.g * 8u);
        za[h] = v4u{z0.x, z0.y, z1.x, z1.y};
      }
#pragma unroll
      for (int j = 0; j < 2; ++j) {
        f4 o[2];
#pragma unroll
        for (int h = 0; h < 2; ++h) {
          const f4 num = mfma32(ka[h], phq[j][h], f4{0.f, 0.f, 0.f, 0.f});
          const f4 den = mfma32(za[h], phq[j][h], f4{0.f, 0.f, 0.f, 0.f});
          o[h] = num * __builtin_amdgcn_rcpf(den.x + 1e-6f);
        }
        stg(x.rs, x.lane16, lb + O_OB + (unsigned)((x.TA + j) * 16 + x.pp) * KB, pack8(o[0], o[1]));
      }
      FS(8);
      {
        const unsigned ob = lb + O_OB + (unsigned)(x.TA * 16 + x.pp) * KB;
        warm(x, (unsigned)((x.lane >> 3) & 1) * 16u * KB + (unsigned)(x.lane & 7) * 128u, ob, ob);
      }
    }
    GBAR();
    {
      refresh(x);
      unsigned lb = lb0;
      asm volatile("" : "+s"(lb));
      SLEEP(DLY_2);
      FS(9);
      v4u bf[16];
      const unsigned so = lb + O_OB + (unsigned)x.T * 16u * KB;
      Stats st;
      unsigned cn;
      const unsigned cvo = (unsigned)(x.lane & 15) * KB + 1020u;
      pro1<16, false, false>(x, bf, so, vz, cvo, st, 0u, cn);
      FS(10); FSP(1);
      BAR();
      chk1<16, false, false>(x, bf, so, vz, cvo, st, 0u, cn);
      FS(11);
      f4 acc[4];
#pragma unroll
      for (int f = 0; f < 4; ++f) acc[f] = f4{0.f, 0.f, 0.f, 0.f};
      kloop1<POS2, 16, false, 0, 16>(x, bf, acc, so, vz, sigv(L, 2));
      FS(12);
      x1r[0] = pack8(ulo(xres[0]) + acc[0], uhi(xres[0]) + acc[1]);
      x1r[1] = pack8(ulo(xres[1]) + acc[2], uhi(xres[1]) + acc[3]);
      const unsigned fo = lb + O_X1B + (unsigned)(x.T * 16 + 2 * x.c) * KB;
      stg(x.rs, x.lane16, fo, x1r[0]); stg(x.rs, x.lane16, fo + KB, x1r[1]);
      put_stats(x, x1r[0], x1r[1], lb + O_X1S);
      sig_set(x, sigv(L, 2), M_SIG2);
      FS(13);
      warm(x, (unsigned)(x.lane & 15) * 128u, fo, lb + O_X1S + (unsigned)(x.T * 8 + x.c) * 128u);
    }
    float mean1, rstd1;
    GBAR();
    {
      refresh(x);
      unsigned lb = lb0;
      asm volatile("" : "+s"(lb));
      SLEEP(DLY_3);
      v4u bf[16];
      Stats st;
      const unsigned so = lb + O_X1B + (unsigned)x.T * 16u * KB;
      unsigned cn;
      const unsigned cvo = (unsigned)(x.lane & 15) * KB + 1020u;
      pro1<16, false, true>(x, bf, so, vz, cvo, st, lb + O_X1S + (unsigned)x.T * 1024u, cn);
      FSP(2);
      BAR();
      chk1<16, false, true>(x, bf, so, vz, cvo, st, lb + O_X1S + (unsigned)x.T * 1024u, cn);
      FS(14);
      f4 acc[4];
#pragma unroll
      for (int f = 0; f < 4; ++f) acc[f] = f4{0.f, 0.f, 0.f, 0.f};
      kloop1<POS3, 16, false, 0, 16>(x, bf, acc, so, vz, sigv(L, 3));
      FS(15);
      float var;
      stats_fin(x, st, lb + O_X1S + (unsigned)x.T * 1024u, mean1, var);
      rstd1 = __builtin_amdgcn_rsqf(var + 1e-5f);
      const float a = rstd1 * __builtin_amdgcn_rsqf(__builtin_fmaf(var * rstd1, rstd1, 1e-5f)), ma = mean1 * a;
      f4 y[4];
#pragma unroll
      for (int f = 0; f < 4; ++f) y[f] = gelu4(acc[f] * a - ldcs(x, 6 + f) * ma);
      const unsigned fo = lb + O_HB + (unsigned)(x.T * 16 + 2 * x.c) * KB;
      stg(x.rs, x.lane16, fo, pack8(y[0], y[1])); stg(x.rs, x.lane16, fo + KB, pack8(y[2], y[3]));
      sig_set(x, sigv(L, 3), M_SIG2);
      FS(16);
      warm(x, (unsigned)(x.lane & 15) * 128u, fo, fo);
    }
    GBAR();
    {
      refresh(x);
      unsigned lb = lb0;
      asm volatile("" : "+s"(lb));
      SLEEP(DLY_4);
      v4u bf[16];
      unsigned vo[4];
#pragma unroll
      for (int q = 0; q < 4; ++q) {
        const int d = q == 0 ? -1 : (q == 1 ? 1 : (q == 2 ? -2 : 2));
        const int src = 16 * x.T + x.tok + d;
        vo[q] = (src >= 0 && src < 2048) ? (unsigned)((src >> 4) * 16 * 1024 + (x.g * 16 + (src & 15)) * 16) : OOB;
      }
      const unsigned so = lb + O_HB;
      unsigned cvo;
      {
        const int s_ = x.lane & 15, nb = x.T + (((s_ >> 2) & 1) ? 1 : -1);
        const int tt = (x.lane & 16) ? nb : x.T;
        cvo = (tt >= 0 && tt < 128) ? (unsigned)(tt * 16 + s_) * KB + 1020u : OOB;
      }
      Stats st;
      unsigned cn;
      pro1<16, true, false>(x, bf, so, vo, cvo, st, 0u, cn);
      FSP(3);
      BAR();
      chk1<16, true, false>(x, bf, so, vo, cvo, st, 0u, cn);
      FS(17);
      f4 acc[4];
#pragma unroll
      for (int f = 0; f < 4; ++f) acc[f] = f4{0.f, 0.f, 0.f, 0.f};
      MARK("P4loop");
      kloop1<POS4, 16, true, 0, 16>(x, bf, acc, so, vo, sigv(L, 4));
      MARK("P4end");
      FS(18);
      const float mr = mean1 * rstd1;
      x2r[0] = pack8(ulo(x1r[0]) * rstd1 - mr + acc[0], uhi(x1r[0]) * rstd1 - mr + acc[1]);
      x2r[1] = pack8(ulo(x1r[1]) * rstd1 - mr + acc[2], uhi(x1r[1]) * rstd1 - mr + acc[3]);
      const unsigned fo = lb + O_X2B + (unsigned)(x.T * 16 + 2 * x.c) * KB;
      stg(x.rs, x.lane16, fo, x2r[0]); stg(x.rs, x.lane16, fo + KB, x2r[1]);
      put_stats(x, x2r[0], x2r[1], lb + O_X2S);
      sig_set(x, sigv(L, 4), M_SIG2);
      FS(19);
      warm(x, (unsigned)(x.lane & 15) * 128u, fo, lb + O_X2S + (unsigned)(x.T * 8 + x.c) * 128u);
    }
    GBAR();
    {
      refresh(x);
      unsigned lb = lb0;
      asm volatile("" : "+s"(lb));
      SLEEP(DLY_5);
      v4u bf[16][2];
      Stats st[2];
      const unsigned so = lb + O_X2B + (unsigned)x.TA * 16u * KB, sst = lb + O_X2S + (unsigned)x.TA * 1024u;
      unsigned cn;
      pro2(x, bf, st, so, sst, cn);
      FSP(4);
      BAR();
      chk2(x, bf, st, so, sst, cn);
      FS(20);
      f4 acc[7][2];
#pragma unroll
      for (int f = 0; f < 7; ++f) { acc[f][0] = f4{0.f, 0.f, 0.f, 0.f}; acc[f][1] = f4{0.f, 0.f, 0.f, 0.f}; }
      unsigned mx = 0;
      MARK("P5loop");
      kloop2<7, POS5, 0, 13>(x, bf, acc, so, sigv(L, 5), mx);
      BAR();
      kloop2<7, POS5, 13, 16>(x, bf, acc, so, sigv(L, 5), mx);
      if (__builtin_expect(anybad(mx), 0)) redo2<7, POS5>(x, acc, so);
      FS(21);
      BAR();
      FS(22);
      MARK("P5epi");
      const int u = x.pp >> 1, odd = x.pp & 1;
#pragma unroll
      for (int j = 0; j < 2; ++j) {
        float mean, var;
        stats_fin(x, st[j], sst + (unsigned)j * 1024u, mean, var);
        const float rstd = __builtin_amdgcn_rsqf(var + 1e-5f), mr = mean * rstd;
        const unsigned hb = lb + O_H3B + (unsigned)((x.TA + j) * 56 + 7 * u) * KB;
#pragma unroll
        for (int r = 0; r < 3; ++r) {
          const f4 y0 = gelu4(acc[2 * r][j] * rstd - ldcs(x, 10 + 2 * r) * mr), y1 = gelu4(acc[2 * r + 1][j] * rstd - ldcs(x, 11 + 2 * r) * mr);
          stg(x.rs, x.lane16, hb + (unsigned)(odd ? 4 + r : r) * KB, pack8(y0, y1));
        }
        const f4 y = gelu4(acc[6][j] * rstd - ldcs(x, 16) * mr);
        stg2(x.rs, x.lane16 + (unsigned)odd * 8u, hb + 3 * KB, v2u{clampw(pk2(y.x, y.y)), clampw(pk2(y.z, y.w))});
      }
      FS(23);
      {
        const unsigned hb0 = lb + O_H3B + (unsigned)(x.TA * 56 + 7 * u + (odd ? 3 : 0)) * KB;
        warm(x, (unsigned)(x.lane >> 5) * 56u * KB + (unsigned)(x.lane & 31) * 128u, hb0, hb0);
      }
    }
    GBAR();
    {
      refresh(x);
      unsigned lb = lb0;
      asm volatile("" : "+s"(lb));
      SLEEP(DLY_6);
      v4u hf[56];
      const unsigned so = lb + O_H3B + (unsigned)x.T * 56u * KB;
      Stats st;
      unsigned cn;
      const unsigned cvo = (unsigned)min(x.lane, 55) * KB + 1020u;
      pro1<56, false, false>(x, hf, so, vz, cvo, st, 0u, cn);
      FSP(5);
      BAR();
      chk1<56, false, false>(x, hf, so, vz, cvo, st, 0u, cn);
      FS(24);
      f4 acc[4];
#pragma unroll
      for (int f = 0; f < 4; ++f) acc[f] = f4{0.f, 0.f, 0.f, 0.f};
      kloop1<POS6, 56, false, 0, 17>(x, hf, acc, so, vz, 0u);
      BAR();
      FS(25);
      kloop1<POS6, 56, false, 17, 39>(x, hf, acc, so, vz, 0u);
      BAR();
      FS(26);
      kloop1<POS6, 56, false, 39, 56>(x, hf, acc, so, vz, 0u);
      FS(27);
      BAR();
      xres[0] = pack8(ulo(x1r[0]) + (ulo(x2r[0]) + acc[0]), uhi(x1r[0]) + (uhi(x2r[0]) + acc[1]));
      xres[1] = pack8(ulo(x1r[1]) + (ulo(x2r[1]) + acc[2]), uhi(x1r[1]) + (uhi(x2r[1]) + acc[3]));
      if (L == 0) {
        const unsigned fo = lb + L_STRIDE + O_XB + (unsigned)(x.T * 16 + 2 * x.c) * KB;
        stg(x.rs, x.lane16, fo, xres[0]); stg(x.rs, x.lane16, fo + KB, xres[1]);
        put_stats(x, xres[0], xres[1], lb + L_STRIDE + O_XS);
        warm(x, (unsigned)(x.lane & 15) * 128u, fo, lb + L_STRIDE + O_XS + (unsigned)(x.T * 8 + x.c) * 128u);
        SLEEP(DLY_1);
      } else {
        GAS unsigned char *op = (GAS unsigned char *)p.out + ((size_t)(16 * x.T + x.tok) * 512 + 64 * x.c + 4 * x.g) * 2;
        *(GAS v2u *)op = v2u{xres[0].x, xres[0].y};
        *(GAS v2u *)(op + 32) = v2u{xres[0].z, xres[0].w};
        *(GAS v2u *)(op + 64) = v2u{xres[1].x, xres[1].y};
        *(GAS v2u *)(op + 96) = v2u{xres[1].z, xres[1].w};
      }
      FS(28); FSP(6);
    }
  }
}

DI void poison_h3(Ctx &x, unsigned lb, int j) {
  const int u = x.pp >> 1, odd = x.pp & 1;
  const unsigned hb = lb + O_H3B + (unsigned)((x.TA + j) * 56 + 7 * u) * KB;
#pragma unroll
  for (int r = 0; r < 3; ++r) poi4(x.rs, x.lane16, hb + (unsigned)(odd ? 4 + r : r) * KB);
  poi2(x.rs, x.lane16 + (unsigned)odd * 8u, hb + 3 * KB);
}
DI void helper_path(Ctx &x, unsigned tag) {
#pragma unroll 1
  for (int L = 0; L < 2; ++L) {
    const unsigned lb0 = OFF_L0 + (unsigned)L * L_STRIDE;
    GBAR();
    {
      refresh(x);
      unsigned lb = lb0;
      asm volatile("" : "+s"(lb));
      FS(0);
      DRAIN_BAR();
      FS(1);
      if (L != 0) {
        sig_wait(x, sigv(L, 0));
        dmaR<96, 156>(x);
      }
      BAR();
      {
        const unsigned fo = (unsigned)(x.T * 16 + 2 * x.c) * KB, sto = (unsigned)(x.T * 8 + x.c) * 128u;
        poi4(x.rs, x.lane16, lb + O_X1B + fo); poi4(x.rs, x.lane16, lb + O_X1B + fo + KB);
        poi4(x.rs, x.lane16, lb + O_X2B + fo); poi4(x.rs, x.lane16, lb + O_X2B + fo + KB);
        if (x.g == 0) { poi2(x.rs, (unsigned)x.tok * 8u, lb + O_X1S + sto); poi2(x.rs, (unsigned)x.tok * 8u, lb + O_X2S + sto); }
      }
      FS(2);
      BAR();
      FS(3);
      {
        const int h = x.m >> 1, sg = x.m & 1;
        const unsigned lo = (unsigned)(4 * x.g + (x.tok >> 2)) * 32u + (unsigned)(x.tok & 3) * 8u;
        const unsigned ia = IMG + (unsigned)(h * 3 + sg) * 4096u + lo, iv = IMG + (unsigned)(h * 3 + 2) * 4096u + lo;
        f4 kv = {0.f, 0.f, 0.f, 0.f}, z = {0.f, 0.f, 0.f, 0.f};
#pragma unroll
        for (int tb = 0; tb < 4; ++tb) {
          const v2u a0 = trread(x, ia + tb * 1024u), a1 = trread(x, ia + tb * 1024u + 512u);
          const v2u b0 = trread(x, iv + tb * 1024u), b1 = trread(x, iv + tb * 1024u + 512u);
          const v4u a = {a0.x, a0.y, a1.x, a1.y};
          kv = mfma32(a, v4u{b0.x, b0.y, b1.x, b1.y}, kv);
          z = mfma32(a, v4u{0x3F803F80u, 0x3F803F80u, 0x3F803F80u, 0x3F803F80u}, z);
        }
        stg2(x.rs, (unsigned)x.lane * 8u, lb + O_KVP + (unsigned)((x.pp * 16 + x.tau) * 4 + x.m) * KB, v2u{clampw(pk2(kv.x, kv.y)), clampw(pk2(kv.z, kv.w))});
        if (x.tok == 0) stg(x.rs, (unsigned)x.g * 16u, lb + O_KVZ + (unsigned)((x.pp * 4 + x.m) * 16 + x.tau) * 64u, clamp4(__builtin_bit_cast(v4u, z)));
      }
      FS(4);
    }
    GBAR();
    {
      refresh(x);
      unsigned lb = lb0;
      asm volatile("" : "+s"(lb));
      if (L == 0) {
        if (x.m == 0) {
          poll_flags(x, OFF_FLAGS2, tag);
          *(volatile LAS unsigned *)(x.sm + M_RDY) = 1u;
        } else {
          for (unsigned sp = 0; sp < (1u << 20); ++sp) {
            if (__builtin_amdgcn_readfirstlane(*(volatile LAS unsigned *)(x.sm + M_RDY))) break;
            __builtin_amdgcn_s_sleep(2);
          }
        }
        v4u cv[3];
#pragma unroll
        for (int jj = 0; jj < 3; ++jj) {
          const int u = min(6 + x.m + 4 * jj, 16);
          cv[jj] = ldg(x.rs, x.lane16, OFF_CSP + (u < 10 ? 96u + (unsigned)(x.c * 4 + u - 6) : 128u + (unsigned)(x.pp * 7 + u - 10)) * KB);
        }
#pragma unroll
        for (int jj = 0; jj < 3; ++jj) {
          const f4 s = shsum(__builtin_bit_cast(f4, cv[jj]));
          const int u = 6 + x.m + 4 * jj;
          if (u < 17 && x.lane < 4) *(LAS f4 *)(x.sm + M_CS + (unsigned)u * 64u + (unsigned)x.lane * 16u) = s;
        }
      }
      SLEEP(DLY_KV);
      v4u pv[16];
      const unsigned so = lb + O_KVP + (unsigned)(x.pp * 64 + x.m) * KB, sz = lb + O_KVZ + (unsigned)(x.pp * 4 + x.m) * KB;
      v4u zv;
      {
        const unsigned cvo = (unsigned)(x.lane & 15) * 4096u + (KV16 ? 508u : 1020u);
        while (true) {
          unsigned cn;
          LDC(cn, cvo, so);
          LDA(zv, x.lane16, sz);
          WAITV2(0, cn, zv);
          if (!anybad(max(cn, max4(zv))) || giveup(x)) break;
        }
      }
      bool again;
      f4 s;
      v2u pw[16];
      do {
        asm volatile("" ::: "memory");
#pragma unroll
        for (int i = 0; i < 16; ++i) pw[i] = __builtin_amdgcn_raw_buffer_load_b64(x.rs, (unsigned)x.lane * 8u, so + (unsigned)i * 4096u, AUX_SC1);
        unsigned mx = 0;
#pragma unroll
        for (int i = 0; i < 16; ++i) mx = max(mx, max(pw[i].x, pw[i].y));
        again = anybad(mx) && !giveup(x);
      } while (__builtin_expect(again, 0));
      s = ulo(v4u{pw[0].x, pw[0].y, 0u, 0u});
#pragma unroll
      for (int i = 1; i < 16; ++i) s += ulo(v4u{pw[i].x, pw[i].y, 0u, 0u});
      *(LAS v2u *)(x.sm + M_KVA + (unsigned)(x.m >> 1) * 1024u + x.lane16 + (unsigned)(x.m & 1) * 8u) = v2u{pk2(s.x, s.y), pk2(s.z, s.w)};
      const f4 zs = shsum(__builtin_bit_cast(f4, zv));
      if (x.lane < 4) *(LAS v2u *)(x.sm + M_ZA + (unsigned)x.m * 32u + (unsigned)x.lane * 8u) = v2u{pk2(zs.x, zs.y), pk2(zs.z, zs.w)};
      FS(5); FSP(0);
      GBAR();
      BAR();
      FS(6);
      if (L == 0) dmaR<96, 156>(x);
      dmaR<156, 188>(x);
    }
    GBAR();
    {
      refresh(x);
      unsigned lb = lb0;
      asm volatile("" : "+s"(lb));
      DRAIN_BAR();
      FS(7);
      sig_wait(x, sigv(L, 2));
      dmaR<188, 220>(x);
      sig_wait(x, sigv(L, 2), M_SIG2);
      dmaR<220, 252>(x);
      poison_h3(x, lb, 0); poison_h3(x, lb, 1);
    }
    GBAR();
    {
      refresh(x);
      unsigned lb = lb0;
      asm volatile("" : "+s"(lb));
      WAITV_BAR(8);
      FS(8);
      sig_wait(x, sigv(L, 3));
      FS(15);
      dmaR<252, 284>(x);
      sig_wait(x, sigv(L, 3), M_SIG2);
      dmaR<284, 316>(x);
      FS(16);
      if (L == 0) {
        const unsigned fo = lb + L_STRIDE + O_XB + (unsigned)(x.T * 16 + 2 * x.c) * KB;
        poi4(x.rs, x.lane16, fo); poi4(x.rs, x.lane16, fo + KB);
        if (x.g == 0) poi2(x.rs, (unsigned)x.tok * 8u, lb + L_STRIDE + O_XS + (unsigned)(x.T * 8 + x.c) * 128u);
      }
    }
    GBAR();
    {
      refresh(x);
      unsigned lb = lb0;
      asm volatile("" : "+s"(lb));
      if (L == 0) WAITV_BAR(3); else WAITV_BAR(0);
      FS(9);
      sig_wait(x, sigv(L, 4));
      FS(18);
      dmaR<316, 348>(x);
      sig_wait(x, sigv(L, 4), M_SIG2);
      dmaR<348, 380>(x);
      FS(19);
      if (L == 0) {
        const unsigned fo = lb + L_STRIDE + O_HB + (unsigned)(x.T * 16 + 2 * x.c) * KB;
        poi4(x.rs, x.lane16, fo); poi4(x.rs, x.lane16, fo + KB);
        poi4(x.rs, x.lane16, lb + L_STRIDE + O_OB + (unsigned)(x.TA * 16 + x.pp) * KB);
        poi4(x.rs, x.lane16, lb + L_STRIDE + O_OB + (unsigned)((x.TA + 1) * 16 + x.pp) * KB);
      }
    }
    GBAR();
    {
      refresh(x);
      if (L == 0) WAITV_BAR(4); else WAITV_BAR(0);
      FS(10);
      dmaRT<380, 400>(x);
      DRAIN_BAR();
      sig_wait(x, sigv(L, 5));
      dmaR<400, 444>(x);
      BAR();
      FS(11);
      dmaR6<444, 556>(x);
    }
    GBAR();
    {
      refresh(x);
      v4u wr[17];
      {
        const unsigned ws0 = OFF_WT + (W6_F + (unsigned)(x.c * 224 + 156 + x.m)) * KB;
#pragma unroll
        for (int i = 0; i < 17; ++i) LDA(wr[i], x.lane16, ws0 + (unsigned)i * 4096u);
      }
      WAITV_BAR(17);
      FS(12);
      BAR();
      asm volatile("s_waitcnt vmcnt(0)" : "+v"(wr[0]), "+v"(wr[1]), "+v"(wr[2]), "+v"(wr[3]), "+v"(wr[4]), "+v"(wr[5]), "+v"(wr[6]), "+v"(wr[7]),
                   "+v"(wr[8]), "+v"(wr[9]), "+v"(wr[10]), "+v"(wr[11]), "+v"(wr[12]), "+v"(wr[13]), "+v"(wr[14]), "+v"(wr[15]), "+v"(wr[16])::"memory");
#pragma unroll
      for (int i = 0; i < 17; ++i) *(LAS v4u *)(x.sm + ((556 + 4 * i) % RING + x.m) * KB + x.lane16) = wr[i];
      DRAIN_BAR();
      FS(13);
      dmaR<624, 712>(x, L == 0);
      BAR();
      dmaR<712, 720>(x, L == 0);
      FS(14);
    }
  }
}

}

__global__ void __launch_bounds__(512, 2) mega_kernel(Params p) {
  extern __shared__ __attribute__((aligned(16))) unsigned char smem_[];
  Ctx x;
  x.sm = (LAS unsigned char *)smem_;
  x.lds0 = (unsigned)(size_t)smem_;
  x.lane = threadIdx.x & 63;
  x.w = __builtin_amdgcn_readfirstlane(threadIdx.x >> 6);
  x.b = blockIdx.x;
  refresh(x);
  const int b = x.b;
  x.spins = 0;
  x.ctl = (gu32 *)p.ws;
  x.rs = __builtin_amdgcn_make_buffer_rsrc((void *)p.ws, 0, WS_BYTES, 0x00020000);
  const bool cw = x.w < 4;

  FSA(0);
  const unsigned tag = __hip_atomic_load(x.ctl + OFF_EPOCH / 4, RLX_AGENT) + 1u;
  unsigned pf0 = 0, pf1 = 0;
  if (x.w == 7) {
    unsigned long long pc;
    asm volatile("s_getpc_b64 %0" : "=s"(pc));
    const GAS unsigned char *cp = (const GAS unsigned char *)(pc & ~127ull) + (size_t)(((b >> 3) + 32 * (x.lane & 15)) * 128);
    const GAS unsigned char *wp = (const GAS unsigned char *)p.ws + (size_t)min(x.lane, 28) * (2 * MB) + 1 * MB;
    if (x.lane >= 32) {
      const int q = x.lane & 7;
      const unsigned short *ip = q == 0 ? p.x : q == 1 ? p.wqkv : q == 2 ? p.wo : q == 3 ? p.w1 : q == 4 ? p.w2 : q == 5 ? p.w3 : q == 6 ? p.w4 : p.x;
      const size_t half = (q == 2 || q == 3 || q == 4) ? 256 * KB : 1 * MB;
      wp = (const GAS unsigned char *)ip + ((x.lane & 8) ? half : 0);
    }
    asm volatile("global_load_dword %0, %2, off\n\tglobal_load_dword %1, %3, off" : "=&v"(pf0), "=&v"(pf1) : "v"(cp), "v"(wp) : "memory");
    asm volatile("s_waitcnt vmcnt(0)" : "+v"(pf0), "+v"(pf1)::"memory");
  }
  v2u xin[4] = {};
  unsigned short wraw[2][4] = {};
  if (cw) {
    const GAS unsigned char *xp = (const GAS unsigned char *)p.x + ((size_t)(16 * x.T + x.tok) * 512 + 64 * x.c + 4 * x.g) * 2;
#pragma unroll
    for (int q = 0; q < 4; ++q) xin[q] = *(const GAS v2u *)(xp + 32 * q);
#pragma unroll
    for (int h = 0; h < 2; ++h) {
      const unsigned short *wp = p.wphi + ((2 * x.pp + h) * 16 + 4 * x.g) * 16 + x.tok;
#pragma unroll
      for (int jj = 0; jj < 4; ++jj) wraw[h][jj] = wp[16 * jj];
    }
  }
  if (threadIdx.x < 9) *(LAS unsigned *)(x.sm + M_SIG + threadIdx.x * 4u) = 0u;
  const int bid = ((x.w + 4) & 7) * 256 + b;
  const unsigned short *src = p.wqkv; int ld = 1536, ra = 0, rb = 0, n0 = 0, kstep = 0, csid = -1; unsigned fr[4] = {0u, 0u, 0u, 0u}, ci[4] = {0u, 0u, 0u, 0u};
  if (bid < 384) {
    kstep = bid & 15; const int nb = bid >> 4; n0 = nb * 64; ra = 32 * kstep; rb = ra + 16;
    const int sec = nb >> 3, h0 = 4 * (nb & 7);
#pragma unroll
    for (int ct = 0; ct < 4; ++ct) {
      const int hh = h0 + ct;
      fr[ct] = W1_F + (unsigned)(((hh >> 1) * 16 + kstep) * 6 + sec * 2 + (hh & 1));
      ci[ct] = (unsigned)((hh >> 1) * 6 + sec * 2 + (hh & 1));
    }
    csid = 0;
  } else if (bid < 768) {
    const int mi = (bid - 384) >> 7, r = (bid - 384) & 127; kstep = r & 15; const int nb = r >> 4;
    src = mi == 0 ? p.wo : (mi == 1 ? p.w1 : p.w2); ld = 512; n0 = nb * 64; ra = 32 * kstep; rb = ra + 16;
#pragma unroll
    for (int ct = 0; ct < 4; ++ct) { fr[ct] = W2_F + (unsigned)(mi * 512 + (nb * 16 + kstep) * 4 + ct); ci[ct] = 96u + (unsigned)(nb * 4 + ct); }
    if (mi == 1) csid = 1;
  } else if (bid < 1216) {
    const int r = bid - 768; kstep = r & 15; const int nb = r >> 4; src = p.w3; ld = 1792; n0 = nb * 64; ra = 32 * kstep; rb = ra + 16;
#pragma unroll
    for (int ct = 0; ct < 4; ++ct) { const int F = nb * 4 + ct; fr[ct] = W5_F + (unsigned)(((F / 7) * 16 + kstep) * 7 + F % 7); ci[ct] = 128u + (unsigned)F; }
    csid = 2;
  } else if (bid < 1664) {
    const int r = bid - 1216; const int nb = r & 7; kstep = r >> 3; src = p.w4; ld = 512; n0 = nb * 64;
    const int u = kstep / 7, q = kstep % 7;
    if (q < 3) { ra = 224 * u + 32 * q; rb = ra + 16; }
    else if (q == 3) { ra = 224 * u + 96; rb = 224 * u + 208; }
    else { ra = 224 * u + 112 + 32 * (q - 4); rb = ra + 16; }
#pragma unroll
    for (int ct = 0; ct < 4; ++ct) fr[ct] = W6_F + (unsigned)((nb * 56 + kstep) * 4 + ct);
  }
  if (bid < 1664) {
    const int ip = x.lane >> 5, uu = (x.lane >> 4) & 1, q = (x.lane >> 2) & 3, c1 = ((x.lane >> 1) & 1) ^ ip, bb = x.lane & 1;
    const int rl = 4 * ip + q, ch = 2 * (2 * uu + c1) + bb;
    const unsigned stg0 = x.lds0 + (unsigned)x.w * 4096u;
#pragma unroll
    for (int u = 0; u < 4; ++u) {
      const int row = (u < 2 ? ra + 8 * u : rb + 8 * (u - 2)) + rl;
      const GAS unsigned char *gp = (const GAS unsigned char *)src + ((size_t)row * ld + n0 + ch * 8) * 2;
      asm volatile("s_mov_b32 m0, %0\n\ts_nop 0\n\tglobal_load_lds_dwordx4 %1, off" ::"s"(stg0 + (unsigned)u * 1024u), "v"(gp) : "memory");
    }
  }
  auto retile = [&]() {
    asm volatile("s_waitcnt vmcnt(0)" ::: "memory");
    const int gq = (x.lane & 15) >> 2, gp_ = x.lane & 3, gi = x.g & 1;
#pragma unroll
    for (int ct = 0; ct < 4; ++ct) {
      const unsigned L = (unsigned)((gi << 5) | ((ct >> 1) << 4) | (gq << 2) | (((ct & 1) ^ gi) << 1) | (gp_ >> 1));
      const unsigned a0 = (unsigned)x.w * 4096u + (unsigned)(x.g >> 1) * 1024u + L * 16u + (unsigned)(gp_ & 1) * 8u;
      const v2u lo = trread(x, a0), hi = trread(x, a0 + 2048u);
      const v4u fragv = {lo.x, lo.y, hi.x, hi.y};
      stg(x.rs, x.lane16, __builtin_amdgcn_readfirstlane(OFF_WT + fr[ct] * KB), fragv);
      if (csid >= 0) {
        const f4 cs = mfma32(fragv, v4u{0x3F803F80u, 0x3F803F80u, 0x3F803F80u, 0x3F803F80u}, f4{0.f, 0.f, 0.f, 0.f});
        const unsigned co = __builtin_amdgcn_readfirstlane(OFF_CSP + (ci[ct] * 16u + (unsigned)kstep) * 64u);
        if (x.tok == 0) stg(x.rs, (unsigned)x.g * 16u, co, __builtin_bit_cast(v4u, cs));
      }
    }
  };
  if (bid < 384) retile();
  FSA(1);
  v4u xres[2];
  v2u wph[2];
  if (cw) {
    xres[0] = clamp4(v4u{xin[0].x, xin[0].y, xin[1].x, xin[1].y});
    xres[1] = clamp4(v4u{xin[2].x, xin[2].y, xin[3].x, xin[3].y});
    const unsigned fo = OFF_L0 + O_XB + (unsigned)(x.T * 16 + 2 * x.c) * KB;
    stg(x.rs, x.lane16, fo, xres[0]); stg(x.rs, x.lane16, fo + KB, xres[1]);
    put_stats(x, xres[0], xres[1], OFF_L0 + O_XS);
#pragma unroll
    for (int h = 0; h < 2; ++h)
      wph[h] = v2u{(unsigned)wraw[h][0] | ((unsigned)wraw[h][1] << 16), (unsigned)wraw[h][2] | ((unsigned)wraw[h][3] << 16)};
  } else {
    xres[0] = xres[1] = v4u{0u, 0u, 0u, 0u};
    wph[0] = wph[1] = v2u{0u, 0u};
  }
  FSA(2);
  asm volatile("s_waitcnt vmcnt(0) lgkmcnt(0)\n\ts_barrier" ::: "memory");
  FSA(3);
  if (threadIdx.x == 0) __hip_atomic_store(x.ctl + OFF_FLAGS / 4 + b * 16, tag, RLX_AGENT);
  if (bid >= 384 && bid < 1664) retile();
  if (!cw) {
    poi4(x.rs, x.lane16, OFF_L0 + O_KVP + (unsigned)((x.pp * 16 + x.tau) * 4 + x.m) * KB);
    if (x.tok == 0) poi4(x.rs, (unsigned)x.g * 16u, OFF_L0 + O_KVZ + (unsigned)((x.pp * 4 + x.m) * 16 + x.tau) * 64u);
    poi4(x.rs, x.lane16, OFF_L0 + O_OB + (unsigned)(x.TA * 16 + x.pp) * KB);
    poi4(x.rs, x.lane16, OFF_L0 + O_OB + (unsigned)((x.TA + 1) * 16 + x.pp) * KB);
    poi4(x.rs, x.lane16, OFF_L0 + O_HB + (unsigned)(x.T * 16 + 2 * x.c) * KB);
    poi4(x.rs, x.lane16, OFF_L0 + O_HB + (unsigned)(x.T * 16 + 2 * x.c + 1) * KB);
    poi4(x.rs, x.lane16, OFF_L0 + L_STRIDE + O_KVP + (unsigned)((x.pp * 16 + x.tau) * 4 + x.m) * KB);
    if (x.tok == 0) poi4(x.rs, (unsigned)x.g * 16u, OFF_L0 + L_STRIDE + O_KVZ + (unsigned)((x.pp * 4 + x.m) * 16 + x.tau) * 64u);
  }
  if (x.w == 4) {
    poll_flags(x, OFF_FLAGS, tag);
    if (b == 0 && x.lane == 0) __hip_atomic_store(x.ctl + OFF_EPOCH / 4, tag, RLX_AGENT);
  }
  BAR();
  FSA(4);
  if (cw) {
    compute_path(x, p, xres, wph, tag);
  } else {
    v4u cv[2];
#pragma unroll
    for (int jj = 0; jj < 2; ++jj) LDA(cv[jj], x.lane16, OFF_CSP + (unsigned)(x.pp * 6 + min(x.m + 4 * jj, 5)) * KB);
    dmaR<POS1, POS2>(x);
#pragma unroll
    for (int jj = 0; jj < 2; ++jj) {
      WAITV1(24, cv[jj]);
      const f4 s = shsum(__builtin_bit_cast(f4, cv[jj]));
      const int u = x.m + 4 * jj;
      if (u < 6 && x.lane < 4) *(LAS f4 *)(x.sm + M_CS + (unsigned)u * 64u + (unsigned)x.lane * 16u) = s;
    }
    helper_path(x, tag);
  }
  asm volatile("s_waitcnt vmcnt(0)" ::: "memory");
}

extern "C" void kernel_launch(void *const *d_in, const int *in_sizes, int n_in, void *d_out, int out_size, void *d_ws, size_t ws_size,
                              hipStream_t stream) {
  Params p;
  p.x = (const unsigned short *)d_in[0];
  p.wqkv = (const unsigned short *)d_in[1];
  p.wphi = (const unsigned short *)d_in[2];
  p.wo = (const unsigned short *)d_in[3];
  p.w1 = (const unsigned short *)d_in[5];
  p.w2 = (const unsigned short *)d_in[6];
  p.w3 = (const unsigned short *)d_in[7];
  p.w4 = (const unsigned short *)d_in[8];
  p.out = (unsigned short *)d_out;
  p.ws = (unsigned char *)d_ws;
  (void)hipFuncSetAttribute((const void *)mega_kernel, hipFuncAttributeMaxDynamicSharedMemorySize, LDS_BYTES);
  hipLaunchKernelGGL(mega_kernel, dim3(256), dim3(512), LDS_BYTES, stream, p);
}
```

```cpp
#include <hip/hip_runtime.h>
#include <stdint.h>

#define GAS __attribute__((address_space(1)))
#define LAS __attribute__((address_space(3)))
#define DI __device__ __forceinline__
typedef unsigned v4u __attribute__((ext_vector_type(4)));
typedef unsigned v2u __attribute__((ext_vector_type(2)));
typedef float f4 __attribute__((ext_vector_type(4)));
typedef float f2 __attribute__((ext_vector_type(2)));
typedef short s8 __attribute__((ext_vector_type(8)));
typedef short s4 __attribute__((ext_vector_type(4)));
typedef __bf16 bf2 __attribute__((ext_vector_type(2)));
typedef GAS unsigned gu32;
typedef __amdgpu_buffer_rsrc_t rsrc_t;
#define RLX_AGENT __ATOMIC_RELAXED, __HIP_MEMORY_SCOPE_AGENT

#define DBG_GBAR 0
#define DBG_NOPOISON 0
#define DBG_STAMP 0
#define SPEC 0
#define PREFETCH 1
#define KV16 1
#define WARM 0

namespace {
constexpr unsigned MB = 1u << 20, KB = 1024u;
constexpr unsigned OFF_EPOCH = 0, OFF_ABORT = 256, OFF_GBAR = 512, OFF_FLAGS = 16384, OFF_FLAGS2 = 32768;
constexpr unsigned OFF_WT = 1 * MB;
constexpr unsigned W1_F = 0, W2_F = 1536, W5_F = 3072, W6_F = 4864;
constexpr unsigned OFF_CSP = 8 * MB;
constexpr unsigned OFF_L0 = 9 * MB, L_STRIDE = 24 * MB;
constexpr unsigned O_XB = 0, O_XS = 2 * MB, O_KVP = 2 * MB + 512 * KB, O_KVZ = 3 * MB + 512 * KB, O_OB = 4 * MB, O_X1B = 6 * MB,
                   O_X1S = 8 * MB, O_HB = 9 * MB, O_X2B = 11 * MB, O_X2S = 13 * MB, O_H3B = 14 * MB;
constexpr unsigned WS_BYTES = OFF_L0 + 2 * L_STRIDE;
constexpr unsigned OOB = 0x7F000000u;
constexpr int RING = 156;
constexpr unsigned MISC = RING * KB;
constexpr unsigned M_CS = MISC, M_KVA = MISC + 1088, M_ZA = MISC + 3136, M_SIG = MISC + 3264, M_SIG2 = MISC + 3280, M_RDY = MISC + 3296, M_FLAG = MISC + 3300;
constexpr unsigned IMG = 0;
constexpr unsigned LDS_BYTES = 163840;
constexpr int POS1 = 0, POS2 = 96, POS3 = 160, POS4 = 224, POS5 = 288, POS6 = 400, POSE = 624;
constexpr unsigned SENT = 0xFFFFFFFFu;
constexpr unsigned SPIN_CAP = 1u << 13;
constexpr int AUX_SC1 = 16;
#define DLY_KV 14
#define DLY_1 19
#define DLY_2 19
#define DLY_3 19
#define DLY_4 19
#define DLY_5 19
#define DLY_6 19

struct Params {
  const unsigned short *x, *wqkv, *wphi, *wo, *w1, *w2, *w3, *w4;
  unsigned short *out;
  unsigned char *ws;
};

DI unsigned pk2(float a, float b) { f2 v = {a, b}; bf2 r = __builtin_convertvector(v, bf2); return __builtin_bit_cast(unsigned, r); }
DI float asf(unsigned u) { return __builtin_bit_cast(float, u); }
DI float blo(unsigned u) { return __builtin_bit_cast(float, u << 16); }
DI float bhi(unsigned u) { return __builtin_bit_cast(float, u & 0xFFFF0000u); }
DI unsigned clampw(unsigned u) { return u < 0xFFFFFFFEu ? u : 0xFFFFFFFEu; }
DI unsigned clampf(float f) { return clampw(__builtin_bit_cast(unsigned, f)); }
DI v4u clamp4(v4u v) { return v4u{clampw(v.x), clampw(v.y), clampw(v.z), clampw(v.w)}; }
DI v4u pack8(f4 a, f4 b) { return clamp4(v4u{pk2(a.x, a.y), pk2(a.z, a.w), pk2(b.x, b.y), pk2(b.z, b.w)}); }
DI f4 ulo(v4u v) { return f4{blo(v.x), bhi(v.x), blo(v.y), bhi(v.y)}; }
DI f4 uhi(v4u v) { return f4{blo(v.z), bhi(v.z), blo(v.w), bhi(v.w)}; }
DI f4 mfma32(v4u a, v4u b, f4 c) {
  return __builtin_amdgcn_mfma_f32_16x16x32_bf16(__builtin_bit_cast(s8, a), __builtin_bit_cast(s8, b), c, 0, 0, 0);
}
DI f4 mfma16(v2u a, v2u b, f4 c) {
  return __builtin_amdgcn_mfma_f32_16x16x16bf16_1k(__builtin_bit_cast(s4, a), __builtin_bit_cast(s4, b), c, 0, 0, 0);
}
DI v4u ldg(rsrc_t rs, unsigned voff, unsigned soff) { return __builtin_amdgcn_raw_buffer_load_b128(rs, voff, soff, AUX_SC1); }
DI void stg(rsrc_t rs, unsigned voff, unsigned soff, v4u v) {
  __builtin_amdgcn_raw_buffer_store_b128(v, rs, voff, soff, AUX_SC1);
  asm volatile("s_nop 1" ::"v"(v));
}
DI void stg2(rsrc_t rs, unsigned voff, unsigned soff, v2u v) { __builtin_amdgcn_raw_buffer_store_b64(v, rs, voff, soff, AUX_SC1); }
DI void poi4(rsrc_t rs, unsigned voff, unsigned soff) { if (!DBG_NOPOISON) stg(rs, voff, soff, v4u{0xFFFFFFFFu, 0xFFFFFFFFu, 0xFFFFFFFFu, 0xFFFFFFFFu}); }
DI void poi2(rsrc_t rs, unsigned voff, unsigned soff) { if (!DBG_NOPOISON) stg2(rs, voff, soff, v2u{0xFFFFFFFFu, 0xFFFFFFFFu}); }
DI unsigned max4(v4u v) { return max(max(v.x, v.y), max(v.z, v.w)); }
DI bool anybad(unsigned mx) { return __any(mx == SENT) != 0; }
DI float xsum(float x) {
  unsigned u = __builtin_bit_cast(unsigned, x);
  auto r = __builtin_amdgcn_permlane16_swap(u, u, false, false);
  x = __builtin_bit_cast(float, (unsigned)r[0]) + __builtin_bit_cast(float, (unsigned)r[1]);
  u = __builtin_bit_cast(unsigned, x);
  auto q = __builtin_amdgcn_permlane32_swap(u, u, false, false);
  return __builtin_bit_cast(float, (unsigned)q[0]) + __builtin_bit_cast(float, (unsigned)q[1]);
}
DI float xmax(float x) {
  unsigned u = __builtin_bit_cast(unsigned, x);
  auto r = __builtin_amdgcn_permlane16_swap(u, u, false, false);
  x = fmaxf(__builtin_bit_cast(float, (unsigned)r[0]), __builtin_bit_cast(float, (unsigned)r[1]));
  u = __builtin_bit_cast(unsigned, x);
  auto q = __builtin_amdgcn_permlane32_swap(u, u, false, false);
  return fmaxf(__builtin_bit_cast(float, (unsigned)q[0]), __builtin_bit_cast(float, (unsigned)q[1]));
}
template <bool MAX> DI float red_op(float a, float b) { return MAX ? fmaxf(a, b) : a + b; }
DI unsigned fbits(float f) { return __builtin_bit_cast(unsigned, f); }
template <bool MAX> DI void xred4(float &a, float &b, float &c, float &d) {
  const auto r1 = __builtin_amdgcn_permlane16_swap(fbits(a), fbits(b), false, false);
  const auto r2 = __builtin_amdgcn_permlane16_swap(fbits(c), fbits(d), false, false);
  const float c1 = red_op<MAX>(asf(r1[0]), asf(r1[1])), c2 = red_op<MAX>(asf(r2[0]), asf(r2[1]));
  const auto r3 = __builtin_amdgcn_permlane32_swap(fbits(c1), fbits(c2), false, false);
  const unsigned e = fbits(red_op<MAX>(asf(r3[0]), asf(r3[1])));
  const auto r4 = __builtin_amdgcn_permlane32_swap(e, e, false, false);
  const unsigned f0 = r4[0], f1 = r4[1];
  const auto r5 = __builtin_amdgcn_permlane16_swap(f0, f0, false, false);
  const auto r6 = __builtin_amdgcn_permlane16_swap(f1, f1, false, false);
  a = asf(r5[0]); b = asf(r5[1]); c = asf(r6[0]); d = asf(r6[1]);
}
template <bool MAX> DI void xred2(float &a, float &b) {
  const auto r1 = __builtin_amdgcn_permlane16_swap(fbits(a), fbits(b), false, false);
  const unsigned c = fbits(red_op<MAX>(asf(r1[0]), asf(r1[1])));
  const auto r3 = __builtin_amdgcn_permlane32_swap(c, c, false, false);
  const unsigned e = fbits(red_op<MAX>(asf(r3[0]), asf(r3[1])));
  const auto r5 = __builtin_amdgcn_permlane16_swap(e, e, false, false);
  a = asf(r5[0]); b = asf(r5[1]);
}
DI f2 gelu2(f2 x) {
  const f2 c1 = {-2.302208198f, -2.302208198f}, c2 = {-0.1029432397f, -0.1029432397f}, one = {1.0f, 1.0f};
  const f2 t = x * __builtin_elementwise_fma(x * x, c2, c1);
  const f2 d = one + f2{__builtin_amdgcn_exp2f(t.x), __builtin_amdgcn_exp2f(t.y)};
  return x * f2{__builtin_amdgcn_rcpf(d.x), __builtin_amdgcn_rcpf(d.y)};
}
DI f4 gelu4(f4 y) {
  const f2 a = gelu2(f2{y.x, y.y}), b = gelu2(f2{y.z, y.w});
  return f4{a.x, a.y, b.x, b.y};
}

#define FSL 1
#define FS(i) do {} while (0)
#define FSA(i) do { unsigned long long t_; asm volatile("s_memtime %0\n\ts_waitcnt lgkmcnt(0)" : "=s"(t_) :: "memory"); \
  int ww_ = x.w; asm volatile("" : "+s"(ww_)); \
  unsigned a_ = (ww_ == 0) ? (70 + (i)) * 4u : ((ww_ == 4) ? 400u + (70 + (i)) * 4u : 780u); \
  asm volatile("" : "+s"(a_)); \
  *(LAS unsigned *)(x.sm + M_FLAG + a_) = (unsigned)t_; } while (0)
#define FSP(i) do {} while (0)
#define SB() __builtin_amdgcn_sched_barrier(0)
#define MARK(s) asm volatile("; MARK " s)
#define BAR() asm volatile("s_waitcnt lgkmcnt(0)\n\ts_barrier" ::: "memory")
#define WAITV_BAR(n) asm volatile("s_waitcnt vmcnt(" #n ") lgkmcnt(0)\n\ts_barrier" ::: "memory")
#define DRAIN_BAR() asm volatile("s_waitcnt vmcnt(0) lgkmcnt(0)\n\ts_barrier" ::: "memory")
DI void sleepn(int n) {
  for (int i = 0; i < n; ++i) __builtin_amdgcn_s_sleep(1);
}

struct Ctx {
  rsrc_t rs;
  LAS unsigned char *sm;
  unsigned lds0;
  unsigned lane16;
  LAS unsigned char *ab[3];
  int lane, w, m, g, tok;
  int t, c, tau, pp, b;
  int T, TA;
  unsigned spins;
  gu32 *ctl;
};
DI void refresh(Ctx &x) {
  asm volatile("" : "+s"(x.w), "+s"(x.b), "+v"(x.lane));
  x.lane16 = (unsigned)x.lane * 16u;
  x.g = x.lane >> 4; x.tok = x.lane & 15;
  x.m = x.w & 3;
  const int xc = x.b & 7, j = x.b >> 3;
  x.t = 4 * xc + (j >> 3); x.c = j & 7;
  x.tau = 2 * xc + (j >> 4); x.pp = j & 15;
  x.T = 4 * x.t + x.m;
  x.TA = 8 * x.tau + 2 * x.m;
  x.ab[0] = x.sm + x.lane16;
  unsigned o1 = x.lane16 + 60u * KB, o2 = x.lane16 + 120u * KB;
  asm volatile("" : "+v"(o1), "+v"(o2));
  x.ab[1] = x.sm + o1; x.ab[2] = x.sm + o2;
}

template <int LO, int HI, int THR> DI void dmaSeg(const Ctx &x, bool live) {
  constexpr int Q = LO % POSE;
  int w = x.w, b = x.b;
  asm volatile("" : "+s"(w), "+s"(b));
  const int m = w & 3, j = b >> 3;
  unsigned fr;
  if constexpr (Q < POS2) fr = W1_F + (unsigned)((j & 15) * 96 + Q + m);
  else if constexpr (Q < POS5) fr = W2_F + (unsigned)(((Q - POS2) / 64) * 512 + (j & 7) * 64 + (Q - POS2) % 64 + m);
  else if constexpr (Q < POS6) fr = W5_F + (unsigned)((j & 15) * 112 + (Q - POS5) + m);
  else fr = W6_F + (unsigned)((j & 7) * 224 + (Q - POS6) + m);
  unsigned so = live ? OFF_WT + fr * KB : OOB;
  unsigned lds = x.lds0 + (unsigned)(LO % RING + m) * KB;
  const unsigned wrap = x.lds0 + (unsigned)(RING + m) * KB;
#pragma unroll 1
  for (int i = 0; i < (HI - LO) / 4; ++i) {
    asm volatile("s_mov_b32 m0, %0\n\ts_nop 0\n\tbuffer_load_dwordx4 %1, %2, %3 offen sc1 lds" ::"s"(lds), "v"(x.lane16), "s"(x.rs), "s"(so) : "memory");
    if constexpr (THR == 3) asm volatile("s_waitcnt vmcnt(3)" ::: "memory");
    if constexpr (THR == 5) asm volatile("s_waitcnt vmcnt(5)" ::: "memory");
    so += 4u * KB;
    lds += 4u * KB;
    lds = lds >= wrap ? lds - RING * KB : lds;
  }
}
constexpr int mat_end(int p) {
  const int base = p - p % POSE, q = p % POSE;
  return base + (q < POS2 ? POS2 : q < POS3 ? POS3 : q < POS4 ? POS4 : q < POS5 ? POS5 : q < POS6 ? POS6 : POSE);
}
template <int LO, int HI, int THR = -1> DI void dmaR(const Ctx &x, bool live = true) {
  if constexpr (LO < HI) {
    constexpr int E = mat_end(LO) < HI ? mat_end(LO) : HI;
    dmaSeg<LO, E, THR>(x, live);
    dmaR<E, HI, THR>(x, live);
  }
}
template <int LO, int HI> DI void dmaRT(const Ctx &x, bool live = true) { dmaR<LO, HI, 3>(x, live); }
template <int LO, int HI> DI void dmaR6(const Ctx &x, bool live = true) { dmaR<LO, HI, 5>(x, live); }
DI void sig_set(const Ctx &x, unsigned v, unsigned word = M_SIG) { *(volatile LAS unsigned *)(x.sm + word + (unsigned)x.m * 4u) = v; }
DI void sig_wait(const Ctx &x, unsigned v, unsigned word = M_SIG) {
  for (unsigned sp = 0; sp < (1u << 20); ++sp) {
    const v4u s = *(volatile LAS v4u *)(x.sm + word);
    if (__builtin_amdgcn_readfirstlane(min(min(s.x, s.y), min(s.z, s.w))) >= v) break;
    __builtin_amdgcn_s_sleep(1);
  }
}

#define LDA(dst, voff, soff) \
  asm volatile("buffer_load_dwordx4 %0, %1, %2, %3 offen sc1" : "=v"(dst) : "v"(voff), "s"(x.rs), "s"(soff) : "memory")
#define LDA2(dst, voff, soff, imm) \
  asm volatile("buffer_load_dwordx2 %0, %1, %2, %3 offen offset:%4 sc1" : "=v"(dst) : "v"(voff), "s"(x.rs), "s"(soff), "i"(imm) : "memory")
#define WAITV1(n, r) asm volatile("s_waitcnt vmcnt(%1)" : "+v"(r) : "i"(n) : "memory")
#define WAITV2(n, r, q) asm volatile("s_waitcnt vmcnt(%2)" : "+v"(r), "+v"(q) : "i"(n) : "memory")
#define TIE(r) asm volatile("" : "+v"(r) : : "memory")
DI bool giveup(Ctx &x) {
  if (++x.spins > SPIN_CAP) {
    if (x.lane == 0) __hip_atomic_store(x.ctl + OFF_ABORT / 4, 1u, RLX_AGENT);
    return true;
  }
  __builtin_amdgcn_s_sleep(1);
  return false;
}
DI v4u load_sure(Ctx &x, unsigned voff, unsigned soff) {
  v4u v;
  while (true) {
    asm volatile("buffer_load_dwordx4 %0, %1, %2, %3 offen sc1\n\ts_waitcnt vmcnt(0)" : "=&v"(v) : "v"(voff), "s"(x.rs), "s"(soff) : "memory");
    if (!anybad(max4(v)) || giveup(x)) break;
  }
  return v;
}
DI v2u load_sure2(Ctx &x, unsigned voff, unsigned soff) {
  v2u v;
  while (true) {
    asm volatile("buffer_load_dwordx2 %0, %1, %2, %3 offen sc1\n\ts_waitcnt vmcnt(0)" : "=&v"(v) : "v"(voff), "s"(x.rs), "s"(soff) : "memory");
    if (!anybad(max(v.x, v.y)) || giveup(x)) break;
  }
  return v;
}

DI void warm(Ctx &x, unsigned voff, unsigned soff, unsigned soff2) {
}
#define LDC(dst, voff, soff) \
  asm volatile("buffer_load_dword %0, %1, %2, %3 offen sc1" : "=v"(dst) : "v"(voff), "s"(x.rs), "s"(soff) : "memory")

#define GBAR() do {} while (0)
#define SLEEP(n) sleepn(n)

DI void put_stats(const Ctx &x, v4u u, v4u v, unsigned sbuf) {
  const f4 a = ulo(u), b = uhi(u), c = ulo(v), d = uhi(v);
  const f4 s = (a + b) + (c + d), q = (a * a + b * b) + (c * c + d * d);
  float s1 = (s.x + s.y) + (s.z + s.w), s2 = (q.x + q.y) + (q.z + q.w);
  xred2<false>(s1, s2);
  if (x.g == 0) stg2(x.rs, (unsigned)x.tok * 8u, sbuf + (unsigned)(x.T * 8 + x.c) * 128u, v2u{clampf(s1), clampf(s2)});
}
struct Stats { v2u v[2]; };
DI void stats_load(const Ctx &x, Stats &s, unsigned soffT) {
  const unsigned vo = (unsigned)(x.g * 256 + x.tok * 8);
  LDA2(s.v[0], vo, soffT, 0); LDA2(s.v[1], vo, soffT, 128);
}
DI void stats_fin(Ctx &x, Stats &s, unsigned soffT, float &mean, float &var) {
  TIE(s.v[0]); TIE(s.v[1]);
  if (__builtin_expect(anybad(max(max(s.v[0].x, s.v[0].y), max(s.v[1].x, s.v[1].y))), 0)) {
    s.v[0] = load_sure2(x, (unsigned)(x.g * 256 + x.tok * 8), soffT);
    s.v[1] = load_sure2(x, (unsigned)(x.g * 256 + x.tok * 8 + 128), soffT);
  }
  float s1 = asf(s.v[0].x) + asf(s.v[1].x), s2 = asf(s.v[0].y) + asf(s.v[1].y);
  xred2<false>(s1, s2);
  mean = s1 * (1.0f / 512.0f);
  var = fmaxf(__builtin_fmaf(-mean, mean, s2 * (1.0f / 512.0f)), 0.0f);
}
DI const LAS v4u *lds4(const Ctx &x, unsigned off) { return (const LAS v4u *)(x.sm + off); }
template <int NT, int P> DI void ldaN(const Ctx &x, v4u (&a)[NT]) {
#pragma unroll
  for (int f = 0; f < NT; ++f) {
    const int slot = (P + f) % RING;
    a[f] = *(const LAS v4u *)(x.ab[slot / 60] + (slot % 60) * KB);
  }
}
template <int NT> DI void tieN(v4u (&a)[NT]) {
  if constexpr (NT == 4) asm volatile("" : "+v"(a[0]), "+v"(a[1]), "+v"(a[2]), "+v"(a[3]));
  else if constexpr (NT == 6) asm volatile("" : "+v"(a[0]), "+v"(a[1]), "+v"(a[2]), "+v"(a[3]), "+v"(a[4]), "+v"(a[5]));
  else asm volatile("" : "+v"(a[0]), "+v"(a[1]), "+v"(a[2]), "+v"(a[3]), "+v"(a[4]), "+v"(a[5]), "+v"(a[6]));
}
DI unsigned mx4(unsigned m, v4u v) {
  unsigned r;
  asm("v_max3_u32 %0, %1, %2, %3" : "=v"(r) : "v"(m), "v"(v.x), "v"(v.y));
  asm("v_max3_u32 %0, %1, %2, %3" : "=v"(m) : "v"(r), "v"(v.z), "v"(v.w));
  return m;
}
DI f4 ldcs(const Ctx &x, int tile) { return *(const LAS f4 *)(x.sm + M_CS + (unsigned)tile * 64u + (unsigned)x.g * 16u); }
constexpr int imin(int a, int b) { return a < b ? a : b; }

template <bool SH> DI unsigned vof(const Ctx &x, int k, const unsigned (&vo)[4]) { return SH ? vo[k >> 2] : x.lane16; }
template <int NK> constexpr int depth1() { return 8; }
template <int POS, int NK, bool SH, int K0, int K1, int I> struct KL1 {
  static DI void run(Ctx &x, v4u (&bf)[NK], f4 (&acc)[4], unsigned &mx, v4u (&cur)[4], v4u (&nxt)[4], unsigned so, const unsigned (&vo)[4],
                     unsigned sig) {
    v4u nn[4];
    constexpr int DP = depth1<NK>();
    WAITV1(imin(DP - 1, NK - 1 - I), bf[I]);
    tieN<4>(cur);
    SB();
    acc[0] = mfma32(cur[0], bf[I], acc[0]);
    SB();
    if constexpr (I + DP < NK) LDA(bf[I + DP], vof<SH>(x, I + DP, vo), so + (unsigned)(I + DP) * KB);
    if constexpr (I + DP == NK - 1) { if (sig) sig_set(x, sig); }
    SB();
    if constexpr (I + 2 < K1) ldaN<4, POS + 4 * (I + 2)>(x, nn);
    mx = mx4(mx, bf[I]);
#pragma unroll
    for (int f = 1; f < 4; ++f) acc[f] = mfma32(cur[f], bf[I], acc[f]);
    __builtin_amdgcn_sched_group_barrier(0x100, 1, 0);
#pragma unroll
    for (int f = 1; f < 4; ++f) {
      __builtin_amdgcn_sched_group_barrier(0x008, 1, 0);
      __builtin_amdgcn_sched_group_barrier(0x100, 1, 0);
      __builtin_amdgcn_sched_group_barrier(0x002, 1, 0);
    }
    SB();
    if constexpr (I + 1 < K1) KL1<POS, NK, SH, K0, K1, I + 1>::run(x, bf, acc, mx, nxt, nn, so, vo, sig);
  }
};
template <int POS, bool SH, int K1, int I> struct KL1R {
  static DI void run(Ctx &x, f4 (&acc)[4], unsigned so, const unsigned (&vo)[4]) {
    const v4u bv = load_sure(x, vof<SH>(x, I, vo), so + (unsigned)I * KB);
    v4u a[4];
    ldaN<4, POS + 4 * I>(x, a);
#pragma unroll
    for (int f = 0; f < 4; ++f) acc[f] = mfma32(a[f], bv, acc[f]);
    if constexpr (I + 1 < K1) KL1R<POS, SH, K1, I + 1>::run(x, acc, so, vo);
  }
};
template <bool ST> DI unsigned canary_poll1(Ctx &x, unsigned so, unsigned cvo, Stats &st, unsigned sst) {
  unsigned cn;
  LDC(cn, cvo, so);
  if constexpr (ST) stats_load(x, st, sst);
  WAITV1(0, cn);
  if constexpr (ST) { TIE(st.v[0]); TIE(st.v[1]); cn = max(cn, max(max(st.v[0].x, st.v[0].y), max(st.v[1].x, st.v[1].y))); }
  return cn;
}
template <int NK, bool SH, bool ST>
DI void pro1(Ctx &x, v4u (&bf)[NK], unsigned so, const unsigned (&vo)[4], unsigned cvo, Stats &st, unsigned sst, unsigned &cn) {
  while (anybad(canary_poll1<ST>(x, so, cvo, st, sst)) && !giveup(x)) {}
  cn = 0;
#pragma unroll
  for (int i = 0; i < depth1<NK>(); ++i) LDA(bf[i], vof<SH>(x, i, vo), so + (unsigned)i * KB);
}
template <int NK, bool SH, bool ST>
DI void chk1(Ctx &x, v4u (&bf)[NK], unsigned so, const unsigned (&vo)[4], unsigned cvo, Stats &st, unsigned sst, unsigned cn) {
}
template <int POS, int NK, bool SH, int K0, int K1>
DI void kloop1(Ctx &x, v4u (&bf)[NK], f4 (&acc)[4], unsigned so, const unsigned (&vo)[4], unsigned sig) {
  f4 save[4];
#pragma unroll
  for (int f = 0; f < 4; ++f) save[f] = acc[f];
  unsigned mx = 0;
  v4u a0[4], a1[4];
  ldaN<4, POS + 4 * K0>(x, a0);
  ldaN<4, POS + 4 * (K0 + 1)>(x, a1);
  KL1<POS, NK, SH, K0, K1, K0>::run(x, bf, acc, mx, a0, a1, so, vo, sig);
  if (__builtin_expect(anybad(mx), 0)) {
#pragma unroll
    for (int f = 0; f < 4; ++f) acc[f] = save[f];
    KL1R<POS, SH, K1, K0>::run(x, acc, so, vo);
  }
}
template <int NT, int POS, int K0, int K1, int I> struct KL2 {
  static DI void run(Ctx &x, v4u (&bf)[16][2], f4 (&acc)[NT][2], unsigned &mx, v4u (&cur)[NT], unsigned so, unsigned sig) {
    v4u nxt[NT];
    WAITV2(2 * imin(3, 15 - I), bf[I][0], bf[I][1]);
    tieN<NT>(cur);
    SB();
    acc[0][0] = mfma32(cur[0], bf[I][0], acc[0][0]);
    acc[0][1] = mfma32(cur[0], bf[I][1], acc[0][1]);
    SB();
    if constexpr (I + 4 < 16) LDA(bf[I + 4][0], x.lane16, so + (unsigned)(I + 4) * KB);
    SB();
    acc[1][0] = mfma32(cur[1], bf[I][0], acc[1][0]);
    acc[1][1] = mfma32(cur[1], bf[I][1], acc[1][1]);
    SB();
    if constexpr (I + 4 < 16) LDA(bf[I + 4][1], x.lane16, so + (unsigned)(16 + I + 4) * KB);
    if constexpr (I == (NT == 6 ? 6 : 11)) sig_set(x, sig);
    SB();
    if constexpr (I + 1 < K1) ldaN<NT, POS + (I + 1) * NT>(x, nxt);
    mx = mx4(mx4(mx, bf[I][0]), bf[I][1]);
#pragma unroll
    for (int f = 2; f < NT; ++f) {
      acc[f][0] = mfma32(cur[f], bf[I][0], acc[f][0]);
      acc[f][1] = mfma32(cur[f], bf[I][1], acc[f][1]);
    }
#pragma unroll
    for (int f = 0; f < NT; ++f) {
      __builtin_amdgcn_sched_group_barrier(0x100, 1, 0);
      __builtin_amdgcn_sched_group_barrier(0x008, 1, 0);
    }
#pragma unroll
    for (int f = 0; f < NT - 4; ++f) {
      __builtin_amdgcn_sched_group_barrier(0x008, 1, 0);
      __builtin_amdgcn_sched_group_barrier(0x002, 2, 0);
    }
    SB();
    if constexpr (I + 1 < K1) KL2<NT, POS, K0, K1, I + 1>::run(x, bf, acc, mx, nxt, so, sig);
  }
};
template <int NT, int POS, int K1, int I> struct KL2R {
  static DI void run(Ctx &x, f4 (&acc)[NT][2], unsigned so) {
    const v4u b0 = load_sure(x, x.lane16, so + (unsigned)I * KB), b1 = load_sure(x, x.lane16, so + (unsigned)(16 + I) * KB);
    v4u a[NT];
    ldaN<NT, POS + I * NT>(x, a);
#pragma unroll
    for (int f = 0; f < NT; ++f) {
      acc[f][0] = mfma32(a[f], b0, acc[f][0]);
      acc[f][1] = mfma32(a[f], b1, acc[f][1]);
    }
    if constexpr (I + 1 < K1) KL2R<NT, POS, K1, I + 1>::run(x, acc, so);
  }
};
DI unsigned stmax(const Stats (&st)[2]) {
  return max(max(max(st[0].v[0].x, st[0].v[0].y), max(st[0].v[1].x, st[0].v[1].y)), max(max(st[1].v[0].x, st[1].v[0].y), max(st[1].v[1].x, st[1].v[1].y)));
}
DI void poll2(Ctx &x, Stats (&st)[2], unsigned so, unsigned sst) {
  const unsigned cvo = (unsigned)(x.lane & 31) * KB + 1020u;
  while (true) {
    unsigned cn;
    LDC(cn, cvo, so);
    stats_load(x, st[0], sst); stats_load(x, st[1], sst + 1024u);
    WAITV1(0, cn);
    TIE(st[0].v[0]); TIE(st[0].v[1]); TIE(st[1].v[0]); TIE(st[1].v[1]);
    if (!anybad(max(cn, stmax(st))) || giveup(x)) break;
  }
}
DI void first2(Ctx &x, v4u (&bf)[16][2], unsigned so) {
#pragma unroll
  for (int i = 0; i < 4; ++i) { LDA(bf[i][0], x.lane16, so + (unsigned)i * KB); LDA(bf[i][1], x.lane16, so + (unsigned)(16 + i) * KB); }
}
DI void pro2(Ctx &x, v4u (&bf)[16][2], Stats (&st)[2], unsigned so, unsigned sst, unsigned &cn) {
  poll2(x, st, so, sst);
  cn = 0;
  first2(x, bf, so);
}
DI void chk2(Ctx &x, v4u (&bf)[16][2], Stats (&st)[2], unsigned so, unsigned sst, unsigned cn) {
}
template <int NT, int POS, int K0, int K1>
DI void kloop2(Ctx &x, v4u (&bf)[16][2], f4 (&acc)[NT][2], unsigned so, unsigned sig, unsigned &mx) {
  v4u a0[NT];
  ldaN<NT, POS + K0 * NT>(x, a0);
  KL2<NT, POS, K0, K1, K0>::run(x, bf, acc, mx, a0, so, sig);
}
template <int NT, int POS> DI void redo2(Ctx &x, f4 (&acc)[NT][2], unsigned so) {
#pragma unroll
  for (int f = 0; f < NT; ++f) { acc[f][0] = f4{0.f, 0.f, 0.f, 0.f}; acc[f][1] = f4{0.f, 0.f, 0.f, 0.f}; }
  KL2R<NT, POS, 16, 0>::run(x, acc, so);
}

DI void featmap4(const f4 (&v)[4], const v2u (&wphi)[2], v2u (&plus)[4], v2u (&minus)[4]) {
  const float L2E = 1.4426950408889634f;
  f4 p[4];
#pragma unroll
  for (int i = 0; i < 4; ++i) p[i] = mfma16(wphi[i & 1], v2u{pk2(v[i].x, v[i].y), pk2(v[i].z, v[i].w)}, f4{0.f, 0.f, 0.f, 0.f});
  float mx[4], nm[4];
#pragma unroll
  for (int i = 0; i < 4; ++i) {
    p[i] *= L2E;
    mx[i] = fmaxf(fmaxf(p[i].x, p[i].y), fmaxf(p[i].z, p[i].w));
    nm[i] = -fminf(fminf(p[i].x, p[i].y), fminf(p[i].z, p[i].w));
  }
  xred4<true>(mx[0], mx[1], mx[2], mx[3]);
  xred4<true>(nm[0], nm[1], nm[2], nm[3]);
  f4 e[4], n[4];
  float se[4], sn[4];
#pragma unroll
  for (int i = 0; i < 4; ++i) {
    const f4 a = p[i] - mx[i], b = -p[i] - nm[i];
    e[i] = f4{__builtin_amdgcn_exp2f(a.x), __builtin_amdgcn_exp2f(a.y), __builtin_amdgcn_exp2f(a.z), __builtin_amdgcn_exp2f(a.w)};
    n[i] = f4{__builtin_amdgcn_exp2f(b.x), __builtin_amdgcn_exp2f(b.y), __builtin_amdgcn_exp2f(b.z), __builtin_amdgcn_exp2f(b.w)};
    se[i] = (e[i].x + e[i].y) + (e[i].z + e[i].w);
    sn[i] = (n[i].x + n[i].y) + (n[i].z + n[i].w);
  }
  xred4<false>(se[0], se[1], se[2], se[3]);
  xred4<false>(sn[0], sn[1], sn[2], sn[3]);
#pragma unroll
  for (int i = 0; i < 4; ++i) {
    const f4 a = e[i] * __builtin_amdgcn_rcpf(se[i]), b = n[i] * __builtin_amdgcn_rcpf(sn[i]);
    plus[i] = v2u{pk2(a.x, a.y), pk2(a.z, a.w)};
    minus[i] = v2u{pk2(b.x, b.y), pk2(b.z, b.w)};
  }
}
DI v2u trread(const Ctx &x, unsigned off) {
  s4 r = __builtin_amdgcn_ds_read_tr16_b64_v4i16((LAS s4 *)(x.sm + off));
  return __builtin_bit_cast(v2u, r);
}
DI f4 shsum(f4 s) {
#pragma unroll
  for (int o = 4; o < 64; o <<= 1) {
    s.x += __shfl_xor(s.x, o); s.y += __shfl_xor(s.y, o); s.z += __shfl_xor(s.z, o); s.w += __shfl_xor(s.w, o);
  }
  return s;
}

DI void poll_flags(Ctx &x, unsigned off, unsigned tag) {
  for (unsigned sp = 0;; ++sp) {
    unsigned f0, f1, f2, f3;
    const unsigned vo = (unsigned)x.lane * 64u;
    LDC(f0, vo, off); LDC(f1, vo, off + 4096u); LDC(f2, vo, off + 8192u); LDC(f3, vo, off + 12288u);
    asm volatile("s_waitcnt vmcnt(0)" : "+v"(f0), "+v"(f1), "+v"(f2), "+v"(f3)::"memory");
    if (__all(f0 == tag && f1 == tag && f2 == tag && f3 == tag)) break;
    if (sp > SPIN_CAP) {
      if (x.lane == 0) __hip_atomic_store(x.ctl + OFF_ABORT / 4, 3u, RLX_AGENT);
      break;
    }
    __builtin_amdgcn_s_sleep(8);
  }
}
DI unsigned sigv(int L, int ph) { return (unsigned)(L * 8 + ph + 1); }

DI void compute_path(Ctx &x, const Params &p, v4u (&xres)[2], const v2u (&wph)[2], unsigned tag) {
  const unsigned vz[4] = {0u, 0u, 0u, 0u};
  v4u x1r[2], x2r[2];
#pragma unroll 1
  for (int L = 0; L < 2; ++L) {
    const unsigned lb0 = OFF_L0 + (unsigned)L * L_STRIDE;
    v4u phq[2][2];
    GBAR();
    {
      refresh(x);
      FS(0);
      unsigned lb = lb0;
      asm volatile("" : "+s"(lb));
      v4u bf[16][2];
      Stats st[2];
      const unsigned so = lb + O_XB + (unsigned)x.TA * 16u * KB, sst = lb + O_XS + (unsigned)x.TA * 1024u;
      unsigned cn;
      pro2(x, bf, st, so, sst, cn);
      FS(1); FSP(0);
      BAR();
      if (L == 0 && threadIdx.x == 0) __hip_atomic_store(x.ctl + OFF_FLAGS2 / 4 + x.b * 16, tag, RLX_AGENT);
      chk2(x, bf, st, so, sst, cn);
      FS(2);
      f4 acc[6][2];
#pragma unroll
      for (int f = 0; f < 6; ++f) { acc[f][0] = f4{0.f, 0.f, 0.f, 0.f}; acc[f][1] = f4{0.f, 0.f, 0.f, 0.f}; }
      unsigned mx = 0;
      kloop2<6, POS1, 0, 16>(x, bf, acc, so, sigv(L, 0), mx);
      if (__builtin_expect(anybad(mx), 0)) redo2<6, POS1>(x, acc, so);
      FS(3);
      BAR();
      MARK("P1epi");
      f4 val[2][6];
#pragma unroll
      for (int j = 0; j < 2; ++j) {
        float mean, var;
        stats_fin(x, st[j], sst + (unsigned)j * 1024u, mean, var);
        const float rstd = __builtin_amdgcn_rsqf(var + 1e-5f), mr = mean * rstd;
#pragma unroll
        for (int f = 0; f < 6; ++f) val[j][f] = acc[f][j] * rstd - ldcs(x, f) * mr;
      }
      {
        v2u pl[4], mi[4];
        const f4 kk[4] = {val[0][2], val[0][3], val[1][2], val[1][3]};
        featmap4(kk, wph, pl, mi);
#pragma unroll
        for (int i = 0; i < 4; ++i) {
          const int j = i >> 1, h = i & 1;
          const unsigned img = IMG + (unsigned)((2 * x.m + j) * 16 + x.tok) * 32u + (unsigned)x.g * 8u + (unsigned)(h * 3) * 4096u;
          *(LAS v2u *)(x.sm + img) = pl[i];
          *(LAS v2u *)(x.sm + img + 4096u) = mi[i];
          *(LAS v2u *)(x.sm + img + 8192u) = v2u{pk2(val[j][4 + h].x, val[j][4 + h].y), pk2(val[j][4 + h].z, val[j][4 + h].w)};
        }
      }
      MARK("P1epiend");
      FS(4);
      BAR();
      FS(5);
      {
        v2u pl[4], mi[4];
        TIE(val[0][0]); TIE(val[0][1]); TIE(val[1][0]); TIE(val[1][1]);
        const f4 qq[4] = {val[0][0], val[0][1], val[1][0], val[1][1]};
        featmap4(qq, wph, pl, mi);
#pragma unroll
        for (int i = 0; i < 4; ++i) phq[i >> 1][i & 1] = v4u{pl[i].x, pl[i].y, mi[i].x, mi[i].y};
      }
      FS(6);
    }
    GBAR();
    GBAR();
    {
      refresh(x);
      unsigned lb = lb0;
      asm volatile("" : "+s"(lb));
      BAR();
      FS(7);
      v4u ka[2], za[2];
#pragma unroll
      for (int h = 0; h < 2; ++h) {
        ka[h] = *lds4(x, M_KVA + (unsigned)h * 1024u + x.lane16);
        const v2u z0 = *(const LAS v2u *)(x.sm + M_ZA + (unsigned)(h * 2) * 32u + (unsigned)x.g * 8u);
        const v2u z1 = *(const LAS v2u *)(x.sm + M_ZA + (unsigned)(h * 2 + 1) * 32u + (unsigned)x.g * 8u);
        za[h] = v4u{z0.x, z0.y, z1.x, z1.y};
      }
#pragma unroll
      for (int j = 0; j < 2; ++j) {
        f4 o[2];
#pragma unroll
        for (int h = 0; h < 2; ++h) {
          const f4 num = mfma32(ka[h], phq[j][h], f4{0.f, 0.f, 0.f, 0.f});
          const f4 den = mfma32(za[h], phq[j][h], f4{0.f, 0.f, 0.f, 0.f});
          o[h] = num * __builtin_amdgcn_rcpf(den.x + 1e-6f);
        }
        stg(x.rs, x.lane16, lb + O_OB + (unsigned)((x.TA + j) * 16 + x.pp) * KB, pack8(o[0], o[1]));
      }
      FS(8);
      {
        const unsigned ob = lb + O_OB + (unsigned)(x.TA * 16 + x.pp) * KB;
        warm(x, (unsigned)((x.lane >> 3) & 1) * 16u * KB + (unsigned)(x.lane & 7) * 128u, ob, ob);
      }
    }
    GBAR();
    {
      refresh(x);
      unsigned lb = lb0;
      asm volatile("" : "+s"(lb));
      SLEEP(DLY_2);
      FS(9);
      v4u bf[16];
      const unsigned so = lb + O_OB + (unsigned)x.T * 16u * KB;
      Stats st;
      unsigned cn;
      const unsigned cvo = (unsigned)(x.lane & 15) * KB + 1020u;
      pro1<16, false, false>(x, bf, so, vz, cvo, st, 0u, cn);
      FS(10); FSP(1);
      BAR();
      chk1<16, false, false>(x, bf, so, vz, cvo, st, 0u, cn);
      FS(11);
      f4 acc[4];
#pragma unroll
      for (int f = 0; f < 4; ++f) acc[f] = f4{0.f, 0.f, 0.f, 0.f};
      kloop1<POS2, 16, false, 0, 16>(x, bf, acc, so, vz, sigv(L, 2));
      FS(12);
      x1r[0] = pack8(ulo(xres[0]) + acc[0], uhi(xres[0]) + acc[1]);
      x1r[1] = pack8(ulo(xres[1]) + acc[2], uhi(xres[1]) + acc[3]);
      const unsigned fo = lb + O_X1B + (unsigned)(x.T * 16 + 2 * x.c) * KB;
      stg(x.rs, x.lane16, fo, x1r[0]); stg(x.rs, x.lane16, fo + KB, x1r[1]);
      put_stats(x, x1r[0], x1r[1], lb + O_X1S);
      sig_set(x, sigv(L, 2), M_SIG2);
      FS(13);
      warm(x, (unsigned)(x.lane & 15) * 128u, fo, lb + O_X1S + (unsigned)(x.T * 8 + x.c) * 128u);
    }
    float mean1, rstd1;
    GBAR();
    {
      refresh(x);
      unsigned lb = lb0;
      asm volatile("" : "+s"(lb));
      SLEEP(DLY_3);
      v4u bf[16];
      Stats st;
      const unsigned so = lb + O_X1B + (unsigned)x.T * 16u * KB;
      unsigned cn;
      const unsigned cvo = (unsigned)(x.lane & 15) * KB + 1020u;
      pro1<16, false, true>(x, bf, so, vz, cvo, st, lb + O_X1S + (unsigned)x.T * 1024u, cn);
      FSP(2);
      BAR();
      chk1<16, false, true>(x, bf, so, vz, cvo, st, lb + O_X1S + (unsigned)x.T * 1024u, cn);
      FS(14);
      f4 acc[4];
#pragma unroll
      for (int f = 0; f < 4; ++f) acc[f] = f4{0.f, 0.f, 0.f, 0.f};
      kloop1<POS3, 16, false, 0, 16>(x, bf, acc, so, vz, sigv(L, 3));
      FS(15);
      float var;
      stats_fin(x, st, lb + O_X1S + (unsigned)x.T * 1024u, mean1, var);
      rstd1 = __builtin_amdgcn_rsqf(var + 1e-5f);
      const float a = rstd1 * __builtin_amdgcn_rsqf(__builtin_fmaf(var * rstd1, rstd1, 1e-5f)), ma = mean1 * a;
      f4 y[4];
#pragma unroll
      for (int f = 0; f < 4; ++f) y[f] = gelu4(acc[f] * a - ldcs(x, 6 + f) * ma);
      const unsigned fo = lb + O_HB + (unsigned)(x.T * 16 + 2 * x.c) * KB;
      stg(x.rs, x.lane16, fo, pack8(y[0], y[1])); stg(x.rs, x.lane16, fo + KB, pack8(y[2], y[3]));
      sig_set(x, sigv(L, 3), M_SIG2);
      FS(16);
      warm(x, (unsigned)(x.lane & 15) * 128u, fo, fo);
    }
    GBAR();
    {
      refresh(x);
      unsigned lb = lb0;
      asm volatile("" : "+s"(lb));
      SLEEP(DLY_4);
      v4u bf[16];
      unsigned vo[4];
#pragma unroll
      for (int q = 0; q < 4; ++q) {
        const int d = q == 0 ? -1 : (q == 1 ? 1 : (q == 2 ? -2 : 2));
        const int src = 16 * x.T + x.tok + d;
        vo[q] = (src >= 0 && src < 2048) ? (unsigned)((src >> 4) * 16 * 1024 + (x.g * 16 + (src & 15)) * 16) : OOB;
      }
      const unsigned so = lb + O_HB;
      unsigned cvo;
      {
        const int s_ = x.lane & 15, nb = x.T + (((s_ >> 2) & 1) ? 1 : -1);
        const int tt = (x.lane & 16) ? nb : x.T;
        cvo = (tt >= 0 && tt < 128) ? (unsigned)(tt * 16 + s_) * KB + 1020u : OOB;
      }
      Stats st;
      unsigned cn;
      pro1<16, true, false>(x, bf, so, vo, cvo, st, 0u, cn);
      FSP(3);
      BAR();
      chk1<16, true, false>(x, bf, so, vo, cvo, st, 0u, cn);
      FS(17);
      f4 acc[4];
#pragma unroll
      for (int f = 0; f < 4; ++f) acc[f] = f4{0.f, 0.f, 0.f, 0.f};
      MARK("P4loop");
      kloop1<POS4, 16, true, 0, 16>(x, bf, acc, so, vo, sigv(L, 4));
      MARK("P4end");
      FS(18);
      const float mr = mean1 * rstd1;
      x2r[0] = pack8(ulo(x1r[0]) * rstd1 - mr + acc[0], uhi(x1r[0]) * rstd1 - mr + acc[1]);
      x2r[1] = pack8(ulo(x1r[1]) * rstd1 - mr + acc[2], uhi(x1r[1]) * rstd1 - mr + acc[3]);
      const unsigned fo = lb + O_X2B + (unsigned)(x.T * 16 + 2 * x.c) * KB;
      stg(x.rs, x.lane16, fo, x2r[0]); stg(x.rs, x.lane16, fo + KB, x2r[1]);
      put_stats(x, x2r[0], x2r[1], lb + O_X2S);
      sig_set(x, sigv(L, 4), M_SIG2);
      FS(19);
      warm(x, (unsigned)(x.lane & 15) * 128u, fo, lb + O_X2S + (unsigned)(x.T * 8 + x.c) * 128u);
    }
    GBAR();
    {
      refresh(x);
      unsigned lb = lb0;
      asm volatile("" : "+s"(lb));
      SLEEP(DLY_5);
      v4u bf[16][2];
      Stats st[2];
      const unsigned so = lb + O_X2B + (unsigned)x.TA * 16u * KB, sst = lb + O_X2S + (unsigned)x.TA * 1024u;
      unsigned cn;
      pro2(x, bf, st, so, sst, cn);
      FSP(4);
      BAR();
      chk2(x, bf, st, so, sst, cn);
      FS(20);
      f4 acc[7][2];
#pragma unroll
      for (int f = 0; f < 7; ++f) { acc[f][0] = f4{0.f, 0.f, 0.f, 0.f}; acc[f][1] = f4{0.f, 0.f, 0.f, 0.f}; }
      unsigned mx = 0;
      MARK("P5loop");
      kloop2<7, POS5, 0, 13>(x, bf, acc, so, sigv(L, 5), mx);
      BAR();
      kloop2<7, POS5, 13, 16>(x, bf, acc, so, sigv(L, 5), mx);
      if (__builtin_expect(anybad(mx), 0)) redo2<7, POS5>(x, acc, so);
      FS(21);
      BAR();
      FS(22);
      MARK("P5epi");
      const int u = x.pp >> 1, odd = x.pp & 1;
#pragma unroll
      for (int j = 0; j < 2; ++j) {
        float mean, var;
        stats_fin(x, st[j], sst + (unsigned)j * 1024u, mean, var);
        const float rstd = __builtin_amdgcn_rsqf(var + 1e-5f), mr = mean * rstd;
        const unsigned hb = lb + O_H3B + (unsigned)((x.TA + j) * 56 + 7 * u) * KB;
#pragma unroll
        for (int r = 0; r < 3; ++r) {
          const f4 y0 = gelu4(acc[2 * r][j] * rstd - ldcs(x, 10 + 2 * r) * mr), y1 = gelu4(acc[2 * r + 1][j] * rstd - ldcs(x, 11 + 2 * r) * mr);
          stg(x.rs, x.lane16, hb + (unsigned)(odd ? 4 + r : r) * KB, pack8(y0, y1));
        }
        const f4 y = gelu4(acc[6][j] * rstd - ldcs(x, 16) * mr);
        stg2(x.rs, x.lane16 + (unsigned)odd * 8u, hb + 3 * KB, v2u{clampw(pk2(y.x, y.y)), clampw(pk2(y.z, y.w))});
      }
      FS(23);
      {
        const unsigned hb0 = lb + O_H3B + (unsigned)(x.TA * 56 + 7 * u + (odd ? 3 : 0)) * KB;
        warm(x, (unsigned)(x.lane >> 5) * 56u * KB + (unsigned)(x.lane & 31) * 128u, hb0, hb0);
      }
    }
    GBAR();
    {
      refresh(x);
      unsigned lb = lb0;
      asm volatile("" : "+s"(lb));
      SLEEP(DLY_6);
      v4u hf[56];
      const unsigned so = lb + O_H3B + (unsigned)x.T * 56u * KB;
      Stats st;
      unsigned cn;
      const unsigned cvo = (unsigned)min(x.lane, 55) * KB + 1020u;
      pro1<56, false, false>(x, hf, so, vz, cvo, st, 0u, cn);
      FSP(5);
      BAR();
      chk1<56, false, false>(x, hf, so, vz, cvo, st, 0u, cn);
      FS(24);
      f4 acc[4];
#pragma unroll
      for (int f = 0; f < 4; ++f) acc[f] = f4{0.f, 0.f, 0.f, 0.f};
      kloop1<POS6, 56, false, 0, 17>(x, hf, acc, so, vz, 0u);
      BAR();
      FS(25);
      kloop1<POS6, 56, false, 17, 39>(x, hf, acc, so, vz, 0u);
      BAR();
      FS(26);
      kloop1<POS6, 56, false, 39, 56>(x, hf, acc, so, vz, 0u);
      FS(27);
      BAR();
      xres[0] = pack8(ulo(x1r[0]) + (ulo(x2r[0]) + acc[0]), uhi(x1r[0]) + (uhi(x2r[0]) + acc[1]));
      xres[1] = pack8(ulo(x1r[1]) + (ulo(x2r[1]) + acc[2]), uhi(x1r[1]) + (uhi(x2r[1]) + acc[3]));
      if (L == 0) {
        const unsigned fo = lb + L_STRIDE + O_XB + (unsigned)(x.T * 16 + 2 * x.c) * KB;
        stg(x.rs, x.lane16, fo, xres[0]); stg(x.rs, x.lane16, fo + KB, xres[1]);
        put_stats(x, xres[0], xres[1], lb + L_STRIDE + O_XS);
        warm(x, (unsigned)(x.lane & 15) * 128u, fo, lb + L_STRIDE + O_XS + (unsigned)(x.T * 8 + x.c) * 128u);
        SLEEP(DLY_1);
      } else {
        GAS unsigned char *op = (GAS unsigned char *)p.out + ((size_t)(16 * x.T + x.tok) * 512 + 64 * x.c + 4 * x.g) * 2;
        *(GAS v2u *)op = v2u{xres[0].x, xres[0].y};
        *(GAS v2u *)(op + 32) = v2u{xres[0].z, xres[0].w};
        *(GAS v2u *)(op + 64) = v2u{xres[1].x, xres[1].y};
        *(GAS v2u *)(op + 96) = v2u{xres[1].z, xres[1].w};
      }
      FS(28); FSP(6);
    }
  }
}

DI void poison_h3(Ctx &x, unsigned lb, int j) {
  const int u = x.pp >> 1, odd = x.pp & 1;
  const unsigned hb = lb + O_H3B + (unsigned)((x.TA + j) * 56 + 7 * u) * KB;
#pragma unroll
  for (int r = 0; r < 3; ++r) poi4(x.rs, x.lane16, hb + (unsigned)(odd ? 4 + r : r) * KB);
  poi2(x.rs, x.lane16 + (unsigned)odd * 8u, hb + 3 * KB);
}
DI void helper_path(Ctx &x, unsigned tag) {
#pragma unroll 1
  for (int L = 0; L < 2; ++L) {
    const unsigned lb0 = OFF_L0 + (unsigned)L * L_STRIDE;
    GBAR();
    {
      refresh(x);
      unsigned lb = lb0;
      asm volatile("" : "+s"(lb));
      FS(0);
      DRAIN_BAR();
      FS(1);
      if (L != 0) {
        sig_wait(x, sigv(L, 0));
        dmaR<96, 156>(x);
      }
      BAR();
      {
        const unsigned fo = (unsigned)(x.T * 16 + 2 * x.c) * KB, sto = (unsigned)(x.T * 8 + x.c) * 128u;
        poi4(x.rs, x.lane16, lb + O_X1B + fo); poi4(x.rs, x.lane16, lb + O_X1B + fo + KB);
        poi4(x.rs, x.lane16, lb + O_X2B + fo); poi4(x.rs, x.lane16, lb + O_X2B + fo + KB);
        if (x.g == 0) { poi2(x.rs, (unsigned)x.tok * 8u, lb + O_X1S + sto); poi2(x.rs, (unsigned)x.tok * 8u, lb + O_X2S + sto); }
      }
      FS(2);
      BAR();
      FS(3);
      {
        const int h = x.m >> 1, sg = x.m & 1;
        const unsigned lo = (unsigned)(4 * x.g + (x.tok >> 2)) * 32u + (unsigned)(x.tok & 3) * 8u;
        const unsigned ia = IMG + (unsigned)(h * 3 + sg) * 4096u + lo, iv = IMG + (unsigned)(h * 3 + 2) * 4096u + lo;
        f4 kv = {0.f, 0.f, 0.f, 0.f}, z = {0.f, 0.f, 0.f, 0.f};
#pragma unroll
        for (int tb = 0; tb < 4; ++tb) {
          const v2u a0 = trread(x, ia + tb * 1024u), a1 = trread(x, ia + tb * 1024u + 512u);
          const v2u b0 = trread(x, iv + tb * 1024u), b1 = trread(x, iv + tb * 1024u + 512u);
          const v4u a = {a0.x, a0.y, a1.x, a1.y};
          kv = mfma32(a, v4u{b0.x, b0.y, b1.x, b1.y}, kv);
          z = mfma32(a, v4u{0x3F803F80u, 0x3F803F80u, 0x3F803F80u, 0x3F803F80u}, z);
        }
        stg2(x.rs, (unsigned)x.lane * 8u, lb + O_KVP + (unsigned)((x.pp * 16 + x.tau) * 4 + x.m) * KB, v2u{clampw(pk2(kv.x, kv.y)), clampw(pk2(kv.z, kv.w))});
        if (x.tok == 0) stg(x.rs, (unsigned)x.g * 16u, lb + O_KVZ + (unsigned)((x.pp * 4 + x.m) * 16 + x.tau) * 64u, clamp4(__builtin_bit_cast(v4u, z)));
      }
      FS(4);
    }
    GBAR();
    {
      refresh(x);
      unsigned lb = lb0;
      asm volatile("" : "+s"(lb));
      if (L == 0) {
        if (x.m == 0) {
          poll_flags(x, OFF_FLAGS2, tag);
          *(volatile LAS unsigned *)(x.sm + M_RDY) = 1u;
        } else {
          for (unsigned sp = 0; sp < (1u << 20); ++sp) {
            if (__builtin_amdgcn_readfirstlane(*(volatile LAS unsigned *)(x.sm + M_RDY))) break;
            __builtin_amdgcn_s_sleep(2);
          }
        }
        v4u cv[3];
#pragma unroll
        for (int jj = 0; jj < 3; ++jj) {
          const int u = min(6 + x.m + 4 * jj, 16);
          cv[jj] = ldg(x.rs, x.lane16, OFF_CSP + (u < 10 ? 96u + (unsigned)(x.c * 4 + u - 6) : 128u + (unsigned)(x.pp * 7 + u - 10)) * KB);
        }
#pragma unroll
        for (int jj = 0; jj < 3; ++jj) {
          const f4 s = shsum(__builtin_bit_cast(f4, cv[jj]));
          const int u = 6 + x.m + 4 * jj;
          if (u < 17 && x.lane < 4) *(LAS f4 *)(x.sm + M_CS + (unsigned)u * 64u + (unsigned)x.lane * 16u) = s;
        }
      }
      SLEEP(DLY_KV);
      v4u pv[16];
      const unsigned so = lb + O_KVP + (unsigned)(x.pp * 64 + x.m) * KB, sz = lb + O_KVZ + (unsigned)(x.pp * 4 + x.m) * KB;
      v4u zv;
      {
        const unsigned cvo = (unsigned)(x.lane & 15) * 4096u + (KV16 ? 508u : 1020u);
        while (true) {
          unsigned cn;
          LDC(cn, cvo, so);
          LDA(zv, x.lane16, sz);
          WAITV2(0, cn, zv);
          if (!anybad(max(cn, max4(zv))) || giveup(x)) break;
        }
      }
      bool again;
      f4 s;
      v2u pw[16];
      do {
        asm volatile("" ::: "memory");
#pragma unroll
        for (int i = 0; i < 16; ++i) pw[i] = __builtin_amdgcn_raw_buffer_load_b64(x.rs, (unsigned)x.lane * 8u, so + (unsigned)i * 4096u, AUX_SC1);
        unsigned mx = 0;
#pragma unroll
        for (int i = 0; i < 16; ++i) mx = max(mx, max(pw[i].x, pw[i].y));
        again = anybad(mx) && !giveup(x);
      } while (__builtin_expect(again, 0));
      s = ulo(v4u{pw[0].x, pw[0].y, 0u, 0u});
#pragma unroll
      for (int i = 1; i < 16; ++i) s += ulo(v4u{pw[i].x, pw[i].y, 0u, 0u});
      *(LAS v2u *)(x.sm + M_KVA + (unsigned)(x.m >> 1) * 1024u + x.lane16 + (unsigned)(x.m & 1) * 8u) = v2u{pk2(s.x, s.y), pk2(s.z, s.w)};
      const f4 zs = shsum(__builtin_bit_cast(f4, zv));
      if (x.lane < 4) *(LAS v2u *)(x.sm + M_ZA + (unsigned)x.m * 32u + (unsigned)x.lane * 8u) = v2u{pk2(zs.x, zs.y), pk2(zs.z, zs.w)};
      FS(5); FSP(0);
      GBAR();
      BAR();
      FS(6);
      if (L == 0) dmaR<96, 156>(x);
      dmaR<156, 188>(x);
    }
    GBAR();
    {
      refresh(x);
      unsigned lb = lb0;
      asm volatile("" : "+s"(lb));
      DRAIN_BAR();
      FS(7);
      sig_wait(x, sigv(L, 2));
      dmaR<188, 220>(x);
      sig_wait(x, sigv(L, 2), M_SIG2);
      dmaR<220, 252>(x);
      poison_h3(x, lb, 0); poison_h3(x, lb, 1);
    }
    GBAR();
    {
      refresh(x);
      unsigned lb = lb0;
      asm volatile("" : "+s"(lb));
      WAITV_BAR(8);
      FS(8);
      sig_wait(x, sigv(L, 3));
      FS(15);
      dmaR<252, 284>(x);
      sig_wait(x, sigv(L, 3), M_SIG2);
      dmaR<284, 316>(x);
      FS(16);
      if (L == 0) {
        const unsigned fo = lb + L_STRIDE + O_XB + (unsigned)(x.T * 16 + 2 * x.c) * KB;
        poi4(x.rs, x.lane16, fo); poi4(x.rs, x.lane16, fo + KB);
        if (x.g == 0) poi2(x.rs, (unsigned)x.tok * 8u, lb + L_STRIDE + O_XS + (unsigned)(x.T * 8 + x.c) * 128u);
      }
    }
    GBAR();
    {
      refresh(x);
      unsigned lb = lb0;
      asm volatile("" : "+s"(lb));
      if (L == 0) WAITV_BAR(3); else WAITV_BAR(0);
      FS(9);
      sig_wait(x, sigv(L, 4));
      FS(18);
      dmaR<316, 348>(x);
      sig_wait(x, sigv(L, 4), M_SIG2);
      dmaR<348, 380>(x);
      FS(19);
      if (L == 0) {
        const unsigned fo = lb + L_STRIDE + O_HB + (unsigned)(x.T * 16 + 2 * x.c) * KB;
        poi4(x.rs, x.lane16, fo); poi4(x.rs, x.lane16, fo + KB);
        poi4(x.rs, x.lane16, lb + L_STRIDE + O_OB + (unsigned)(x.TA * 16 + x.pp) * KB);
        poi4(x.rs, x.lane16, lb + L_STRIDE + O_OB + (unsigned)((x.TA + 1) * 16 + x.pp) * KB);
      }
    }
    GBAR();
    {
      refresh(x);
      if (L == 0) WAITV_BAR(4); else WAITV_BAR(0);
      FS(10);
      dmaRT<380, 400>(x);
      DRAIN_BAR();
      sig_wait(x, sigv(L, 5));
      dmaR<400, 444>(x);
      BAR();
      FS(11);
      dmaR6<444, 556>(x);
    }
    GBAR();
    {
      refresh(x);
      v4u wr[17];
      {
        const unsigned ws0 = OFF_WT + (W6_F + (unsigned)(x.c * 224 + 156 + x.m)) * KB;
#pragma unroll
        for (int i = 0; i < 17; ++i) LDA(wr[i], x.lane16, ws0 + (unsigned)i * 4096u);
      }
      WAITV_BAR(17);
      FS(12);
      BAR();
      asm volatile("s_waitcnt vmcnt(0)" : "+v"(wr[0]), "+v"(wr[1]), "+v"(wr[2]), "+v"(wr[3]), "+v"(wr[4]), "+v"(wr[5]), "+v"(wr[6]), "+v"(wr[7]),
                   "+v"(wr[8]), "+v"(wr[9]), "+v"(wr[10]), "+v"(wr[11]), "+v"(wr[12]), "+v"(wr[13]), "+v"(wr[14]), "+v"(wr[15]), "+v"(wr[16])::"memory");
#pragma unroll
      for (int i = 0; i < 17; ++i) *(LAS v4u *)(x.sm + ((556 + 4 * i) % RING + x.m) * KB + x.lane16) = wr[i];
      DRAIN_BAR();
      FS(13);
      dmaR<624, 712>(x, L == 0);
      BAR();
      dmaR<712, 720>(x, L == 0);
      FS(14);
    }
  }
}

}

__global__ void __launch_bounds__(512, 2) mega_kernel(Params p) {
  extern __shared__ __attribute__((aligned(16))) unsigned char smem_[];
  Ctx x;
  x.sm = (LAS unsigned char *)smem_;
  x.lds0 = (unsigned)(size_t)smem_;
  x.lane = threadIdx.x & 63;
  x.w = __builtin_amdgcn_readfirstlane(threadIdx.x >> 6);
  x.b = blockIdx.x;
  refresh(x);
  const int b = x.b;
  x.spins = 0;
  x.ctl = (gu32 *)p.ws;
  x.rs = __builtin_amdgcn_make_buffer_rsrc((void *)p.ws, 0, WS_BYTES, 0x00020000);
  const bool cw = x.w < 4;

  FSA(0);
  const unsigned tag = __hip_atomic_load(x.ctl + OFF_EPOCH / 4, RLX_AGENT) + 1u;
  unsigned pf0 = 0, pf1 = 0;
  if (x.w == 7) {
    unsigned long long pc;
    asm volatile("s_getpc_b64 %0" : "=s"(pc));
    const GAS unsigned char *cp = (const GAS unsigned char *)(pc & ~127ull) + (size_t)(((b >> 3) + 32 * (x.lane & 15)) * 128);
    const GAS unsigned char *wp = (const GAS unsigned char *)p.ws + (size_t)min(x.lane, 28) * (2 * MB) + 1 * MB;
    if (x.lane >= 32) {
      const int q = x.lane & 7;
      const unsigned short *ip = q == 0 ? p.x : q == 1 ? p.wqkv : q == 2 ? p.wo : q == 3 ? p.w1 : q == 4 ? p.w2 : q == 5 ? p.w3 : q == 6 ? p.w4 : p.x;
      const size_t half = (q == 2 || q == 3 || q == 4) ? 256 * KB : 1 * MB;
      wp = (const GAS unsigned char *)ip + ((x.lane & 8) ? half : 0);
    }
    asm volatile("global_load_dword %0, %2, off\n\tglobal_load_dword %1, %3, off" : "=&v"(pf0), "=&v"(pf1) : "v"(cp), "v"(wp) : "memory");
    asm volatile("s_waitcnt vmcnt(0)" : "+v"(pf0), "+v"(pf1)::"memory");
  }
  v2u xin[4] = {};
  unsigned short wraw[2][4] = {};
  if (cw) {
    const GAS unsigned char *xp = (const GAS unsigned char *)p.x + ((size_t)(16 * x.T + x.tok) * 512 + 64 * x.c + 4 * x.g) * 2;
#pragma unroll
    for (int q = 0; q < 4; ++q) xin[q] = *(const GAS v2u *)(xp + 32 * q);
#pragma unroll
    for (int h = 0; h < 2; ++h) {
      const unsigned short *wp = p.wphi + ((2 * x.pp + h) * 16 + 4 * x.g) * 16 + x.tok;
#pragma unroll
      for (int jj = 0; jj < 4; ++jj) wraw[h][jj] = wp[16 * jj];
    }
  }
  if (threadIdx.x < 9) *(LAS unsigned *)(x.sm + M_SIG + threadIdx.x * 4u) = 0u;
  const int bid = ((x.w + 4) & 7) * 256 + b;
  const unsigned short *src = p.wqkv; int ld = 1536, ra = 0, rb = 0, n0 = 0, kstep = 0, csid = -1; unsigned fr[4] = {0u, 0u, 0u, 0u}, ci[4] = {0u, 0u, 0u, 0u};
  if (bid < 384) {
    kstep = bid & 15; const int nb = bid >> 4; n0 = nb * 64; ra = 32 * kstep; rb = ra + 16;
    const int sec = nb >> 3, h0 = 4 * (nb & 7);
#pragma unroll
    for (int ct = 0; ct < 4; ++ct) {
      const int hh = h0 + ct;
      fr[ct] = W1_F + (unsigned)(((hh >> 1) * 16 + kstep) * 6 + sec * 2 + (hh & 1));
      ci[ct] = (unsigned)((hh >> 1) * 6 + sec * 2 + (hh & 1));
    }
    csid = 0;
  } else if (bid < 768) {
    const int mi = (bid - 384) >> 7, r = (bid - 384) & 127; kstep = r & 15; const int nb = r >> 4;
    src = mi == 0 ? p.wo : (mi == 1 ? p.w1 : p.w2); ld = 512; n0 = nb * 64; ra = 32 * kstep; rb = ra + 16;
#pragma unroll
    for (int ct = 0; ct < 4; ++ct) { fr[ct] = W2_F + (unsigned)(mi * 512 + (nb * 16 + kstep) * 4 + ct); ci[ct] = 96u + (unsigned)(nb * 4 + ct); }
    if (mi == 1) csid = 1;
  } else if (bid < 1216) {
    const int r = bid - 768; kstep = r & 15; const int nb = r >> 4; src = p.w3; ld = 1792; n0 = nb * 64; ra = 32 * kstep; rb = ra + 16;
#pragma unroll
    for (int ct = 0; ct < 4; ++ct) { const int F = nb * 4 + ct; fr[ct] = W5_F + (unsigned)(((F / 7) * 16 + kstep) * 7 + F % 7); ci[ct] = 128u + (unsigned)F; }
    csid = 2;
  } else if (bid < 1664) {
    const int r = bid - 1216; const int nb = r & 7; kstep = r >> 3; src = p.w4; ld = 512; n0 = nb * 64;
    const int u = kstep / 7, q = kstep % 7;
    if (q < 3) { ra = 224 * u + 32 * q; rb = ra + 16; }
    else if (q == 3) { ra = 224 * u + 96; rb = 224 * u + 208; }
    else { ra = 224 * u + 112 + 32 * (q - 4); rb = ra + 16; }
#pragma unroll
    for (int ct = 0; ct < 4; ++ct) fr[ct] = W6_F + (unsigned)((nb * 56 + kstep) * 4 + ct);
  }
  if (bid < 1664) {
    const int ip = x.lane >> 5, uu = (x.lane >> 4) & 1, q = (x.lane >> 2) & 3, c1 = ((x.lane >> 1) & 1) ^ ip, bb = x.lane & 1;
    const int rl = 4 * ip + q, ch = 2 * (2 * uu + c1) + bb;
    const unsigned stg0 = x.lds0 + (unsigned)x.w * 4096u;
#pragma unroll
    for (int u = 0; u < 4; ++u) {
      const int row = (u < 2 ? ra + 8 * u : rb + 8 * (u - 2)) + rl;
      const GAS unsigned char *gp = (const GAS unsigned char *)src + ((size_t)row * ld + n0 + ch * 8) * 2;
      asm volatile("s_mov_b32 m0, %0\n\ts_nop 0\n\tglobal_load_lds_dwordx4 %1, off" ::"s"(stg0 + (unsigned)u * 1024u), "v"(gp) : "memory");
    }
  }
  auto retile = [&]() {
    asm volatile("s_waitcnt vmcnt(0)" ::: "memory");
    const int gq = (x.lane & 15) >> 2, gp_ = x.lane & 3, gi = x.g & 1;
#pragma unroll
    for (int ct = 0; ct < 4; ++ct) {
      const unsigned L = (unsigned)((gi << 5) | ((ct >> 1) << 4) | (gq << 2) | (((ct & 1) ^ gi) << 1) | (gp_ >> 1));
      const unsigned a0 = (unsigned)x.w * 4096u + (unsigned)(x.g >> 1) * 1024u + L * 16u + (unsigned)(gp_ & 1) * 8u;
      const v2u lo = trread(x, a0), hi = trread(x, a0 + 2048u);
      const v4u fragv = {lo.x, lo.y, hi.x, hi.y};
      stg(x.rs, x.lane16, __builtin_amdgcn_readfirstlane(OFF_WT + fr[ct] * KB), fragv);
      if (csid >= 0) {
        const f4 cs = mfma32(fragv, v4u{0x3F803F80u, 0x3F803F80u, 0x3F803F80u, 0x3F803F80u}, f4{0.f, 0.f, 0.f, 0.f});
        const unsigned co = __builtin_amdgcn_readfirstlane(OFF_CSP + (ci[ct] * 16u + (unsigned)kstep) * 64u);
        if (x.tok == 0) stg(x.rs, (unsigned)x.g * 16u, co, __builtin_bit_cast(v4u, cs));
      }
    }
  };
  if (bid < 384) retile();
  FSA(1);
  v4u xres[2];
  v2u wph[2];
  if (cw) {
    xres[0] = clamp4(v4u{xin[0].x, xin[0].y, xin[1].x, xin[1].y});
    xres[1] = clamp4(v4u{xin[2].x, xin[2].y, xin[3].x, xin[3].y});
    const unsigned fo = OFF_L0 + O_XB + (unsigned)(x.T * 16 + 2 * x.c) * KB;
    stg(x.rs, x.lane16, fo, xres[0]); stg(x.rs, x.lane16, fo + KB, xres[1]);
    put_stats(x, xres[0], xres[1], OFF_L0 + O_XS);
#pragma unroll
    for (int h = 0; h < 2; ++h)
      wph[h] = v2u{(unsigned)wraw[h][0] | ((unsigned)wraw[h][1] << 16), (unsigned)wraw[h][2] | ((unsigned)wraw[h][3] << 16)};
  } else {
    xres[0] = xres[1] = v4u{0u, 0u, 0u, 0u};
    wph[0] = wph[1] = v2u{0u, 0u};
  }
  FSA(2);
  asm volatile("s_waitcnt vmcnt(0) lgkmcnt(0)\n\ts_barrier" ::: "memory");
  FSA(3);
  if (threadIdx.x == 0) __hip_atomic_store(x.ctl + OFF_FLAGS / 4 + b * 16, tag, RLX_AGENT);
  if (bid >= 384 && bid < 1664) retile();
  if (!cw) {
    poi4(x.rs, x.lane16, OFF_L0 + O_KVP + (unsigned)((x.pp * 16 + x.tau) * 4 + x.m) * KB);
    if (x.tok == 0) poi4(x.rs, (unsigned)x.g * 16u, OFF_L0 + O_KVZ + (unsigned)((x.pp * 4 + x.m) * 16 + x.tau) * 64u);
    poi4(x.rs, x.lane16, OFF_L0 + O_OB + (unsigned)(x.TA * 16 + x.pp) * KB);
    poi4(x.rs, x.lane16, OFF_L0 + O_OB + (unsigned)((x.TA + 1) * 16 + x.pp) * KB);
    poi4(x.rs, x.lane16, OFF_L0 + O_HB + (unsigned)(x.T * 16 + 2 * x.c) * KB);
    poi4(x.rs, x.lane16, OFF_L0 + O_HB + (unsigned)(x.T * 16 + 2 * x.c + 1) * KB);
    poi4(x.rs, x.lane16, OFF_L0 + L_STRIDE + O_KVP + (unsigned)((x.pp * 16 + x.tau) * 4 + x.m) * KB);
    if (x.tok == 0) poi4(x.rs, (unsigned)x.g * 16u, OFF_L0 + L_STRIDE + O_KVZ + (unsigned)((x.pp * 4 + x.m) * 16 + x.tau) * 64u);
  }
  if (x.w == 4) {
    poll_flags(x, OFF_FLAGS, tag);
    if (b == 0 && x.lane == 0) __hip_atomic_store(x.ctl + OFF_EPOCH / 4, tag, RLX_AGENT);
  }
  BAR();
  FSA(4);
  if (cw) {
    compute_path(x, p, xres, wph, tag);
  } else {
    v4u cv[2];
#pragma unroll
    for (int jj = 0; jj < 2; ++jj) LDA(cv[jj], x.lane16, OFF_CSP + (unsigned)(x.pp * 6 + min(x.m + 4 * jj, 5)) * KB);
    dmaR<POS1, POS2>(x);
#pragma unroll
    for (int jj = 0; jj < 2; ++jj) {
      WAITV1(24, cv[jj]);
      const f4 s = shsum(__builtin_bit_cast(f4, cv[jj]));
      const int u = x.m + 4 * jj;
      if (u < 6 && x.lane < 4) *(LAS f4 *)(x.sm + M_CS + (unsigned)u * 64u + (unsigned)x.lane * 16u) = s;
    }
    helper_path(x, tag);
  }
  asm volatile("s_waitcnt vmcnt(0)" ::: "memory");
}

extern "C" void kernel_launch(void *const *d_in, const int *in_sizes, int n_in, void *d_out, int out_size, void *d_ws, size_t ws_size,
                              hipStream_t stream) {
  Params p;
  p.x = (const unsigned short *)d_in[0];
  p.wqkv = (const unsigned short *)d_in[1];
  p.wphi = (const unsigned short *)d_in[2];
  p.wo = (const unsigned short *)d_in[3];
  p.w1 = (const unsigned short *)d_in[5];
  p.w2 = (const unsigned short *)d_in[6];
  p.w3 = (const unsigned short *)d_in[7];
  p.w4 = (const unsigned short *)d_in[8];
  p.out = (unsigned short *)d_out;
  p.ws = (unsigned char *)d_ws;
  (void)hipFuncSetAttribute((const void *)mega_kernel, hipFuncAttributeMaxDynamicSharedMemorySize, LDS_BYTES);
  hipLaunchKernelGGL(mega_kernel, dim3(256), dim3(512), LDS_BYTES, stream, p);
}
```

```cpp
#include <hip/hip_runtime.h>
#include <stdint.h>

#define GAS __attribute__((address_space(1)))
#define LAS __attribute__((address_space(3)))
#define DI __device__ __forceinline__
typedef unsigned v4u __attribute__((ext_vector_type(4)));
typedef unsigned v2u __attribute__((ext_vector_type(2)));
typedef float f4 __attribute__((ext_vector_type(4)));
typedef float f2 __attribute__((ext_vector_type(2)));
typedef short s8 __attribute__((ext_vector_type(8)));
typedef short s4 __attribute__((ext_vector_type(4)));
typedef __bf16 bf2 __attribute__((ext_vector_type(2)));
typedef GAS unsigned gu32;
typedef __amdgpu_buffer_rsrc_t rsrc_t;
#define RLX_AGENT __ATOMIC_RELAXED, __HIP_MEMORY_SCOPE_AGENT

#define DBG_GBAR 0
#define DBG_NOPOISON 0
#define DBG_STAMP 0
#define SPEC 0
#define PREFETCH 1
#define KV16 1
#define WARM 0

namespace {
constexpr unsigned MB = 1u << 20, KB = 1024u;
constexpr unsigned OFF_EPOCH = 0, OFF_ABORT = 256, OFF_GBAR = 512, OFF_FLAGS = 16384, OFF_FLAGS2 = 32768;
constexpr unsigned OFF_WT = 1 * MB;
constexpr unsigned W1_F = 0, W2_F = 1536, W5_F = 3072, W6_F = 4864;
constexpr unsigned OFF_CSP = 8 * MB;
constexpr unsigned OFF_L0 = 9 * MB, L_STRIDE = 24 * MB;
constexpr unsigned O_XB = 0, O_XS = 2 * MB, O_KVP = 2 * MB + 512 * KB, O_KVZ = 3 * MB + 512 * KB, O_OB = 4 * MB, O_X1B = 6 * MB,
                   O_X1S = 8 * MB, O_HB = 9 * MB, O_X2B = 11 * MB, O_X2S = 13 * MB, O_H3B = 14 * MB;
constexpr unsigned WS_BYTES = OFF_L0 + 2 * L_STRIDE;
constexpr unsigned OOB = 0x7F000000u;
constexpr int RING = 156;
constexpr unsigned MISC = RING * KB;
constexpr unsigned M_CS = MISC, M_KVA = MISC + 1088, M_ZA = MISC + 3136, M_SIG = MISC + 3264, M_SIG2 = MISC + 3280, M_RDY = MISC + 3296, M_FLAG = MISC + 3300;
constexpr unsigned IMG = 0;
constexpr unsigned LDS_BYTES = 163840;
constexpr int POS1 = 0, POS2 = 96, POS3 = 160, POS4 = 224, POS5 = 288, POS6 = 400, POSE = 624;
constexpr unsigned SENT = 0xFFFFFFFFu;
constexpr unsigned SPIN_CAP = 1u << 13;
constexpr int AUX_SC1 = 16;
#define DLY_KV 14
#define DLY_1 19
#define DLY_2 19
#define DLY_3 19
#define DLY_4 19
#define DLY_5 19
#define DLY_6 19

struct Params {
  const unsigned short *x, *wqkv, *wphi, *wo, *w1, *w2, *w3, *w4;
  unsigned short *out;
  unsigned char *ws;
};

DI unsigned pk2(float a, float b) { f2 v = {a, b}; bf2 r = __builtin_convertvector(v, bf2); return __builtin_bit_cast(unsigned, r); }
DI float asf(unsigned u) { return __builtin_bit_cast(float, u); }
DI float blo(unsigned u) { return __builtin_bit_cast(float, u << 16); }
DI float bhi(unsigned u) { return __builtin_bit_cast(float, u & 0xFFFF0000u); }
DI unsigned clampw(unsigned u) { return u < 0xFFFFFFFEu ? u : 0xFFFFFFFEu; }
DI unsigned clampf(float f) { return clampw(__builtin_bit_cast(unsigned, f)); }
DI v4u clamp4(v4u v) { return v4u{clampw(v.x), clampw(v.y), clampw(v.z), clampw(v.w)}; }
DI v4u pack8(f4 a, f4 b) { return clamp4(v4u{pk2(a.x, a.y), pk2(a.z, a.w), pk2(b.x, b.y), pk2(b.z, b.w)}); }
DI f4 ulo(v4u v) { return f4{blo(v.x), bhi(v.x), blo(v.y), bhi(v.y)}; }
DI f4 uhi(v4u v) { return f4{blo(v.z), bhi(v.z), blo(v.w), bhi(v.w)}; }
DI f4 mfma32(v4u a, v4u b, f4 c) {
  return __builtin_amdgcn_mfma_f32_16x16x32_bf16(__builtin_bit_cast(s8, a), __builtin_bit_cast(s8, b), c, 0, 0, 0);
}
DI f4 mfma16(v2u a, v2u b, f4 c) {
  return __builtin_amdgcn_mfma_f32_16x16x16bf16_1k(__builtin_bit_cast(s4, a), __builtin_bit_cast(s4, b), c, 0, 0, 0);
}
DI v4u ldg(rsrc_t rs, unsigned voff, unsigned soff) { return __builtin_amdgcn_raw_buffer_load_b128(rs, voff, soff, AUX_SC1); }
DI void stg(rsrc_t rs, unsigned voff, unsigned soff, v4u v) {
  __builtin_amdgcn_raw_buffer_store_b128(v, rs, voff, soff, AUX_SC1);
  asm volatile("s_nop 1" ::"v"(v));
}
DI void stg2(rsrc_t rs, unsigned voff, unsigned soff, v2u v) { __builtin_amdgcn_raw_buffer_store_b64(v, rs, voff, soff, AUX_SC1); }
DI void poi4(rsrc_t rs, unsigned voff, unsigned soff) { if (!DBG_NOPOISON) stg(rs, voff, soff, v4u{0xFFFFFFFFu, 0xFFFFFFFFu, 0xFFFFFFFFu, 0xFFFFFFFFu}); }
DI void poi2(rsrc_t rs, unsigned voff, unsigned soff) { if (!DBG_NOPOISON) stg2(rs, voff, soff, v2u{0xFFFFFFFFu, 0xFFFFFFFFu}); }
DI unsigned max4(v4u v) { return max(max(v.x, v.y), max(v.z, v.w)); }
DI bool anybad(unsigned mx) { return __any(mx == SENT) != 0; }
DI float xsum(float x) {
  unsigned u = __builtin_bit_cast(unsigned, x);
  auto r = __builtin_amdgcn_permlane16_swap(u, u, false, false);
  x = __builtin_bit_cast(float, (unsigned)r[0]) + __builtin_bit_cast(float, (unsigned)r[1]);
  u = __builtin_bit_cast(unsigned, x);
  auto q = __builtin_amdgcn_permlane32_swap(u, u, false, false);
  return __builtin_bit_cast(float, (unsigned)q[0]) + __builtin_bit_cast(float, (unsigned)q[1]);
}
DI float xmax(float x) {
  unsigned u = __builtin_bit_cast(unsigned, x);
  auto r = __builtin_amdgcn_permlane16_swap(u, u, false, false);
  x = fmaxf(__builtin_bit_cast(float, (unsigned)r[0]), __builtin_bit_cast(float, (unsigned)r[1]));
  u = __builtin_bit_cast(unsigned, x);
  auto q = __builtin_amdgcn_permlane32_swap(u, u, false, false);
  return fmaxf(__builtin_bit_cast(float, (unsigned)q[0]), __builtin_bit_cast(float, (unsigned)q[1]));
}
template <bool MAX> DI float red_op(float a, float b) { return MAX ? fmaxf(a, b) : a + b; }
DI unsigned fbits(float f) { return __builtin_bit_cast(unsigned, f); }
template <bool MAX> DI void xred4(float &a, float &b, float &c, float &d) {
  const auto r1 = __builtin_amdgcn_permlane16_swap(fbits(a), fbits(b), false, false);
  const auto r2 = __builtin_amdgcn_permlane16_swap(fbits(c), fbits(d), false, false);
  const float c1 = red_op<MAX>(asf(r1[0]), asf(r1[1])), c2 = red_op<MAX>(asf(r2[0]), asf(r2[1]));
  const auto r3 = __builtin_amdgcn_permlane32_swap(fbits(c1), fbits(c2), false, false);
  const unsigned e = fbits(red_op<MAX>(asf(r3[0]), asf(r3[1])));
  const auto r4 = __builtin_amdgcn_permlane32_swap(e, e, false, false);
  const unsigned f0 = r4[0], f1 = r4[1];
  const auto r5 = __builtin_amdgcn_permlane16_swap(f0, f0, false, false);
  const auto r6 = __builtin_amdgcn_permlane16_swap(f1, f1, false, false);
  a = asf(r5[0]); b = asf(r5[1]); c = asf(r6[0]); d = asf(r6[1]);
}
template <bool MAX> DI void xred2(float &a, float &b) {
  const auto r1 = __builtin_amdgcn_permlane16_swap(fbits(a), fbits(b), false, false);
  const unsigned c = fbits(red_op<MAX>(asf(r1[0]), asf(r1[1])));
  const auto r3 = __builtin_amdgcn_permlane32_swap(c, c, false, false);
  const unsigned e = fbits(red_op<MAX>(asf(r3[0]), asf(r3[1])));
  const auto r5 = __builtin_amdgcn_permlane16_swap(e, e, false, false);
  a = asf(r5[0]); b = asf(r5[1]);
}
DI f2 gelu2(f2 x) {
  const f2 c1 = {-2.302208198f, -2.302208198f}, c2 = {-0.1029432397f, -0.1029432397f}, one = {1.0f, 1.0f};
  const f2 t = x * __builtin_elementwise_fma(x * x, c2, c1);
  const f2 d = one + f2{__builtin_amdgcn_exp2f(t.x), __builtin_amdgcn_exp2f(t.y)};
  return x * f2{__builtin_amdgcn_rcpf(d.x), __builtin_amdgcn_rcpf(d.y)};
}
DI f4 gelu4(f4 y) {
  const f2 a = gelu2(f2{y.x, y.y}), b = gelu2(f2{y.z, y.w});
  return f4{a.x, a.y, b.x, b.y};
}

#define FSL 1
#define FS(i) do {} while (0)
#define FSA(i) do { unsigned long long t_; asm volatile("s_memtime %0\n\ts_waitcnt lgkmcnt(0)" : "=s"(t_) :: "memory"); \
  int ww_ = x.w; asm volatile("" : "+s"(ww_)); \
  unsigned a_ = (ww_ == 0) ? (70 + (i)) * 4u : ((ww_ == 4) ? 400u + (70 + (i)) * 4u : 780u); \
  asm volatile("" : "+s"(a_)); \
  *(LAS unsigned *)(x.sm + M_FLAG + a_) = (unsigned)t_; } while (0)
#define FSP(i) do {} while (0)
#define SB() __builtin_amdgcn_sched_barrier(0)
#define MARK(s) asm volatile("; MARK " s)
#define BAR() asm volatile("s_waitcnt lgkmcnt(0)\n\ts_barrier" ::: "memory")
#define WAITV_BAR(n) asm volatile("s_waitcnt vmcnt(" #n ") lgkmcnt(0)\n\ts_barrier" ::: "memory")
#define DRAIN_BAR() asm volatile("s_waitcnt vmcnt(0) lgkmcnt(0)\n\ts_barrier" ::: "memory")
DI void sleepn(int n) {
  for (int i = 0; i < n; ++i) __builtin_amdgcn_s_sleep(1);
}

struct Ctx {
  rsrc_t rs;
  LAS unsigned char *sm;
  unsigned lds0;
  unsigned lane16;
  LAS unsigned char *ab[3];
  int lane, w, m, g, tok;
  int t, c, tau, pp, b;
  int T, TA;
  unsigned spins;
  gu32 *ctl;
};
DI void refresh(Ctx &x) {
  asm volatile("" : "+s"(x.w), "+s"(x.b), "+v"(x.lane));
  x.lane16 = (unsigned)x.lane * 16u;
  x.g = x.lane >> 4; x.tok = x.lane & 15;
  x.m = x.w & 3;
  const int xc = x.b & 7, j = x.b >> 3;
  x.t = 4 * xc + (j >> 3); x.c = j & 7;
  x.tau = 2 * xc + (j >> 4); x.pp = j & 15;
  x.T = 4 * x.t + x.m;
  x.TA = 8 * x.tau + 2 * x.m;
  x.ab[0] = x.sm + x.lane16;
  unsigned o1 = x.lane16 + 60u * KB, o2 = x.lane16 + 120u * KB;
  asm volatile("" : "+v"(o1), "+v"(o2));
  x.ab[1] = x.sm + o1; x.ab[2] = x.sm + o2;
}

template <int LO, int HI, int THR> DI void dmaSeg(const Ctx &x, bool live) {
  constexpr int Q = LO % POSE;
  int w = x.w, b = x.b;
  asm volatile("" : "+s"(w), "+s"(b));
  const int m = w & 3, j = b >> 3;
  unsigned fr;
  if constexpr (Q < POS2) fr = W1_F + (unsigned)((j & 15) * 96 + Q + m);
  else if constexpr (Q < POS5) fr = W2_F + (unsigned)(((Q - POS2) / 64) * 512 + (j & 7) * 64 + (Q - POS2) % 64 + m);
  else if constexpr (Q < POS6) fr = W5_F + (unsigned)((j & 15) * 112 + (Q - POS5) + m);
  else fr = W6_F + (unsigned)((j & 7) * 224 + (Q - POS6) + m);
  unsigned so = live ? OFF_WT + fr * KB : OOB;
  unsigned lds = x.lds0 + (unsigned)(LO % RING + m) * KB;
  const unsigned wrap = x.lds0 + (unsigned)(RING + m) * KB;
#pragma unroll 1
  for (int i = 0; i < (HI - LO) / 4; ++i) {
    asm volatile("s_mov_b32 m0, %0\n\ts_nop 0\n\tbuffer_load_dwordx4 %1, %2, %3 offen sc1 lds" ::"s"(lds), "v"(x.lane16), "s"(x.rs), "s"(so) : "memory");
    if constexpr (THR == 3) asm volatile("s_waitcnt vmcnt(3)" ::: "memory");
    if constexpr (THR == 5) asm volatile("s_waitcnt vmcnt(5)" ::: "memory");
    so += 4u * KB;
    lds += 4u * KB;
    lds = lds >= wrap ? lds - RING * KB : lds;
  }
}
constexpr int mat_end(int p) {
  const int base = p - p % POSE, q = p % POSE;
  return base + (q < POS2 ? POS2 : q < POS3 ? POS3 : q < POS4 ? POS4 : q < POS5 ? POS5 : q < POS6 ? POS6 : POSE);
}
template <int LO, int HI, int THR = -1> DI void dmaR(const Ctx &x, bool live = true) {
  if constexpr (LO < HI) {
    constexpr int E = mat_end(LO) < HI ? mat_end(LO) : HI;
    dmaSeg<LO, E, THR>(x, live);
    dmaR<E, HI, THR>(x, live);
  }
}
template <int LO, int HI> DI void dmaRT(const Ctx &x, bool live = true) { dmaR<LO, HI, 3>(x, live); }
template <int LO, int HI> DI void dmaR6(const Ctx &x, bool live = true) { dmaR<LO, HI, 5>(x, live); }
DI void sig_set(const Ctx &x, unsigned v, unsigned word = M_SIG) { *(volatile LAS unsigned *)(x.sm + word + (unsigned)x.m * 4u) = v; }
DI void sig_wait(const Ctx &x, unsigned v, unsigned word = M_SIG) {
  for (unsigned sp = 0; sp < (1u << 20); ++sp) {
    const v4u s = *(volatile LAS v4u *)(x.sm + word);
    if (__builtin_amdgcn_readfirstlane(min(min(s.x, s.y), min(s.z, s.w))) >= v) break;
    __builtin_amdgcn_s_sleep(1);
  }
}

#define LDA(dst, voff, soff) \
  asm volatile("buffer_load_dwordx4 %0, %1, %2, %3 offen sc1" : "=v"(dst) : "v"(voff), "s"(x.rs), "s"(soff) : "memory")
#define LDA2(dst, voff, soff, imm) \
  asm volatile("buffer_load_dwordx2 %0, %1, %2, %3 offen offset:%4 sc1" : "=v"(dst) : "v"(voff), "s"(x.rs), "s"(soff), "i"(imm) : "memory")
#define WAITV1(n, r) asm volatile("s_waitcnt vmcnt(%1)" : "+v"(r) : "i"(n) : "memory")
#define WAITV2(n, r, q) asm volatile("s_waitcnt vmcnt(%2)" : "+v"(r), "+v"(q) : "i"(n) : "memory")
#define TIE(r) asm volatile("" : "+v"(r) : : "memory")
DI bool giveup(Ctx &x) {
  if (++x.spins > SPIN_CAP) {
    if (x.lane == 0) __hip_atomic_store(x.ctl + OFF_ABORT / 4, 1u, RLX_AGENT);
    return true;
  }
  __builtin_amdgcn_s_sleep(1);
  return false;
}
DI v4u load_sure(Ctx &x, unsigned voff, unsigned soff) {
  v4u v;
  while (true) {
    asm volatile("buffer_load_dwordx4 %0, %1, %2, %3 offen sc1\n\ts_waitcnt vmcnt(0)" : "=&v"(v) : "v"(voff), "s"(x.rs), "s"(soff) : "memory");
    if (!anybad(max4(v)) || giveup(x)) break;
  }
  return v;
}
DI v2u load_sure2(Ctx &x, unsigned voff, unsigned soff) {
  v2u v;
  while (true) {
    asm volatile("buffer_load_dwordx2 %0, %1, %2, %3 offen sc1\n\ts_waitcnt vmcnt(0)" : "=&v"(v) : "v"(voff), "s"(x.rs), "s"(soff) : "memory");
    if (!anybad(max(v.x, v.y)) || giveup(x)) break;
  }
  return v;
}

DI void warm(Ctx &x, unsigned voff, unsigned soff, unsigned soff2) {
}
#define LDC(dst, voff, soff) \
  asm volatile("buffer_load_dword %0, %1, %2, %3 offen sc1" : "=v"(dst) : "v"(voff), "s"(x.rs), "s"(soff) : "memory")

#define GBAR() do {} while (0)
#define SLEEP(n) sleepn(n)

DI void put_stats(const Ctx &x, v4u u, v4u v, unsigned sbuf) {
  const f4 a = ulo(u), b = uhi(u), c = ulo(v), d = uhi(v);
  const f4 s = (a + b) + (c + d), q = (a * a + b * b) + (c * c + d * d);
  float s1 = (s.x + s.y) + (s.z + s.w), s2 = (q.x + q.y) + (q.z + q.w);
  xred2<false>(s1, s2);
  if (x.g == 0) stg2(x.rs, (unsigned)x.tok * 8u, sbuf + (unsigned)(x.T * 8 + x.c) * 128u, v2u{clampf(s1), clampf(s2)});
}
struct Stats { v2u v[2]; };
DI void stats_load(const Ctx &x, Stats &s, unsigned soffT) {
  const unsigned vo = (unsigned)(x.g * 256 + x.tok * 8);
  LDA2(s.v[0], vo, soffT, 0); LDA2(s.v[1], vo, soffT, 128);
}
DI void stats_fin(Ctx &x, Stats &s, unsigned soffT, float &mean, float &var) {
  TIE(s.v[0]); TIE(s.v[1]);
  if (__builtin_expect(anybad(max(max(s.v[0].x, s.v[0].y), max(s.v[1].x, s.v[1].y))), 0)) {
    s.v[0] = load_sure2(x, (unsigned)(x.g * 256 + x.tok * 8), soffT);
    s.v[1] = load_sure2(x, (unsigned)(x.g * 256 + x.tok * 8 + 128), soffT);
  }
  float s1 = asf(s.v[0].x) + asf(s.v[1].x), s2 = asf(s.v[0].y) + asf(s.v[1].y);
  xred2<false>(s1, s2);
  mean = s1 * (1.0f / 512.0f);
  var = fmaxf(__builtin_fmaf(-mean, mean, s2 * (1.0f / 512.0f)), 0.0f);
}
DI const LAS v4u *lds4(const Ctx &x, unsigned off) { return (const LAS v4u *)(x.sm + off); }
template <int NT, int P> DI void ldaN(const Ctx &x, v4u (&a)[NT]) {
#pragma unroll
  for (int f = 0; f < NT; ++f) {
    const int slot = (P + f) % RING;
    a[f] = *(const LAS v4u *)(x.ab[slot / 60] + (slot % 60) * KB);
  }
}
template <int NT> DI void tieN(v4u (&a)[NT]) {
  if constexpr (NT == 4) asm volatile("" : "+v"(a[0]), "+v"(a[1]), "+v"(a[2]), "+v"(a[3]));
  else if constexpr (NT == 6) asm volatile("" : "+v"(a[0]), "+v"(a[1]), "+v"(a[2]), "+v"(a[3]), "+v"(a[4]), "+v"(a[5]));
  else asm volatile("" : "+v"(a[0]), "+v"(a[1]), "+v"(a[2]), "+v"(a[3]), "+v"(a[4]), "+v"(a[5]), "+v"(a[6]));
}
DI unsigned mx4(unsigned m, v4u v) {
  unsigned r;
  asm("v_max3_u32 %0, %1, %2, %3" : "=v"(r) : "v"(m), "v"(v.x), "v"(v.y));
  asm("v_max3_u32 %0, %1, %2, %3" : "=v"(m) : "v"(r), "v"(v.z), "v"(v.w));
  return m;
}
DI f4 ldcs(const Ctx &x, int tile) { return *(const LAS f4 *)(x.sm + M_CS + (unsigned)tile * 64u + (unsigned)x.g * 16u); }
constexpr int imin(int a, int b) { return a < b ? a : b; }

template <bool SH> DI unsigned vof(const Ctx &x, int k, const unsigned (&vo)[4]) { return SH ? vo[k >> 2] : x.lane16; }
template <int NK> constexpr int depth1() { return 8; }
template <int POS, int NK, bool SH, int K0, int K1, int I> struct KL1 {
  static DI void run(Ctx &x, v4u (&bf)[NK], f4 (&acc)[4], unsigned &mx, v4u (&cur)[4], v4u (&nxt)[4], unsigned so, const unsigned (&vo)[4],
                     unsigned sig) {
    v4u nn[4];
    constexpr int DP = depth1<NK>();
    WAITV1(imin(DP - 1, NK - 1 - I), bf[I]);
    tieN<4>(cur);
    SB();
    acc[0] = mfma32(cur[0], bf[I], acc[0]);
    SB();
    if constexpr (I + DP < NK) LDA(bf[I + DP], vof<SH>(x, I + DP, vo), so + (unsigned)(I + DP) * KB);
    if constexpr (I + DP == NK - 1) { if (sig) sig_set(x, sig); }
    SB();
    if constexpr (I + 2 < K1) ldaN<4, POS + 4 * (I + 2)>(x, nn);
    mx = mx4(mx, bf[I]);
#pragma unroll
    for (int f = 1; f < 4; ++f) acc[f] = mfma32(cur[f], bf[I], acc[f]);
    __builtin_amdgcn_sched_group_barrier(0x100, 1, 0);
#pragma unroll
    for (int f = 1; f < 4; ++f) {
      __builtin_amdgcn_sched_group_barrier(0x008, 1, 0);
      __builtin_amdgcn_sched_group_barrier(0x100, 1, 0);
      __builtin_amdgcn_sched_group_barrier(0x002, 1, 0);
    }
    SB();
    if constexpr (I + 1 < K1) KL1<POS, NK, SH, K0, K1, I + 1>::run(x, bf, acc, mx, nxt, nn, so, vo, sig);
  }
};
template <int POS, bool SH, int K1, int I> struct KL1R {
  static DI void run(Ctx &x, f4 (&acc)[4], unsigned so, const unsigned (&vo)[4]) {
    const v4u bv = load_sure(x, vof<SH>(x, I, vo), so + (unsigned)I * KB);
    v4u a[4];
    ldaN<4, POS + 4 * I>(x, a);
#pragma unroll
    for (int f = 0; f < 4; ++f) acc[f] = mfma32(a[f], bv, acc[f]);
    if constexpr (I + 1 < K1) KL1R<POS, SH, K1, I + 1>::run(x, acc, so, vo);
  }
};
template <bool ST> DI unsigned canary_poll1(Ctx &x, unsigned so, unsigned cvo, Stats &st, unsigned sst) {
  unsigned cn;
  LDC(cn, cvo, so);
  if constexpr (ST) stats_load(x, st, sst);
  WAITV1(0, cn);
  if constexpr (ST) { TIE(st.v[0]); TIE(st.v[1]); cn = max(cn, max(max(st.v[0].x, st.v[0].y), max(st.v[1].x, st.v[1].y))); }
  return cn;
}
template <int NK, bool SH, bool ST>
DI void pro1(Ctx &x, v4u (&bf)[NK], unsigned so, const unsigned (&vo)[4], unsigned cvo, Stats &st, unsigned sst, unsigned &cn) {
  while (anybad(canary_poll1<ST>(x, so, cvo, st, sst)) && !giveup(x)) {}
  cn = 0;
#pragma unroll
  for (int i = 0; i < depth1<NK>(); ++i) LDA(bf[i], vof<SH>(x, i, vo), so + (unsigned)i * KB);
}
template <int NK, bool SH, bool ST>
DI void chk1(Ctx &x, v4u (&bf)[NK], unsigned so, const unsigned (&vo)[4], unsigned cvo, Stats &st, unsigned sst, unsigned cn) {
}
template <int POS, int NK, bool SH, int K0, int K1>
DI void kloop1(Ctx &x, v4u (&bf)[NK], f4 (&acc)[4], unsigned so, const unsigned (&vo)[4], unsigned sig) {
  f4 save[4];
#pragma unroll
  for (int f = 0; f < 4; ++f) save[f] = acc[f];
  unsigned mx = 0;
  v4u a0[4], a1[4];
  ldaN<4, POS + 4 * K0>(x, a0);
  ldaN<4, POS + 4 * (K0 + 1)>(x, a1);
  KL1<POS, NK, SH, K0, K1, K0>::run(x, bf, acc, mx, a0, a1, so, vo, sig);
  if (__builtin_expect(anybad(mx), 0)) {
#pragma unroll
    for (int f = 0; f < 4; ++f) acc[f] = save[f];
    KL1R<POS, SH, K1, K0>::run(x, acc, so, vo);
  }
}
template <int NT, int POS, int K0, int K1, int I> struct KL2 {
  static DI void run(Ctx &x, v4u (&bf)[16][2], f4 (&acc)[NT][2], unsigned &mx, v4u (&cur)[NT], unsigned so, unsigned sig) {
    v4u nxt[NT];
    WAITV2(2 * imin(3, 15 - I), bf[I][0], bf[I][1]);
    tieN<NT>(cur);
    SB();
    acc[0][0] = mfma32(cur[0], bf[I][0], acc[0][0]);
    acc[0][1] = mfma32(cur[0], bf[I][1], acc[0][1]);
    SB();
    if constexpr (I + 4 < 16) LDA(bf[I + 4][0], x.lane16, so + (unsigned)(I + 4) * KB);
    SB();
    acc[1][0] = mfma32(cur[1], bf[I][0], acc[1][0]);
    acc[1][1] = mfma32(cur[1], bf[I][1], acc[1][1]);
    SB();
    if constexpr (I + 4 < 16) LDA(bf[I + 4][1], x.lane16, so + (unsigned)(16 + I + 4) * KB);
    if constexpr (I == (NT == 6 ? 6 : 11)) sig_set(x, sig);
    SB();
    if constexpr (I + 1 < K1) ldaN<NT, POS + (I + 1) * NT>(x, nxt);
    mx = mx4(mx4(mx, bf[I][0]), bf[I][1]);
#pragma unroll
    for (int f = 2; f < NT; ++f) {
      acc[f][0] = mfma32(cur[f], bf[I][0], acc[f][0]);
      acc[f][1] = mfma32(cur[f], bf[I][1], acc[f][1]);
    }
#pragma unroll
    for (int f = 0; f < NT; ++f) {
      __builtin_amdgcn_sched_group_barrier(0x100, 1, 0);
      __builtin_amdgcn_sched_group_barrier(0x008, 1, 0);
    }
#pragma unroll
    for (int f = 0; f < NT - 4; ++f) {
      __builtin_amdgcn_sched_group_barrier(0x008, 1, 0);
      __builtin_amdgcn_sched_group_barrier(0x002, 2, 0);
    }
    SB();
    if constexpr (I + 1 < K1) KL2<NT, POS, K0, K1, I + 1>::run(x, bf, acc, mx, nxt, so, sig);
  }
};
template <int NT, int POS, int K1, int I> struct KL2R {
  static DI void run(Ctx &x, f4 (&acc)[NT][2], unsigned so) {
    const v4u b0 = load_sure(x, x.lane16, so + (unsigned)I * KB), b1 = load_sure(x, x.lane16, so + (unsigned)(16 + I) * KB);
    v4u a[NT];
    ldaN<NT, POS + I * NT>(x, a);
#pragma unroll
    for (int f = 0; f < NT; ++f) {
      acc[f][0] = mfma32(a[f], b0, acc[f][0]);
      acc[f][1] = mfma32(a[f], b1, acc[f][1]);
    }
    if constexpr (I + 1 < K1) KL2R<NT, POS, K1, I + 1>::run(x, acc, so);
  }
};
DI unsigned stmax(const Stats (&st)[2]) {
  return max(max(max(st[0].v[0].x, st[0].v[0].y), max(st[0].v[1].x, st[0].v[1].y)), max(max(st[1].v[0].x, st[1].v[0].y), max(st[1].v[1].x, st[1].v[1].y)));
}
DI void poll2(Ctx &x, Stats (&st)[2], unsigned so, unsigned sst) {
  const unsigned cvo = (unsigned)(x.lane & 31) * KB + 1020u;
  while (true) {
    unsigned cn;
    LDC(cn, cvo, so);
    stats_load(x, st[0], sst); stats_load(x, st[1], sst + 1024u);
    WAITV1(0, cn);
    TIE(st[0].v[0]); TIE(st[0].v[1]); TIE(st[1].v[0]); TIE(st[1].v[1]);
    if (!anybad(max(cn, stmax(st))) || giveup(x)) break;
  }
}
DI void first2(Ctx &x, v4u (&bf)[16][2], unsigned so) {
#pragma unroll
  for (int i = 0; i < 4; ++i) { LDA(bf[i][0], x.lane16, so + (unsigned)i * KB); LDA(bf[i][1], x.lane16, so + (unsigned)(16 + i) * KB); }
}
DI void pro2(Ctx &x, v4u (&bf)[16][2], Stats (&st)[2], unsigned so, unsigned sst, unsigned &cn) {
  poll2(x, st, so, sst);
  cn = 0;
  first2(x, bf, so);
}
DI void chk2(Ctx &x, v4u (&bf)[16][2], Stats (&st)[2], unsigned so, unsigned sst, unsigned cn) {
}
template <int NT, int POS, int K0, int K1>
DI void kloop2(Ctx &x, v4u (&bf)[16][2], f4 (&acc)[NT][2], unsigned so, unsigned sig, unsigned &mx) {
  v4u a0[NT];
  ldaN<NT, POS + K0 * NT>(x, a0);
  KL2<NT, POS, K0, K1, K0>::run(x, bf, acc, mx, a0, so, sig);
}
template <int NT, int POS> DI void redo2(Ctx &x, f4 (&acc)[NT][2], unsigned so) {
#pragma unroll
  for (int f = 0; f < NT; ++f) { acc[f][0] = f4{0.f, 0.f, 0.f, 0.f}; acc[f][1] = f4{0.f, 0.f, 0.f, 0.f}; }
  KL2R<NT, POS, 16, 0>::run(x, acc, so);
}

DI void featmap4(const f4 (&v)[4], const v2u (&wphi)[2], v2u (&plus)[4], v2u (&minus)[4]) {
  const float L2E = 1.4426950408889634f;
  f4 p[4];
#pragma unroll
  for (int i = 0; i < 4; ++i) p[i] = mfma16(wphi[i & 1], v2u{pk2(v[i].x, v[i].y), pk2(v[i].z, v[i].w)}, f4{0.f, 0.f, 0.f, 0.f});
  float mx[4], nm[4];
#pragma unroll
  for (int i = 0; i < 4; ++i) {
    p[i] *= L2E;
    mx[i] = fmaxf(fmaxf(p[i].x, p[i].y), fmaxf(p[i].z, p[i].w));
    nm[i] = -fminf(fminf(p[i].x, p[i].y), fminf(p[i].z, p[i].w));
  }
  xred4<true>(mx[0], mx[1], mx[2], mx[3]);
  xred4<true>(nm[0], nm[1], nm[2], nm[3]);
  f4 e[4], n[4];
  float se[4], sn[4];
#pragma unroll
  for (int i = 0; i < 4; ++i) {
    const f4 a = p[i] - mx[i], b = -p[i] - nm[i];
    e[i] = f4{__builtin_amdgcn_exp2f(a.x), __builtin_amdgcn_exp2f(a.y), __builtin_amdgcn_exp2f(a.z), __builtin_amdgcn_exp2f(a.w)};
    n[i] = f4{__builtin_amdgcn_exp2f(b.x), __builtin_amdgcn_exp2f(b.y), __builtin_amdgcn_exp2f(b.z), __builtin_amdgcn_exp2f(b.w)};
    se[i] = (e[i].x + e[i].y) + (e[i].z + e[i].w);
    sn[i] = (n[i].x + n[i].y) + (n[i].z + n[i].w);
  }
  xred4<false>(se[0], se[1], se[2], se[3]);
  xred4<false>(sn[0], sn[1], sn[2], sn[3]);
#pragma unroll
  for (int i = 0; i < 4; ++i) {
    const f4 a = e[i] * __builtin_amdgcn_rcpf(se[i]), b = n[i] * __builtin_amdgcn_rcpf(sn[i]);
    plus[i] = v2u{pk2(a.x, a.y), pk2(a.z, a.w)};
    minus[i] = v2u{pk2(b.x, b.y), pk2(b.z, b.w)};
  }
}
DI v2u trread(const Ctx &x, unsigned off) {
  s4 r = __builtin_amdgcn_ds_read_tr16_b64_v4i16((LAS s4 *)(x.sm + off));
  return __builtin_bit_cast(v2u, r);
}
DI f4 shsum(f4 s) {
#pragma unroll
  for (int o = 4; o < 64; o <<= 1) {
    s.x += __shfl_xor(s.x, o); s.y += __shfl_xor(s.y, o); s.z += __shfl_xor(s.z, o); s.w += __shfl_xor(s.w, o);
  }
  return s;
}

DI void poll_flags(Ctx &x, unsigned off, unsigned tag) {
  for (unsigned sp = 0;; ++sp) {
    unsigned f0, f1, f2, f3;
    const unsigned vo = (unsigned)x.lane * 64u;
    LDC(f0, vo, off); LDC(f1, vo, off + 4096u); LDC(f2, vo, off + 8192u); LDC(f3, vo, off + 12288u);
    asm volatile("s_waitcnt vmcnt(0)" : "+v"(f0), "+v"(f1), "+v"(f2), "+v"(f3)::"memory");
    if (__all(f0 == tag && f1 == tag && f2 == tag && f3 == tag)) break;
    if (sp > SPIN_CAP) {
      if (x.lane == 0) __hip_atomic_store(x.ctl + OFF_ABORT / 4, 3u, RLX_AGENT);
      break;
    }
    __builtin_amdgcn_s_sleep(8);
  }
}
DI unsigned sigv(int L, int ph) { return (unsigned)(L * 8 + ph + 1); }

DI void compute_path(Ctx &x, const Params &p, v4u (&xres)[2], const v2u (&wph)[2], unsigned tag) {
  const unsigned vz[4] = {0u, 0u, 0u, 0u};
  v4u x1r[2], x2r[2];
#pragma unroll 1
  for (int L = 0; L < 2; ++L) {
    const unsigned lb0 = OFF_L0 + (unsigned)L * L_STRIDE;
    v4u phq[2][2];
    GBAR();
    {
      refresh(x);
      FS(0);
      unsigned lb = lb0;
      asm volatile("" : "+s"(lb));
      v4u bf[16][2];
      Stats st[2];
      const unsigned so = lb + O_XB + (unsigned)x.TA * 16u * KB, sst = lb + O_XS + (unsigned)x.TA * 1024u;
      unsigned cn;
      pro2(x, bf, st, so, sst, cn);
      FS(1); FSP(0);
      BAR();
      if (L == 0 && threadIdx.x == 0) __hip_atomic_store(x.ctl + OFF_FLAGS2 / 4 + x.b * 16, tag, RLX_AGENT);
      chk2(x, bf, st, so, sst, cn);
      FS(2);
      f4 acc[6][2];
#pragma unroll
      for (int f = 0; f < 6; ++f) { acc[f][0] = f4{0.f, 0.f, 0.f, 0.f}; acc[f][1] = f4{0.f, 0.f, 0.f, 0.f}; }
      unsigned mx = 0;
      kloop2<6, POS1, 0, 16>(x, bf, acc, so, sigv(L, 0), mx);
      if (__builtin_expect(anybad(mx), 0)) redo2<6, POS1>(x, acc, so);
      FS(3);
      BAR();
      MARK("P1epi");
      f4 val[2][6];
#pragma unroll
      for (int j = 0; j < 2; ++j) {
        float mean, var;
        stats_fin(x, st[j], sst + (unsigned)j * 1024u, mean, var);
        const float rstd = __builtin_amdgcn_rsqf(var + 1e-5f), mr = mean * rstd;
#pragma unroll
        for (int f = 0; f < 6; ++f) val[j][f] = acc[f][j] * rstd - ldcs(x, f) * mr;
      }
      {
        v2u pl[4], mi[4];
        const f4 kk[4] = {val[0][2], val[0][3], val[1][2], val[1][3]};
        featmap4(kk, wph, pl, mi);
#pragma unroll
        for (int i = 0; i < 4; ++i) {
          const int j = i >> 1, h = i & 1;
          const unsigned img = IMG + (unsigned)((2 * x.m + j) * 16 + x.tok) * 32u + (unsigned)x.g * 8u + (unsigned)(h * 3) * 4096u;
          *(LAS v2u *)(x.sm + img) = pl[i];
          *(LAS v2u *)(x.sm + img + 4096u) = mi[i];
          *(LAS v2u *)(x.sm + img + 8192u) = v2u{pk2(val[j][4 + h].x, val[j][4 + h].y), pk2(val[j][4 + h].z, val[j][4 + h].w)};
        }
      }
      MARK("P1epiend");
      FS(4);
      BAR();
      FS(5);
      {
        v2u pl[4], mi[4];
        TIE(val[0][0]); TIE(val[0][1]); TIE(val[1][0]); TIE(val[1][1]);
        const f4 qq[4] = {val[0][0], val[0][1], val[1][0], val[1][1]};
        featmap4(qq, wph, pl, mi);
#pragma unroll
        for (int i = 0; i < 4; ++i) phq[i >> 1][i & 1] = v4u{pl[i].x, pl[i].y, mi[i].x, mi[i].y};
      }
      FS(6);
    }
    GBAR();
    GBAR();
    {
      refresh(x);
      unsigned lb = lb0;
      asm volatile("" : "+s"(lb));
      BAR();
      FS(7);
      v4u ka[2], za[2];
#pragma unroll
      for (int h = 0; h < 2; ++h) {
        ka[h] = *lds4(x, M_KVA + (unsigned)h * 1024u + x.lane16);
        const v2u z0 = *(const LAS v2u *)(x.sm + M_ZA + (unsigned)(h * 2) * 32u + (unsigned)x.g * 8u);
        const v2u z1 = *(const LAS v2u *)(x.sm + M_ZA + (unsigned)(h * 2 + 1) * 32u + (unsigned)x.g * 8u);
        za[h] = v4u{z0.x, z0.y, z1.x, z1.y};
      }
#pragma unroll
      for (int j = 0; j < 2; ++j) {
        f4 o[2];
#pragma unroll
        for (int h = 0; h < 2; ++h) {
          const f4 num = mfma32(ka[h], phq[j][h], f4{0.f, 0.f, 0.f, 0.f});
          const f4 den = mfma32(za[h], phq[j][h], f4{0.f, 0.f, 0.f, 0.f});
          o[h] = num * __builtin_amdgcn_rcpf(den.x + 1e-6f);
        }
        stg(x.rs, x.lane16, lb + O_OB + (unsigned)((x.TA + j) * 16 + x.pp) * KB, pack8(o[0], o[1]));
      }
      FS(8);
      {
        const unsigned ob = lb + O_OB + (unsigned)(x.TA * 16 + x.pp) * KB;
        warm(x, (unsigned)((x.lane >> 3) & 1) * 16u * KB + (unsigned)(x.lane & 7) * 128u, ob, ob);
      }
    }
    GBAR();
    {
      refresh(x);
      unsigned lb = lb0;
      asm volatile("" : "+s"(lb));
      SLEEP(DLY_2);
      FS(9);
      v4u bf[16];
      const unsigned so = lb + O_OB + (unsigned)x.T * 16u * KB;
      Stats st;
      unsigned cn;
      const unsigned cvo = (unsigned)(x.lane & 15) * KB + 1020u;
      pro1<16, false, false>(x, bf, so, vz, cvo, st, 0u, cn);
      FS(10); FSP(1);
      BAR();
      chk1<16, false, false>(x, bf, so, vz, cvo, st, 0u, cn);
      FS(11);
      f4 acc[4];
#pragma unroll
      for (int f = 0; f < 4; ++f) acc[f] = f4{0.f, 0.f, 0.f, 0.f};
      kloop1<POS2, 16, false, 0, 16>(x, bf, acc, so, vz, sigv(L, 2));
      FS(12);
      x1r[0] = pack8(ulo(xres[0]) + acc[0], uhi(xres[0]) + acc[1]);
      x1r[1] = pack8(ulo(xres[1]) + acc[2], uhi(xres[1]) + acc[3]);
      const unsigned fo = lb + O_X1B + (unsigned)(x.T * 16 + 2 * x.c) * KB;
      stg(x.rs, x.lane16, fo, x1r[0]); stg(x.rs, x.lane16, fo + KB, x1r[1]);
      put_stats(x, x1r[0], x1r[1], lb + O_X1S);
      sig_set(x, sigv(L, 2), M_SIG2);
      FS(13);
      warm(x, (unsigned)(x.lane & 15) * 128u, fo, lb + O_X1S + (unsigned)(x.T * 8 + x.c) * 128u);
    }
    float mean1, rstd1;
    GBAR();
    {
      refresh(x);
      unsigned lb = lb0;
      asm volatile("" : "+s"(lb));
      SLEEP(DLY_3);
      v4u bf[16];
      Stats st;
      const unsigned so = lb + O_X1B + (unsigned)x.T * 16u * KB;
      unsigned cn;
      const unsigned cvo = (unsigned)(x.lane & 15) * KB + 1020u;
      pro1<16, false, true>(x, bf, so, vz, cvo, st, lb + O_X1S + (unsigned)x.T * 1024u, cn);
      FSP(2);
      BAR();
      chk1<16, false, true>(x, bf, so, vz, cvo, st, lb + O_X1S + (unsigned)x.T * 1024u, cn);
      FS(14);
      f4 acc[4];
#pragma unroll
      for (int f = 0; f < 4; ++f) acc[f] = f4{0.f, 0.f, 0.f, 0.f};
      kloop1<POS3, 16, false, 0, 16>(x, bf, acc, so, vz, sigv(L, 3));
      FS(15);
      float var;
      stats_fin(x, st, lb + O_X1S + (unsigned)x.T * 1024u, mean1, var);
      rstd1 = __builtin_amdgcn_rsqf(var + 1e-5f);
      const float a = rstd1 * __builtin_amdgcn_rsqf(__builtin_fmaf(var * rstd1, rstd1, 1e-5f)), ma = mean1 * a;
      f4 y[4];
#pragma unroll
      for (int f = 0; f < 4; ++f) y[f] = gelu4(acc[f] * a - ldcs(x, 6 + f) * ma);
      const unsigned fo = lb + O_HB + (unsigned)(x.T * 16 + 2 * x.c) * KB;
      stg(x.rs, x.lane16, fo, pack8(y[0], y[1])); stg(x.rs, x.lane16, fo + KB, pack8(y[2], y[3]));
      sig_set(x, sigv(L, 3), M_SIG2);
      FS(16);
      warm(x, (unsigned)(x.lane & 15) * 128u, fo, fo);
    }
    GBAR();
    {
      refresh(x);
      unsigned lb = lb0;
      asm volatile("" : "+s"(lb));
      SLEEP(DLY_4);
      v4u bf[16];
      unsigned vo[4];
#pragma unroll
      for (int q = 0; q < 4; ++q) {
        const int d = q == 0 ? -1 : (q == 1 ? 1 : (q == 2 ? -2 : 2));
        const int src = 16 * x.T + x.tok + d;
        vo[q] = (src >= 0 && src < 2048) ? (unsigned)((src >> 4) * 16 * 1024 + (x.g * 16 + (src & 15)) * 16) : OOB;
      }
      const unsigned so = lb + O_HB;
      unsigned cvo;
      {
        const int s_ = x.lane & 15, nb = x.T + (((s_ >> 2) & 1) ? 1 : -1);
        const int tt = (x.lane & 16) ? nb : x.T;
        cvo = (tt >= 0 && tt < 128) ? (unsigned)(tt * 16 + s_) * KB + 1020u : OOB;
      }
      Stats st;
      unsigned cn;
      pro1<16, true, false>(x, bf, so, vo, cvo, st, 0u, cn);
      FSP(3);
      BAR();
      chk1<16, true, false>(x, bf, so, vo, cvo, st, 0u, cn);
      FS(17);
      f4 acc[4];
#pragma unroll
      for (int f = 0; f < 4; ++f) acc[f] = f4{0.f, 0.f, 0.f, 0.f};
      MARK("P4loop");
      kloop1<POS4, 16, true, 0, 16>(x, bf, acc, so, vo, sigv(L, 4));
      MARK("P4end");
      FS(18);
      const float mr = mean1 * rstd1;
      x2r[0] = pack8(ulo(x1r[0]) * rstd1 - mr + acc[0], uhi(x1r[0]) * rstd1 - mr + acc[1]);
      x2r[1] = pack8(ulo(x1r[1]) * rstd1 - mr + acc[2], uhi(x1r[1]) * rstd1 - mr + acc[3]);
      const unsigned fo = lb + O_X2B + (unsigned)(x.T * 16 + 2 * x.c) * KB;
      stg(x.rs, x.lane16, fo, x2r[0]); stg(x.rs, x.lane16, fo + KB, x2r[1]);
      put_stats(x, x2r[0], x2r[1], lb + O_X2S);
      sig_set(x, sigv(L, 4), M_SIG2);
      FS(19);
      warm(x, (unsigned)(x.lane & 15) * 128u, fo, lb + O_X2S + (unsigned)(x.T * 8 + x.c) * 128u);
    }
    GBAR();
    {
      refresh(x);
      unsigned lb = lb0;
      asm volatile("" : "+s"(lb));
      SLEEP(DLY_5);
      v4u bf[16][2];
      Stats st[2];
      const unsigned so = lb + O_X2B + (unsigned)x.TA * 16u * KB, sst = lb + O_X2S + (unsigned)x.TA * 1024u;
      unsigned cn;
      pro2(x, bf, st, so, sst, cn);
      FSP(4);
      BAR();
      chk2(x, bf, st, so, sst, cn);
      FS(20);
      f4 acc[7][2];
#pragma unroll
      for (int f = 0; f < 7; ++f) { acc[f][0] = f4{0.f, 0.f, 0.f, 0.f}; acc[f][1] = f4{0.f, 0.f, 0.f, 0.f}; }
      unsigned mx = 0;
      MARK("P5loop");
      kloop2<7, POS5, 0, 13>(x, bf, acc, so, sigv(L, 5), mx);
      BAR();
      kloop2<7, POS5, 13, 16>(x, bf, acc, so, sigv(L, 5), mx);
      if (__builtin_expect(anybad(mx), 0)) redo2<7, POS5>(x, acc, so);
      FS(21);
      BAR();
      FS(22);
      MARK("P5epi");
      const int u = x.pp >> 1, odd = x.pp & 1;
#pragma unroll
      for (int j = 0; j < 2; ++j) {
        float mean, var;
        stats_fin(x, st[j], sst + (unsigned)j * 1024u, mean, var);
        const float rstd = __builtin_amdgcn_rsqf(var + 1e-5f), mr = mean * rstd;
        const unsigned hb = lb + O_H3B + (unsigned)((x.TA + j) * 56 + 7 * u) * KB;
#pragma unroll
        for (int r = 0; r < 3; ++r) {
          const f4 y0 = gelu4(acc[2 * r][j] * rstd - ldcs(x, 10 + 2 * r) * mr), y1 = gelu4(acc[2 * r + 1][j] * rstd - ldcs(x, 11 + 2 * r) * mr);
          stg(x.rs, x.lane16, hb + (unsigned)(odd ? 4 + r : r) * KB, pack8(y0, y1));
        }
        const f4 y = gelu4(acc[6][j] * rstd - ldcs(x, 16) * mr);
        stg2(x.rs, x.lane16 + (unsigned)odd * 8u, hb + 3 * KB, v2u{clampw(pk2(y.x, y.y)), clampw(pk2(y.z, y.w))});
      }
      FS(23);
      {
        const unsigned hb0 = lb + O_H3B + (unsigned)(x.TA * 56 + 7 * u + (odd ? 3 : 0)) * KB;
        warm(x, (unsigned)(x.lane >> 5) * 56u * KB + (unsigned)(x.lane & 31) * 128u, hb0, hb0);
      }
    }
    GBAR();
    {
      refresh(x);
      unsigned lb = lb0;
      asm volatile("" : "+s"(lb));
      SLEEP(DLY_6);
      v4u hf[56];
      const unsigned so = lb + O_H3B + (unsigned)x.T * 56u * KB;
      Stats st;
      unsigned cn;
      const unsigned cvo = (unsigned)min(x.lane, 55) * KB + 1020u;
      pro1<56, false, false>(x, hf, so, vz, cvo, st, 0u, cn);
      FSP(5);
      BAR();
      chk1<56, false, false>(x, hf, so, vz, cvo, st, 0u, cn);
      FS(24);
      f4 acc[4];
#pragma unroll
      for (int f = 0; f < 4; ++f) acc[f] = f4{0.f, 0.f, 0.f, 0.f};
      kloop1<POS6, 56, false, 0, 17>(x, hf, acc, so, vz, 0u);
      BAR();
      FS(25);
      kloop1<POS6, 56, false, 17, 39>(x, hf, acc, so, vz, 0u);
      BAR();
      FS(26);
      kloop1<POS6, 56, false, 39, 56>(x, hf, acc, so, vz, 0u);
      FS(27);
      BAR();
      xres[0] = pack8(ulo(x1r[0]) + (ulo(x2r[0]) + acc[0]), uhi(x1r[0]) + (uhi(x2r[0]) + acc[1]));
      xres[1] = pack8(ulo(x1r[1]) + (ulo(x2r[1]) + acc[2]), uhi(x1r[1]) + (uhi(x2r[1]) + acc[3]));
      if (L == 0) {
        const unsigned fo = lb + L_STRIDE + O_XB + (unsigned)(x.T * 16 + 2 * x.c) * KB;
        stg(x.rs, x.lane16, fo, xres[0]); stg(x.rs, x.lane16, fo + KB, xres[1]);
        put_stats(x, xres[0], xres[1], lb + L_STRIDE + O_XS);
        warm(x, (unsigned)(x.lane & 15) * 128u, fo, lb + L_STRIDE + O_XS + (unsigned)(x.T * 8 + x.c) * 128u);
        SLEEP(DLY_1);
      } else {
        GAS unsigned char *op = (GAS unsigned char *)p.out + ((size_t)(16 * x.T + x.tok) * 512 + 64 * x.c + 4 * x.g) * 2;
        *(GAS v2u *)op = v2u{xres[0].x, xres[0].y};
        *(GAS v2u *)(op + 32) = v2u{xres[0].z, xres[0].w};
        *(GAS v2u *)(op + 64) = v2u{xres[1].x, xres[1].y};
        *(GAS v2u *)(op + 96) = v2u{xres[1].z, xres[1].w};
      }
      FS(28); FSP(6);
    }
  }
}

DI void poison_h3(Ctx &x, unsigned lb, int j) {
  const int u = x.pp >> 1, odd = x.pp & 1;
  const unsigned hb = lb + O_H3B + (unsigned)((x.TA + j) * 56 + 7 * u) * KB;
#pragma unroll
  for (int r = 0; r < 3; ++r) poi4(x.rs, x.lane16, hb + (unsigned)(odd ? 4 + r : r) * KB);
  poi2(x.rs, x.lane16 + (unsigned)odd * 8u, hb + 3 * KB);
}
DI void helper_path(Ctx &x, unsigned tag) {
#pragma unroll 1
  for (int L = 0; L < 2; ++L) {
    const unsigned lb0 = OFF_L0 + (unsigned)L * L_STRIDE;
    GBAR();
    {
      refresh(x);
      unsigned lb = lb0;
      asm volatile("" : "+s"(lb));
      FS(0);
      DRAIN_BAR();
      FS(1);
      if (L != 0) {
        sig_wait(x, sigv(L, 0));
        dmaR<96, 128>(x);
      }
      BAR();
      if (L != 0) dmaR<128, 156>(x);
      {
        const unsigned fo = (unsigned)(x.T * 16 + 2 * x.c) * KB, sto = (unsigned)(x.T * 8 + x.c) * 128u;
        poi4(x.rs, x.lane16, lb + O_X1B + fo); poi4(x.rs, x.lane16, lb + O_X1B + fo + KB);
        poi4(x.rs, x.lane16, lb + O_X2B + fo); poi4(x.rs, x.lane16, lb + O_X2B + fo + KB);
        if (x.g == 0) { poi2(x.rs, (unsigned)x.tok * 8u, lb + O_X1S + sto); poi2(x.rs, (unsigned)x.tok * 8u, lb + O_X2S + sto); }
      }
      FS(2);
      BAR();
      FS(3);
      {
        const int h = x.m >> 1, sg = x.m & 1;
        const unsigned lo = (unsigned)(4 * x.g + (x.tok >> 2)) * 32u + (unsigned)(x.tok & 3) * 8u;
        const unsigned ia = IMG + (unsigned)(h * 3 + sg) * 4096u + lo, iv = IMG + (unsigned)(h * 3 + 2) * 4096u + lo;
        f4 kv = {0.f, 0.f, 0.f, 0.f}, z = {0.f, 0.f, 0.f, 0.f};
#pragma unroll
        for (int tb = 0; tb < 4; ++tb) {
          const v2u a0 = trread(x, ia + tb * 1024u), a1 = trread(x, ia + tb * 1024u + 512u);
          const v2u b0 = trread(x, iv + tb * 1024u), b1 = trread(x, iv + tb * 1024u + 512u);
          const v4u a = {a0.x, a0.y, a1.x, a1.y};
          kv = mfma32(a, v4u{b0.x, b0.y, b1.x, b1.y}, kv);
          z = mfma32(a, v4u{0x3F803F80u, 0x3F803F80u, 0x3F803F80u, 0x3F803F80u}, z);
        }
        stg2(x.rs, (unsigned)x.lane * 8u, lb + O_KVP + (unsigned)((x.pp * 16 + x.tau) * 4 + x.m) * KB, v2u{clampw(pk2(kv.x, kv.y)), clampw(pk2(kv.z, kv.w))});
        if (x.tok == 0) stg(x.rs, (unsigned)x.g * 16u, lb + O_KVZ + (unsigned)((x.pp * 4 + x.m) * 16 + x.tau) * 64u, clamp4(__builtin_bit_cast(v4u, z)));
      }
      poison_h3(x, lb, 0); poison_h3(x, lb, 1);
      FS(4);
    }
    GBAR();
    {
      refresh(x);
      unsigned lb = lb0;
      asm volatile("" : "+s"(lb));
      if (L == 0) {
        if (x.m == 0) {
          poll_flags(x, OFF_FLAGS2, tag);
          *(volatile LAS unsigned *)(x.sm + M_RDY) = 1u;
        } else {
          for (unsigned sp = 0; sp < (1u << 20); ++sp) {
            if (__builtin_amdgcn_readfirstlane(*(volatile LAS unsigned *)(x.sm + M_RDY))) break;
            __builtin_amdgcn_s_sleep(2);
          }
        }
        v4u cv[3];
#pragma unroll
        for (int jj = 0; jj < 3; ++jj) {
          const int u = min(6 + x.m + 4 * jj, 16);
          cv[jj] = ldg(x.rs, x.lane16, OFF_CSP + (u < 10 ? 96u + (unsigned)(x.c * 4 + u - 6) : 128u + (unsigned)(x.pp * 7 + u - 10)) * KB);
        }
#pragma unroll
        for (int jj = 0; jj < 3; ++jj) {
          const f4 s = shsum(__builtin_bit_cast(f4, cv[jj]));
          const int u = 6 + x.m + 4 * jj;
          if (u < 17 && x.lane < 4) *(LAS f4 *)(x.sm + M_CS + (unsigned)u * 64u + (unsigned)x.lane * 16u) = s;
        }
      }
      SLEEP(DLY_KV);
      v4u pv[16];
      const unsigned so = lb + O_KVP + (unsigned)(x.pp * 64 + x.m) * KB, sz = lb + O_KVZ + (unsigned)(x.pp * 4 + x.m) * KB;
      v4u zv;
      {
        const unsigned cvo = (unsigned)(x.lane & 15) * 4096u + (KV16 ? 508u : 1020u);
        while (true) {
          unsigned cn;
          LDC(cn, cvo, so);
          LDA(zv, x.lane16, sz);
          WAITV2(0, cn, zv);
          if (!anybad(max(cn, max4(zv))) || giveup(x)) break;
        }
      }
      bool again;
      f4 s;
      v2u pw[16];
      do {
        asm volatile("" ::: "memory");
#pragma unroll
        for (int i = 0; i < 16; ++i) pw[i] = __builtin_amdgcn_raw_buffer_load_b64(x.rs, (unsigned)x.lane * 8u, so + (unsigned)i * 4096u, AUX_SC1);
        unsigned mx = 0;
#pragma unroll
        for (int i = 0; i < 16; ++i) mx = max(mx, max(pw[i].x, pw[i].y));
        again = anybad(mx) && !giveup(x);
      } while (__builtin_expect(again, 0));
      s = ulo(v4u{pw[0].x, pw[0].y, 0u, 0u});
#pragma unroll
      for (int i = 1; i < 16; ++i) s += ulo(v4u{pw[i].x, pw[i].y, 0u, 0u});
      *(LAS v2u *)(x.sm + M_KVA + (unsigned)(x.m >> 1) * 1024u + x.lane16 + (unsigned)(x.m & 1) * 8u) = v2u{pk2(s.x, s.y), pk2(s.z, s.w)};
      const f4 zs = shsum(__builtin_bit_cast(f4, zv));
      if (x.lane < 4) *(LAS v2u *)(x.sm + M_ZA + (unsigned)x.m * 32u + (unsigned)x.lane * 8u) = v2u{pk2(zs.x, zs.y), pk2(zs.z, zs.w)};
      FS(5); FSP(0);
      GBAR();
      BAR();
      FS(6);
      if (L == 0) dmaR<96, 156>(x);
      dmaR<156, 188>(x);
    }
    GBAR();
    {
      refresh(x);
      unsigned lb = lb0;
      asm volatile("" : "+s"(lb));
      DRAIN_BAR();
      FS(7);
      sig_wait(x, sigv(L, 2));
      dmaR<188, 220>(x);
      sig_wait(x, sigv(L, 2), M_SIG2);
      dmaR<220, 252>(x);
    }
    GBAR();
    {
      refresh(x);
      unsigned lb = lb0;
      asm volatile("" : "+s"(lb));
      DRAIN_BAR();
      FS(8);
      sig_wait(x, sigv(L, 3));
      FS(15);
      dmaR<252, 284>(x);
      sig_wait(x, sigv(L, 3), M_SIG2);
      dmaR<284, 316>(x);
      FS(16);
    }
    GBAR();
    {
      refresh(x);
      unsigned lb = lb0;
      asm volatile("" : "+s"(lb));
      DRAIN_BAR();
      if (L == 0) {
        const unsigned fo = lb + L_STRIDE + O_XB + (unsigned)(x.T * 16 + 2 * x.c) * KB;
        poi4(x.rs, x.lane16, fo); poi4(x.rs, x.lane16, fo + KB);
        if (x.g == 0) poi2(x.rs, (unsigned)x.tok * 8u, lb + L_STRIDE + O_XS + (unsigned)(x.T * 8 + x.c) * 128u);
      }
      FS(9);
      sig_wait(x, sigv(L, 4));
      FS(18);
      dmaR<316, 348>(x);
      sig_wait(x, sigv(L, 4), M_SIG2);
      dmaR<348, 380>(x);
      FS(19);
    }
    GBAR();
    {
      refresh(x);
      unsigned lb = lb0;
      asm volatile("" : "+s"(lb));
      DRAIN_BAR();
      if (L == 0) {
        const unsigned fo = lb + L_STRIDE + O_HB + (unsigned)(x.T * 16 + 2 * x.c) * KB;
        poi4(x.rs, x.lane16, fo); poi4(x.rs, x.lane16, fo + KB);
        poi4(x.rs, x.lane16, lb + L_STRIDE + O_OB + (unsigned)(x.TA * 16 + x.pp) * KB);
        poi4(x.rs, x.lane16, lb + L_STRIDE + O_OB + (unsigned)((x.TA + 1) * 16 + x.pp) * KB);
      }
      FS(10);
      dmaRT<380, 400>(x);
      DRAIN_BAR();
      sig_wait(x, sigv(L, 5));
      dmaR<400, 444>(x);
      BAR();
      FS(11);
      dmaR6<444, 556>(x);
    }
    GBAR();
    {
      refresh(x);
      v4u wr[17];
      {
        const unsigned ws0 = OFF_WT + (W6_F + (unsigned)(x.c * 224 + 156 + x.m)) * KB;
#pragma unroll
        for (int i = 0; i < 17; ++i) LDA(wr[i], x.lane16, ws0 + (unsigned)i * 4096u);
      }
      WAITV_BAR(17);
      FS(12);
      BAR();
      asm volatile("s_waitcnt vmcnt(0)" : "+v"(wr[0]), "+v"(wr[1]), "+v"(wr[2]), "+v"(wr[3]), "+v"(wr[4]), "+v"(wr[5]), "+v"(wr[6]), "+v"(wr[7]),
                   "+v"(wr[8]), "+v"(wr[9]), "+v"(wr[10]), "+v"(wr[11]), "+v"(wr[12]), "+v"(wr[13]), "+v"(wr[14]), "+v"(wr[15]), "+v"(wr[16])::"memory");
#pragma unroll
      for (int i = 0; i < 17; ++i) *(LAS v4u *)(x.sm + ((556 + 4 * i) % RING + x.m) * KB + x.lane16) = wr[i];
      DRAIN_BAR();
      FS(13);
      dmaR<624, 712>(x, L == 0);
      BAR();
      dmaR<712, 720>(x, L == 0);
      FS(14);
    }
  }
}

}

__global__ void __launch_bounds__(512, 2) mega_kernel(Params p) {
  extern __shared__ __attribute__((aligned(16))) unsigned char smem_[];
  Ctx x;
  x.sm = (LAS unsigned char *)smem_;
  x.lds0 = (unsigned)(size_t)smem_;
  x.lane = threadIdx.x & 63;
  x.w = __builtin_amdgcn_readfirstlane(threadIdx.x >> 6);
  x.b = blockIdx.x;
  refresh(x);
  const int b = x.b;
  x.spins = 0;
  x.ctl = (gu32 *)p.ws;
  x.rs = __builtin_amdgcn_make_buffer_rsrc((void *)p.ws, 0, WS_BYTES, 0x00020000);
  const bool cw = x.w < 4;

  FSA(0);
  const unsigned tag = __hip_atomic_load(x.ctl + OFF_EPOCH / 4, RLX_AGENT) + 1u;
  unsigned pf0 = 0, pf1 = 0;
  if (x.w == 7) {
    unsigned long long pc;
    asm volatile("s_getpc_b64 %0" : "=s"(pc));
    const GAS unsigned char *cp = (const GAS unsigned char *)(pc & ~127ull) + (size_t)(((b >> 3) + 32 * (x.lane & 15)) * 128);
    const GAS unsigned char *wp = (const GAS unsigned char *)p.ws + (size_t)min(x.lane, 28) * (2 * MB) + 1 * MB;
    if (x.lane >= 32) {
      const int q = x.lane & 7;
      const unsigned short *ip = q == 0 ? p.x : q == 1 ? p.wqkv : q == 2 ? p.wo : q == 3 ? p.w1 : q == 4 ? p.w2 : q == 5 ? p.w3 : q == 6 ? p.w4 : p.x;
      const size_t half = (q == 2 || q == 3 || q == 4) ? 256 * KB : 1 * MB;
      wp = (const GAS unsigned char *)ip + ((x.lane & 8) ? half : 0);
    }
    asm volatile("global_load_dword %0, %2, off\n\tglobal_load_dword %1, %3, off" : "=&v"(pf0), "=&v"(pf1) : "v"(cp), "v"(wp) : "memory");
    asm volatile("s_waitcnt vmcnt(0)" : "+v"(pf0), "+v"(pf1)::"memory");
  }
  v2u xin[4] = {};
  unsigned short wraw[2][4] = {};
  if (cw) {
    const GAS unsigned char *xp = (const GAS unsigned char *)p.x + ((size_t)(16 * x.T + x.tok) * 512 + 64 * x.c + 4 * x.g) * 2;
#pragma unroll
    for (int q = 0; q < 4; ++q) xin[q] = *(const GAS v2u *)(xp + 32 * q);
#pragma unroll
    for (int h = 0; h < 2; ++h) {
      const unsigned short *wp = p.wphi + ((2 * x.pp + h) * 16 + 4 * x.g) * 16 + x.tok;
#pragma unroll
      for (int jj = 0; jj < 4; ++jj) wraw[h][jj] = wp[16 * jj];
    }
  }
  if (threadIdx.x < 9) *(LAS unsigned *)(x.sm + M_SIG + threadIdx.x * 4u) = 0u;
  const int bid = ((x.w + 4) & 7) * 256 + b;
  const unsigned short *src = p.wqkv; int ld = 1536, ra = 0, rb = 0, n0 = 0, kstep = 0, csid = -1; unsigned fr[4] = {0u, 0u, 0u, 0u}, ci[4] = {0u, 0u, 0u, 0u};
  if (bid < 384) {
    kstep = bid & 15; const int nb = bid >> 4; n0 = nb * 64; ra = 32 * kstep; rb = ra + 16;
    const int sec = nb >> 3, h0 = 4 * (nb & 7);
#pragma unroll
    for (int ct = 0; ct < 4; ++ct) {
      const int hh = h0 + ct;
      fr[ct] = W1_F + (unsigned)(((hh >> 1) * 16 + kstep) * 6 + sec * 2 + (hh & 1));
      ci[ct] = (unsigned)((hh >> 1) * 6 + sec * 2 + (hh & 1));
    }
    csid = 0;
  } else if (bid < 768) {
    const int mi = (bid - 384) >> 7, r = (bid - 384) & 127; kstep = r & 15; const int nb = r >> 4;
    src = mi == 0 ? p.wo : (mi == 1 ? p.w1 : p.w2); ld = 512; n0 = nb * 64; ra = 32 * kstep; rb = ra + 16;
#pragma unroll
    for (int ct = 0; ct < 4; ++ct) { fr[ct] = W2_F + (unsigned)(mi * 512 + (nb * 16 + kstep) * 4 + ct); ci[ct] = 96u + (unsigned)(nb * 4 + ct); }
    if (mi == 1) csid = 1;
  } else if (bid < 1216) {
    const int r = bid - 768; kstep = r & 15; const int nb = r >> 4; src = p.w3; ld = 1792; n0 = nb * 64; ra = 32 * kstep; rb = ra + 16;
#pragma unroll
    for (int ct = 0; ct < 4; ++ct) { const int F = nb * 4 + ct; fr[ct] = W5_F + (unsigned)(((F / 7) * 16 + kstep) * 7 + F % 7); ci[ct] = 128u + (unsigned)F; }
    csid = 2;
  } else if (bid < 1664) {
    const int r = bid - 1216; const int nb = r & 7; kstep = r >> 3; src = p.w4; ld = 512; n0 = nb * 64;
    const int u = kstep / 7, q = kstep % 7;
    if (q < 3) { ra = 224 * u + 32 * q; rb = ra + 16; }
    else if (q == 3) { ra = 224 * u + 96; rb = 224 * u + 208; }
    else { ra = 224 * u + 112 + 32 * (q - 4); rb = ra + 16; }
#pragma unroll
    for (int ct = 0; ct < 4; ++ct) fr[ct] = W6_F + (unsigned)((nb * 56 + kstep) * 4 + ct);
  }
  if (bid < 1664) {
    const int ip = x.lane >> 5, uu = (x.lane >> 4) & 1, q = (x.lane >> 2) & 3, c1 = ((x.lane >> 1) & 1) ^ ip, bb = x.lane & 1;
    const int rl = 4 * ip + q, ch = 2 * (2 * uu + c1) + bb;
    const unsigned stg0 = x.lds0 + (unsigned)x.w * 4096u;
#pragma unroll
    for (int u = 0; u < 4; ++u) {
      const int row = (u < 2 ? ra + 8 * u : rb + 8 * (u - 2)) + rl;
      const GAS unsigned char *gp = (const GAS unsigned char *)src + ((size_t)row * ld + n0 + ch * 8) * 2;
      asm volatile("s_mov_b32 m0, %0\n\ts_nop 0\n\tglobal_load_lds_dwordx4 %1, off" ::"s"(stg0 + (unsigned)u * 1024u), "v"(gp) : "memory");
    }
  }
  auto retile = [&]() {
    asm volatile("s_waitcnt vmcnt(0)" ::: "memory");
    const int gq = (x.lane & 15) >> 2, gp_ = x.lane & 3, gi = x.g & 1;
#pragma unroll
    for (int ct = 0; ct < 4; ++ct) {
      const unsigned L = (unsigned)((gi << 5) | ((ct >> 1) << 4) | (gq << 2) | (((ct & 1) ^ gi) << 1) | (gp_ >> 1));
      const unsigned a0 = (unsigned)x.w * 4096u + (unsigned)(x.g >> 1) * 1024u + L * 16u + (unsigned)(gp_ & 1) * 8u;
      const v2u lo = trread(x, a0), hi = trread(x, a0 + 2048u);
      const v4u fragv = {lo.x, lo.y, hi.x, hi.y};
      stg(x.rs, x.lane16, __builtin_amdgcn_readfirstlane(OFF_WT + fr[ct] * KB), fragv);
      if (csid >= 0) {
        const f4 cs = mfma32(fragv, v4u{0x3F803F80u, 0x3F803F80u, 0x3F803F80u, 0x3F803F80u}, f4{0.f, 0.f, 0.f, 0.f});
        const unsigned co = __builtin_amdgcn_readfirstlane(OFF_CSP + (ci[ct] * 16u + (unsigned)kstep) * 64u);
        if (x.tok == 0) stg(x.rs, (unsigned)x.g * 16u, co, __builtin_bit_cast(v4u, cs));
      }
    }
  };
  if (bid < 384) retile();
  FSA(1);
  v4u xres[2];
  v2u wph[2];
  if (cw) {
    xres[0] = clamp4(v4u{xin[0].x, xin[0].y, xin[1].x, xin[1].y});
    xres[1] = clamp4(v4u{xin[2].x, xin[2].y, xin[3].x, xin[3].y});
    const unsigned fo = OFF_L0 + O_XB + (unsigned)(x.T * 16 + 2 * x.c) * KB;
    stg(x.rs, x.lane16, fo, xres[0]); stg(x.rs, x.lane16, fo + KB, xres[1]);
    put_stats(x, xres[0], xres[1], OFF_L0 + O_XS);
#pragma unroll
    for (int h = 0; h < 2; ++h)
      wph[h] = v2u{(unsigned)wraw[h][0] | ((unsigned)wraw[h][1] << 16), (unsigned)wraw[h][2] | ((unsigned)wraw[h][3] << 16)};
  } else {
    xres[0] = xres[1] = v4u{0u, 0u, 0u, 0u};
    wph[0] = wph[1] = v2u{0u, 0u};
  }
  FSA(2);
  asm volatile("s_waitcnt vmcnt(0) lgkmcnt(0)\n\ts_barrier" ::: "memory");
  FSA(3);
  if (threadIdx.x == 0) __hip_atomic_store(x.ctl + OFF_FLAGS / 4 + b * 16, tag, RLX_AGENT);
  if (bid >= 384 && bid < 1664) retile();
  if (!cw) {
    poi4(x.rs, x.lane16, OFF_L0 + O_KVP + (unsigned)((x.pp * 16 + x.tau) * 4 + x.m) * KB);
    if (x.tok == 0) poi4(x.rs, (unsigned)x.g * 16u, OFF_L0 + O_KVZ + (unsigned)((x.pp * 4 + x.m) * 16 + x.tau) * 64u);
    poi4(x.rs, x.lane16, OFF_L0 + O_OB + (unsigned)(x.TA * 16 + x.pp) * KB);
    poi4(x.rs, x.lane16, OFF_L0 + O_OB + (unsigned)((x.TA + 1) * 16 + x.pp) * KB);
    poi4(x.rs, x.lane16, OFF_L0 + O_HB + (unsigned)(x.T * 16 + 2 * x.c) * KB);
    poi4(x.rs, x.lane16, OFF_L0 + O_HB + (unsigned)(x.T * 16 + 2 * x.c + 1) * KB);
    poi4(x.rs, x.lane16, OFF_L0 + L_STRIDE + O_KVP + (unsigned)((x.pp * 16 + x.tau) * 4 + x.m) * KB);
    if (x.tok == 0) poi4(x.rs, (unsigned)x.g * 16u, OFF_L0 + L_STRIDE + O_KVZ + (unsigned)((x.pp * 4 + x.m) * 16 + x.tau) * 64u);
  }
  if (x.w == 4) {
    poll_flags(x, OFF_FLAGS, tag);
    if (b == 0 && x.lane == 0) __hip_atomic_store(x.ctl + OFF_EPOCH / 4, tag, RLX_AGENT);
  }
  BAR();
  FSA(4);
  if (cw) {
    compute_path(x, p, xres, wph, tag);
  } else {
    v4u cv[2];
#pragma unroll
    for (int jj = 0; jj < 2; ++jj) LDA(cv[jj], x.lane16, OFF_CSP + (unsigned)(x.pp * 6 + min(x.m + 4 * jj, 5)) * KB);
    dmaR<POS1, POS2>(x);
#pragma unroll
    for (int jj = 0; jj < 2; ++jj) {
      WAITV1(24, cv[jj]);
      const f4 s = shsum(__builtin_bit_cast(f4, cv[jj]));
      const int u = x.m + 4 * jj;
      if (u < 6 && x.lane < 4) *(LAS f4 *)(x.sm + M_CS + (unsigned)u * 64u + (unsigned)x.lane * 16u) = s;
    }
    helper_path(x, tag);
  }
  asm volatile("s_waitcnt vmcnt(0)" ::: "memory");
}

extern "C" void kernel_launch(void *const *d_in, const int *in_sizes, int n_in, void *d_out, int out_size, void *d_ws, size_t ws_size,
                              hipStream_t stream) {
  Params p;
  p.x = (const unsigned short *)d_in[0];
  p.wqkv = (const unsigned short *)d_in[1];
  p.wphi = (const unsigned short *)d_in[2];
  p.wo = (const unsigned short *)d_in[3];
  p.w1 = (const unsigned short *)d_in[5];
  p.w2 = (const unsigned short *)d_in[6];
  p.w3 = (const unsigned short *)d_in[7];
  p.w4 = (const unsigned short *)d_in[8];
  p.out = (unsigned short *)d_out;
  p.ws = (unsigned char *)d_ws;
  (void)hipFuncSetAttribute((const void *)mega_kernel, hipFuncAttributeMaxDynamicSharedMemorySize, LDS_BYTES);
  hipLaunchKernelGGL(mega_kernel, dim3(256), dim3(512), LDS_BYTES, stream, p);
}
```

```cpp
#include <hip/hip_runtime.h>
#include <stdint.h>

#define GAS __attribute__((address_space(1)))
#define LAS __attribute__((address_space(3)))
#define DI __device__ __forceinline__
typedef unsigned v4u __attribute__((ext_vector_type(4)));
typedef unsigned v2u __attribute__((ext_vector_type(2)));
typedef float f4 __attribute__((ext_vector_type(4)));
typedef float f2 __attribute__((ext_vector_type(2)));
typedef short s8 __attribute__((ext_vector_type(8)));
typedef short s4 __attribute__((ext_vector_type(4)));
typedef __bf16 bf2 __attribute__((ext_vector_type(2)));
typedef GAS unsigned gu32;
typedef __amdgpu_buffer_rsrc_t rsrc_t;
#define RLX_AGENT __ATOMIC_RELAXED, __HIP_MEMORY_SCOPE_AGENT

#define DBG_GBAR 0
#define DBG_NOPOISON 0
#define DBG_STAMP 0
#define SPEC 0
#define PREFETCH 1
#define KV16 1
#define WARM 0

namespace {
constexpr unsigned MB = 1u << 20, KB = 1024u;
constexpr unsigned OFF_EPOCH = 0, OFF_ABORT = 256, OFF_GBAR = 512, OFF_FLAGS = 16384, OFF_FLAGS2 = 32768;
constexpr unsigned OFF_WT = 1 * MB;
constexpr unsigned W1_F = 0, W2_F = 1536, W5_F = 3072, W6_F = 4864;
constexpr unsigned OFF_CSP = 8 * MB;
constexpr unsigned OFF_L0 = 9 * MB, L_STRIDE = 24 * MB;
constexpr unsigned O_XB = 0, O_XS = 2 * MB, O_KVP = 2 * MB + 512 * KB, O_KVZ = 3 * MB + 512 * KB, O_OB = 4 * MB, O_X1B = 6 * MB,
                   O_X1S = 8 * MB, O_HB = 9 * MB, O_X2B = 11 * MB, O_X2S = 13 * MB, O_H3B = 14 * MB;
constexpr unsigned WS_BYTES = OFF_L0 + 2 * L_STRIDE;
constexpr unsigned OOB = 0x7F000000u;
constexpr int RING = 156;
constexpr unsigned MISC = RING * KB;
constexpr unsigned M_CS = MISC, M_KVA = MISC + 1088, M_ZA = MISC + 3136, M_SIG = MISC + 3264, M_SIG2 = MISC + 3280, M_RDY = MISC + 3296, M_FLAG = MISC + 3300;
constexpr unsigned XSTG = 64 * KB;
constexpr unsigned IMG = 0;
constexpr unsigned LDS_BYTES = 163840;
constexpr int POS1 = 0, POS2 = 96, POS3 = 160, POS4 = 224, POS5 = 288, POS6 = 400, POSE = 624;
constexpr unsigned SENT = 0xFFFFFFFFu;
constexpr unsigned SPIN_CAP = 1u << 13;
constexpr int AUX_SC1 = 16;
#define DLY_KV 14
#define DLY_1 19
#define DLY_2 19
#define DLY_3 19
#define DLY_4 19
#define DLY_5 19
#define DLY_6 19

struct Params {
  const unsigned short *x, *wqkv, *wphi, *wo, *w1, *w2, *w3, *w4;
  unsigned short *out;
  unsigned char *ws;
};

DI unsigned pk2(float a, float b) { f2 v = {a, b}; bf2 r = __builtin_convertvector(v, bf2); return __builtin_bit_cast(unsigned, r); }
DI float asf(unsigned u) { return __builtin_bit_cast(float, u); }
DI float blo(unsigned u) { return __builtin_bit_cast(float, u << 16); }
DI float bhi(unsigned u) { return __builtin_bit_cast(float, u & 0xFFFF0000u); }
DI unsigned clampw(unsigned u) { return u < 0xFFFFFFFEu ? u : 0xFFFFFFFEu; }
DI unsigned clampf(float f) { return clampw(__builtin_bit_cast(unsigned, f)); }
DI v4u clamp4(v4u v) { return v4u{clampw(v.x), clampw(v.y), clampw(v.z), clampw(v.w)}; }
DI v4u pack8(f4 a, f4 b) { return clamp4(v4u{pk2(a.x, a.y), pk2(a.z, a.w), pk2(b.x, b.y), pk2(b.z, b.w)}); }
DI f4 ulo(v4u v) { return f4{blo(v.x), bhi(v.x), blo(v.y), bhi(v.y)}; }
DI f4 uhi(v4u v) { return f4{blo(v.z), bhi(v.z), blo(v.w), bhi(v.w)}; }
DI f4 mfma32(v4u a, v4u b, f4 c) {
  return __builtin_amdgcn_mfma_f32_16x16x32_bf16(__builtin_bit_cast(s8, a), __builtin_bit_cast(s8, b), c, 0, 0, 0);
}
DI f4 mfma16(v2u a, v2u b, f4 c) {
  return __builtin_amdgcn_mfma_f32_16x16x16bf16_1k(__builtin_bit_cast(s4, a), __builtin_bit_cast(s4, b), c, 0, 0, 0);
}
DI v4u ldg(rsrc_t rs, unsigned voff, unsigned soff) { return __builtin_amdgcn_raw_buffer_load_b128(rs, voff, soff, AUX_SC1); }
DI void stg(rsrc_t rs, unsigned voff, unsigned soff, v4u v) {
  __builtin_amdgcn_raw_buffer_store_b128(v, rs, voff, soff, AUX_SC1);
  asm volatile("s_nop 1" ::"v"(v));
}
DI void stg2(rsrc_t rs, unsigned voff, unsigned soff, v2u v) { __builtin_amdgcn_raw_buffer_store_b64(v, rs, voff, soff, AUX_SC1); }
DI void poi4(rsrc_t rs, unsigned voff, unsigned soff) { if (!DBG_NOPOISON) stg(rs, voff, soff, v4u{0xFFFFFFFFu, 0xFFFFFFFFu, 0xFFFFFFFFu, 0xFFFFFFFFu}); }
DI void poi2(rsrc_t rs, unsigned voff, unsigned soff) { if (!DBG_NOPOISON) stg2(rs, voff, soff, v2u{0xFFFFFFFFu, 0xFFFFFFFFu}); }
DI unsigned max4(v4u v) { return max(max(v.x, v.y), max(v.z, v.w)); }
DI bool anybad(unsigned mx) { return __any(mx == SENT) != 0; }
DI float xsum(float x) {
  unsigned u = __builtin_bit_cast(unsigned, x);
  auto r = __builtin_amdgcn_permlane16_swap(u, u, false, false);
  x = __builtin_bit_cast(float, (unsigned)r[0]) + __builtin_bit_cast(float, (unsigned)r[1]);
  u = __builtin_bit_cast(unsigned, x);
  auto q = __builtin_amdgcn_permlane32_swap(u, u, false, false);
  return __builtin_bit_cast(float, (unsigned)q[0]) + __builtin_bit_cast(float, (unsigned)q[1]);
}
DI float xmax(float x) {
  unsigned u = __builtin_bit_cast(unsigned, x);
  auto r = __builtin_amdgcn_permlane16_swap(u, u, false, false);
  x = fmaxf(__builtin_bit_cast(float, (unsigned)r[0]), __builtin_bit_cast(float, (unsigned)r[1]));
  u = __builtin_bit_cast(unsigned, x);
  auto q = __builtin_amdgcn_permlane32_swap(u, u, false, false);
  return fmaxf(__builtin_bit_cast(float, (unsigned)q[0]), __builtin_bit_cast(float, (unsigned)q[1]));
}
template <bool MAX> DI float red_op(float a, float b) { return MAX ? fmaxf(a, b) : a + b; }
DI unsigned fbits(float f) { return __builtin_bit_cast(unsigned, f); }
template <bool MAX> DI void xred4(float &a, float &b, float &c, float &d) {
  const auto r1 = __builtin_amdgcn_permlane16_swap(fbits(a), fbits(b), false, false);
  const auto r2 = __builtin_amdgcn_permlane16_swap(fbits(c), fbits(d), false, false);
  const float c1 = red_op<MAX>(asf(r1[0]), asf(r1[1])), c2 = red_op<MAX>(asf(r2[0]), asf(r2[1]));
  const auto r3 = __builtin_amdgcn_permlane32_swap(fbits(c1), fbits(c2), false, false);
  const unsigned e = fbits(red_op<MAX>(asf(r3[0]), asf(r3[1])));
  const auto r4 = __builtin_amdgcn_permlane32_swap(e, e, false, false);
  const unsigned f0 = r4[0], f1 = r4[1];
  const auto r5 = __builtin_amdgcn_permlane16_swap(f0, f0, false, false);
  const auto r6 = __builtin_amdgcn_permlane16_swap(f1, f1, false, false);
  a = asf(r5[0]); b = asf(r5[1]); c = asf(r6[0]); d = asf(r6[1]);
}
template <bool MAX> DI void xred2(float &a, float &b) {
  const auto r1 = __builtin_amdgcn_permlane16_swap(fbits(a), fbits(b), false, false);
  const unsigned c = fbits(red_op<MAX>(asf(r1[0]), asf(r1[1])));
  const auto r3 = __builtin_amdgcn_permlane32_swap(c, c, false, false);
  const unsigned e = fbits(red_op<MAX>(asf(r3[0]), asf(r3[1])));
  const auto r5 = __builtin_amdgcn_permlane16_swap(e, e, false, false);
  a = asf(r5[0]); b = asf(r5[1]);
}
DI f2 gelu2(f2 x) {
  const f2 c1 = {-2.302208198f, -2.302208198f}, c2 = {-0.1029432397f, -0.1029432397f}, one = {1.0f, 1.0f};
  const f2 t = x * __builtin_elementwise_fma(x * x, c2, c1);
  const f2 d = one + f2{__builtin_amdgcn_exp2f(t.x), __builtin_amdgcn_exp2f(t.y)};
  return x * f2{__builtin_amdgcn_rcpf(d.x), __builtin_amdgcn_rcpf(d.y)};
}
DI f4 gelu4(f4 y) {
  const f2 a = gelu2(f2{y.x, y.y}), b = gelu2(f2{y.z, y.w});
  return f4{a.x, a.y, b.x, b.y};
}

#define FSL 1
#define FS(i) do {} while (0)
#define FSA(i) do { unsigned long long t_; asm volatile("s_memtime %0\n\ts_waitcnt lgkmcnt(0)" : "=s"(t_) :: "memory"); \
  int ww_ = x.w; asm volatile("" : "+s"(ww_)); \
  unsigned a_ = (ww_ == 0) ? (70 + (i)) * 4u : ((ww_ == 4) ? 400u + (70 + (i)) * 4u : 780u); \
  asm volatile("" : "+s"(a_)); \
  *(LAS unsigned *)(x.sm + M_FLAG + a_) = (unsigned)t_; } while (0)
#define FSP(i) do {} while (0)
#define SB() __builtin_amdgcn_sched_barrier(0)
#define MARK(s) asm volatile("; MARK " s)
#define BAR() asm volatile("s_waitcnt lgkmcnt(0)\n\ts_barrier" ::: "memory")
#define WAITV_BAR(n) asm volatile("s_waitcnt vmcnt(" #n ") lgkmcnt(0)\n\ts_barrier" ::: "memory")
#define DRAIN_BAR() asm volatile("s_waitcnt vmcnt(0) lgkmcnt(0)\n\ts_barrier" ::: "memory")
DI void sleepn(int n) {
  for (int i = 0; i < n; ++i) __builtin_amdgcn_s_sleep(1);
}

struct Ctx {
  rsrc_t rs;
  LAS unsigned char *sm;
  unsigned lds0;
  unsigned lane16;
  LAS unsigned char *ab[3];
  int lane, w, m, g, tok;
  int t, c, tau, pp, b;
  int T, TA;
  unsigned spins;
  gu32 *ctl;
};
DI void refresh(Ctx &x) {
  asm volatile("" : "+s"(x.w), "+s"(x.b), "+v"(x.lane));
  x.lane16 = (unsigned)x.lane * 16u;
  x.g = x.lane >> 4; x.tok = x.lane & 15;
  x.m = x.w & 3;
  const int xc = x.b & 7, j = x.b >> 3;
  x.t = 4 * xc + (j >> 3); x.c = j & 7;
  x.tau = 2 * xc + (j >> 4); x.pp = j & 15;
  x.T = 4 * x.t + x.m;
  x.TA = 8 * x.tau + 2 * x.m;
  x.ab[0] = x.sm + x.lane16;
  unsigned o1 = x.lane16 + 60u * KB, o2 = x.lane16 + 120u * KB;
  asm volatile("" : "+v"(o1), "+v"(o2));
  x.ab[1] = x.sm + o1; x.ab[2] = x.sm + o2;
}

template <int LO, int HI, int THR> DI void dmaSeg(const Ctx &x, bool live) {
  constexpr int Q = LO % POSE;
  int w = x.w, b = x.b;
  asm volatile("" : "+s"(w), "+s"(b));
  const int m = w & 3, j = b >> 3;
  unsigned fr;
  if constexpr (Q < POS2) fr = W1_F + (unsigned)((j & 15) * 96 + Q + m);
  else if constexpr (Q < POS5) fr = W2_F + (unsigned)(((Q - POS2) / 64) * 512 + (j & 7) * 64 + (Q - POS2) % 64 + m);
  else if constexpr (Q < POS6) fr = W5_F + (unsigned)((j & 15) * 112 + (Q - POS5) + m);
  else fr = W6_F + (unsigned)((j & 7) * 224 + (Q - POS6) + m);
  unsigned so = live ? OFF_WT + fr * KB : OOB;
  unsigned lds = x.lds0 + (unsigned)(LO % RING + m) * KB;
  const unsigned wrap = x.lds0 + (unsigned)(RING + m) * KB;
#pragma unroll 1
  for (int i = 0; i < (HI - LO) / 4; ++i) {
    asm volatile("s_mov_b32 m0, %0\n\ts_nop 0\n\tbuffer_load_dwordx4 %1, %2, %3 offen sc1 lds" ::"s"(lds), "v"(x.lane16), "s"(x.rs), "s"(so) : "memory");
    if constexpr (THR == 3) asm volatile("s_waitcnt vmcnt(3)" ::: "memory");
    if constexpr (THR == 5) asm volatile("s_waitcnt vmcnt(5)" ::: "memory");
    so += 4u * KB;
    lds += 4u * KB;
    lds = lds >= wrap ? lds - RING * KB : lds;
  }
}
constexpr int mat_end(int p) {
  const int base = p - p % POSE, q = p % POSE;
  return base + (q < POS2 ? POS2 : q < POS3 ? POS3 : q < POS4 ? POS4 : q < POS5 ? POS5 : q < POS6 ? POS6 : POSE);
}
template <int LO, int HI, int THR = -1> DI void dmaR(const Ctx &x, bool live = true) {
  if constexpr (LO < HI) {
    constexpr int E = mat_end(LO) < HI ? mat_end(LO) : HI;
    dmaSeg<LO, E, THR>(x, live);
    dmaR<E, HI, THR>(x, live);
  }
}
template <int LO, int HI> DI void dmaRT(const Ctx &x, bool live = true) { dmaR<LO, HI, 3>(x, live); }
template <int LO, int HI> DI void dmaR6(const Ctx &x, bool live = true) { dmaR<LO, HI, 5>(x, live); }
DI void sig_set(const Ctx &x, unsigned v, unsigned word = M_SIG) { *(volatile LAS unsigned *)(x.sm + word + (unsigned)x.m * 4u) = v; }
DI void sig_wait(const Ctx &x, unsigned v, unsigned word = M_SIG) {
  for (unsigned sp = 0; sp < (1u << 20); ++sp) {
    const v4u s = *(volatile LAS v4u *)(x.sm + word);
    if (__builtin_amdgcn_readfirstlane(min(min(s.x, s.y), min(s.z, s.w))) >= v) break;
    __builtin_amdgcn_s_sleep(1);
  }
}

#define LDA(dst, voff, soff) \
  asm volatile("buffer_load_dwordx4 %0, %1, %2, %3 offen sc1" : "=v"(dst) : "v"(voff), "s"(x.rs), "s"(soff) : "memory")
#define LDA2(dst, voff, soff, imm) \
  asm volatile("buffer_load_dwordx2 %0, %1, %2, %3 offen offset:%4 sc1" : "=v"(dst) : "v"(voff), "s"(x.rs), "s"(soff), "i"(imm) : "memory")
#define WAITV1(n, r) asm volatile("s_waitcnt vmcnt(%1)" : "+v"(r) : "i"(n) : "memory")
#define WAITV2(n, r, q) asm volatile("s_waitcnt vmcnt(%2)" : "+v"(r), "+v"(q) : "i"(n) : "memory")
#define TIE(r) asm volatile("" : "+v"(r) : : "memory")
DI bool giveup(Ctx &x) {
  if (++x.spins > SPIN_CAP) {
    if (x.lane == 0) __hip_atomic_store(x.ctl + OFF_ABORT / 4, 1u, RLX_AGENT);
    return true;
  }
  __builtin_amdgcn_s_sleep(1);
  return false;
}
DI v4u load_sure(Ctx &x, unsigned voff, unsigned soff) {
  v4u v;
  while (true) {
    asm volatile("buffer_load_dwordx4 %0, %1, %2, %3 offen sc1\n\ts_waitcnt vmcnt(0)" : "=&v"(v) : "v"(voff), "s"(x.rs), "s"(soff) : "memory");
    if (!anybad(max4(v)) || giveup(x)) break;
  }
  return v;
}
DI v2u load_sure2(Ctx &x, unsigned voff, unsigned soff) {
  v2u v;
  while (true) {
    asm volatile("buffer_load_dwordx2 %0, %1, %2, %3 offen sc1\n\ts_waitcnt vmcnt(0)" : "=&v"(v) : "v"(voff), "s"(x.rs), "s"(soff) : "memory");
    if (!anybad(max(v.x, v.y)) || giveup(x)) break;
  }
  return v;
}

DI void warm(Ctx &x, unsigned voff, unsigned soff, unsigned soff2) {
}
#define LDC(dst, voff, soff) \
  asm volatile("buffer_load_dword %0, %1, %2, %3 offen sc1" : "=v"(dst) : "v"(voff), "s"(x.rs), "s"(soff) : "memory")

#define GBAR() do {} while (0)
#define SLEEP(n) sleepn(n)

DI void put_stats(const Ctx &x, v4u u, v4u v, unsigned sbuf) {
  const f4 a = ulo(u), b = uhi(u), c = ulo(v), d = uhi(v);
  const f4 s = (a + b) + (c + d), q = (a * a + b * b) + (c * c + d * d);
  float s1 = (s.x + s.y) + (s.z + s.w), s2 = (q.x + q.y) + (q.z + q.w);
  xred2<false>(s1, s2);
  if (x.g == 0) stg2(x.rs, (unsigned)x.tok * 8u, sbuf + (unsigned)(x.T * 8 + x.c) * 128u, v2u{clampf(s1), clampf(s2)});
}
struct Stats { v2u v[2]; };
DI void stats_load(const Ctx &x, Stats &s, unsigned soffT) {
  const unsigned vo = (unsigned)(x.g * 256 + x.tok * 8);
  LDA2(s.v[0], vo, soffT, 0); LDA2(s.v[1], vo, soffT, 128);
}
DI void stats_fin(Ctx &x, Stats &s, unsigned soffT, float &mean, float &var) {
  TIE(s.v[0]); TIE(s.v[1]);
  if (__builtin_expect(anybad(max(max(s.v[0].x, s.v[0].y), max(s.v[1].x, s.v[1].y))), 0)) {
    s.v[0] = load_sure2(x, (unsigned)(x.g * 256 + x.tok * 8), soffT);
    s.v[1] = load_sure2(x, (unsigned)(x.g * 256 + x.tok * 8 + 128), soffT);
  }
  float s1 = asf(s.v[0].x) + asf(s.v[1].x), s2 = asf(s.v[0].y) + asf(s.v[1].y);
  xred2<false>(s1, s2);
  mean = s1 * (1.0f / 512.0f);
  var = fmaxf(__builtin_fmaf(-mean, mean, s2 * (1.0f / 512.0f)), 0.0f);
}
DI const LAS v4u *lds4(const Ctx &x, unsigned off) { return (const LAS v4u *)(x.sm + off); }
template <int NT, int P> DI void ldaN(const Ctx &x, v4u (&a)[NT]) {
#pragma unroll
  for (int f = 0; f < NT; ++f) {
    const int slot = (P + f) % RING;
    a[f] = *(const LAS v4u *)(x.ab[slot / 60] + (slot % 60) * KB);
  }
}
template <int NT> DI void tieN(v4u (&a)[NT]) {
  if constexpr (NT == 4) asm volatile("" : "+v"(a[0]), "+v"(a[1]), "+v"(a[2]), "+v"(a[3]));
  else if constexpr (NT == 6) asm volatile("" : "+v"(a[0]), "+v"(a[1]), "+v"(a[2]), "+v"(a[3]), "+v"(a[4]), "+v"(a[5]));
  else asm volatile("" : "+v"(a[0]), "+v"(a[1]), "+v"(a[2]), "+v"(a[3]), "+v"(a[4]), "+v"(a[5]), "+v"(a[6]));
}
DI unsigned mx4(unsigned m, v4u v) {
  unsigned r;
  asm("v_max3_u32 %0, %1, %2, %3" : "=v"(r) : "v"(m), "v"(v.x), "v"(v.y));
  asm("v_max3_u32 %0, %1, %2, %3" : "=v"(m) : "v"(r), "v"(v.z), "v"(v.w));
  return m;
}
DI f4 ldcs(const Ctx &x, int tile) { return *(const LAS f4 *)(x.sm + M_CS + (unsigned)tile * 64u + (unsigned)x.g * 16u); }
constexpr int imin(int a, int b) { return a < b ? a : b; }

template <bool SH> DI unsigned vof(const Ctx &x, int k, const unsigned (&vo)[4]) { return SH ? vo[k >> 2] : x.lane16; }
template <int NK> constexpr int depth1() { return 8; }
template <int POS, int NK, bool SH, int K0, int K1, int I> struct KL1 {
  static DI void run(Ctx &x, v4u (&bf)[NK], f4 (&acc)[4], unsigned &mx, v4u (&cur)[4], v4u (&nxt)[4], unsigned so, const unsigned (&vo)[4],
                     unsigned sig) {
    v4u nn[4];
    constexpr int DP = depth1<NK>();
    WAITV1(imin(DP - 1, NK - 1 - I), bf[I]);
    tieN<4>(cur);
    SB();
    acc[0] = mfma32(cur[0], bf[I], acc[0]);
    SB();
    if constexpr (I + DP < NK) LDA(bf[I + DP], vof<SH>(x, I + DP, vo), so + (unsigned)(I + DP) * KB);
    if constexpr (I + DP == NK - 1) { if (sig) sig_set(x, sig); }
    SB();
    if constexpr (I + 2 < K1) ldaN<4, POS + 4 * (I + 2)>(x, nn);
    mx = mx4(mx, bf[I]);
#pragma unroll
    for (int f = 1; f < 4; ++f) acc[f] = mfma32(cur[f], bf[I], acc[f]);
    __builtin_amdgcn_sched_group_barrier(0x100, 1, 0);
#pragma unroll
    for (int f = 1; f < 4; ++f) {
      __builtin_amdgcn_sched_group_barrier(0x008, 1, 0);
      __builtin_amdgcn_sched_group_barrier(0x100, 1, 0);
      __builtin_amdgcn_sched_group_barrier(0x002, 1, 0);
    }
    SB();
    if constexpr (I + 1 < K1) KL1<POS, NK, SH, K0, K1, I + 1>::run(x, bf, acc, mx, nxt, nn, so, vo, sig);
  }
};
template <int POS, bool SH, int K1, int I> struct KL1R {
  static DI void run(Ctx &x, f4 (&acc)[4], unsigned so, const unsigned (&vo)[4]) {
    const v4u bv = load_sure(x, vof<SH>(x, I, vo), so + (unsigned)I * KB);
    v4u a[4];
    ldaN<4, POS + 4 * I>(x, a);
#pragma unroll
    for (int f = 0; f < 4; ++f) acc[f] = mfma32(a[f], bv, acc[f]);
    if constexpr (I + 1 < K1) KL1R<POS, SH, K1, I + 1>::run(x, acc, so, vo);
  }
};
template <bool ST> DI unsigned canary_poll1(Ctx &x, unsigned so, unsigned cvo, Stats &st, unsigned sst) {
  unsigned cn;
  LDC(cn, cvo, so);
  if constexpr (ST) stats_load(x, st, sst);
  WAITV1(0, cn);
  if constexpr (ST) { TIE(st.v[0]); TIE(st.v[1]); cn = max(cn, max(max(st.v[0].x, st.v[0].y), max(st.v[1].x, st.v[1].y))); }
  return cn;
}
template <int NK, bool SH, bool ST>
DI void pro1(Ctx &x, v4u (&bf)[NK], unsigned so, const unsigned (&vo)[4], unsigned cvo, Stats &st, unsigned sst, unsigned &cn) {
  while (anybad(canary_poll1<ST>(x, so, cvo, st, sst)) && !giveup(x)) {}
  cn = 0;
#pragma unroll
  for (int i = 0; i < depth1<NK>(); ++i) LDA(bf[i], vof<SH>(x, i, vo), so + (unsigned)i * KB);
}
template <int NK, bool SH, bool ST>
DI void chk1(Ctx &x, v4u (&bf)[NK], unsigned so, const unsigned (&vo)[4], unsigned cvo, Stats &st, unsigned sst, unsigned cn) {
}
template <int POS, int NK, bool SH, int K0, int K1>
DI void kloop1(Ctx &x, v4u (&bf)[NK], f4 (&acc)[4], unsigned so, const unsigned (&vo)[4], unsigned sig) {
  f4 save[4];
#pragma unroll
  for (int f = 0; f < 4; ++f) save[f] = acc[f];
  unsigned mx = 0;
  v4u a0[4], a1[4];
  ldaN<4, POS + 4 * K0>(x, a0);
  ldaN<4, POS + 4 * (K0 + 1)>(x, a1);
  KL1<POS, NK, SH, K0, K1, K0>::run(x, bf, acc, mx, a0, a1, so, vo, sig);
  if (__builtin_expect(anybad(mx), 0)) {
#pragma unroll
    for (int f = 0; f < 4; ++f) acc[f] = save[f];
    KL1R<POS, SH, K1, K0>::run(x, acc, so, vo);
  }
}
template <int NT, int POS, int K0, int K1, int I> struct KL2 {
  static DI void run(Ctx &x, v4u (&bf)[16][2], f4 (&acc)[NT][2], unsigned &mx, v4u (&cur)[NT], unsigned so, unsigned sig) {
    v4u nxt[NT];
    WAITV2(2 * imin(3, 15 - I), bf[I][0], bf[I][1]);
    tieN<NT>(cur);
    SB();
    acc[0][0] = mfma32(cur[0], bf[I][0], acc[0][0]);
    acc[0][1] = mfma32(cur[0], bf[I][1], acc[0][1]);
    SB();
    if constexpr (I + 4 < 16) LDA(bf[I + 4][0], x.lane16, so + (unsigned)(I + 4) * KB);
    SB();
    acc[1][0] = mfma32(cur[1], bf[I][0], acc[1][0]);
    acc[1][1] = mfma32(cur[1], bf[I][1], acc[1][1]);
    SB();
    if constexpr (I + 4 < 16) LDA(bf[I + 4][1], x.lane16, so + (unsigned)(16 + I + 4) * KB);
    if constexpr (I == (NT == 6 ? 6 : 11)) sig_set(x, sig);
    SB();
    if constexpr (I + 1 < K1) ldaN<NT, POS + (I + 1) * NT>(x, nxt);
    mx = mx4(mx4(mx, bf[I][0]), bf[I][1]);
#pragma unroll
    for (int f = 2; f < NT; ++f) {
      acc[f][0] = mfma32(cur[f], bf[I][0], acc[f][0]);
      acc[f][1] = mfma32(cur[f], bf[I][1], acc[f][1]);
    }
#pragma unroll
    for (int f = 0; f < NT; ++f) {
      __builtin_amdgcn_sched_group_barrier(0x100, 1, 0);
      __builtin_amdgcn_sched_group_barrier(0x008, 1, 0);
    }
#pragma unroll
    for (int f = 0; f < NT - 4; ++f) {
      __builtin_amdgcn_sched_group_barrier(0x008, 1, 0);
      __builtin_amdgcn_sched_group_barrier(0x002, 2, 0);
    }
    SB();
    if constexpr (I + 1 < K1) KL2<NT, POS, K0, K1, I + 1>::run(x, bf, acc, mx, nxt, so, sig);
  }
};
template <int NT, int POS, int K1, int I> struct KL2R {
  static DI void run(Ctx &x, f4 (&acc)[NT][2], unsigned so) {
    const v4u b0 = load_sure(x, x.lane16, so + (unsigned)I * KB), b1 = load_sure(x, x.lane16, so + (unsigned)(16 + I) * KB);
    v4u a[NT];
    ldaN<NT, POS + I * NT>(x, a);
#pragma unroll
    for (int f = 0; f < NT; ++f) {
      acc[f][0] = mfma32(a[f], b0, acc[f][0]);
      acc[f][1] = mfma32(a[f], b1, acc[f][1]);
    }
    if constexpr (I + 1 < K1) KL2R<NT, POS, K1, I + 1>::run(x, acc, so);
  }
};
DI unsigned stmax(const Stats (&st)[2]) {
  return max(max(max(st[0].v[0].x, st[0].v[0].y), max(st[0].v[1].x, st[0].v[1].y)), max(max(st[1].v[0].x, st[1].v[0].y), max(st[1].v[1].x, st[1].v[1].y)));
}
DI void poll2(Ctx &x, Stats (&st)[2], unsigned so, unsigned sst) {
  const unsigned cvo = (unsigned)(x.lane & 31) * KB + 1020u;
  while (true) {
    unsigned cn;
    LDC(cn, cvo, so);
    stats_load(x, st[0], sst); stats_load(x, st[1], sst + 1024u);
    WAITV1(0, cn);
    TIE(st[0].v[0]); TIE(st[0].v[1]); TIE(st[1].v[0]); TIE(st[1].v[1]);
    if (!anybad(max(cn, stmax(st))) || giveup(x)) break;
  }
}
DI void first2(Ctx &x, v4u (&bf)[16][2], unsigned so) {
#pragma unroll
  for (int i = 0; i < 4; ++i) { LDA(bf[i][0], x.lane16, so + (unsigned)i * KB); LDA(bf[i][1], x.lane16, so + (unsigned)(16 + i) * KB); }
}
DI void pro2(Ctx &x, v4u (&bf)[16][2], Stats (&st)[2], unsigned so, unsigned sst, unsigned &cn) {
  poll2(x, st, so, sst);
  cn = 0;
  first2(x, bf, so);
}
DI void chk2(Ctx &x, v4u (&bf)[16][2], Stats (&st)[2], unsigned so, unsigned sst, unsigned cn) {
}
template <int NT, int POS, int K0, int K1>
DI void kloop2(Ctx &x, v4u (&bf)[16][2], f4 (&acc)[NT][2], unsigned so, unsigned sig, unsigned &mx) {
  v4u a0[NT];
  ldaN<NT, POS + K0 * NT>(x, a0);
  KL2<NT, POS, K0, K1, K0>::run(x, bf, acc, mx, a0, so, sig);
}
template <int NT, int POS> DI void redo2(Ctx &x, f4 (&acc)[NT][2], unsigned so) {
#pragma unroll
  for (int f = 0; f < NT; ++f) { acc[f][0] = f4{0.f, 0.f, 0.f, 0.f}; acc[f][1] = f4{0.f, 0.f, 0.f, 0.f}; }
  KL2R<NT, POS, 16, 0>::run(x, acc, so);
}

DI void featmap4(const f4 (&v)[4], const v2u (&wphi)[2], v2u (&plus)[4], v2u (&minus)[4]) {
  const float L2E = 1.4426950408889634f;
  f4 p[4];
#pragma unroll
  for (int i = 0; i < 4; ++i) p[i] = mfma16(wphi[i & 1], v2u{pk2(v[i].x, v[i].y), pk2(v[i].z, v[i].w)}, f4{0.f, 0.f, 0.f, 0.f});
  float mx[4], nm[4];
#pragma unroll
  for (int i = 0; i < 4; ++i) {
    p[i] *= L2E;
    mx[i] = fmaxf(fmaxf(p[i].x, p[i].y), fmaxf(p[i].z, p[i].w));
    nm[i] = -fminf(fminf(p[i].x, p[i].y), fminf(p[i].z, p[i].w));
  }
  xred4<true>(mx[0], mx[1], mx[2], mx[3]);
  xred4<true>(nm[0], nm[1], nm[2], nm[3]);
  f4 e[4], n[4];
  float se[4], sn[4];
#pragma unroll
  for (int i = 0; i < 4; ++i) {
    const f4 a = p[i] - mx[i], b = -p[i] - nm[i];
    e[i] = f4{__builtin_amdgcn_exp2f(a.x), __builtin_amdgcn_exp2f(a.y), __builtin_amdgcn_exp2f(a.z), __builtin_amdgcn_exp2f(a.w)};
    n[i] = f4{__builtin_amdgcn_exp2f(b.x), __builtin_amdgcn_exp2f(b.y), __builtin_amdgcn_exp2f(b.z), __builtin_amdgcn_exp2f(b.w)};
    se[i] = (e[i].x + e[i].y) + (e[i].z + e[i].w);
    sn[i] = (n[i].x + n[i].y) + (n[i].z + n[i].w);
  }
  xred4<false>(se[0], se[1], se[2], se[3]);
  xred4<false>(sn[0], sn[1], sn[2], sn[3]);
#pragma unroll
  for (int i = 0; i < 4; ++i) {
    const f4 a = e[i] * __builtin_amdgcn_rcpf(se[i]), b = n[i] * __builtin_amdgcn_rcpf(sn[i]);
    plus[i] = v2u{pk2(a.x, a.y), pk2(a.z, a.w)};
    minus[i] = v2u{pk2(b.x, b.y), pk2(b.z, b.w)};
  }
}
DI v2u trread(const Ctx &x, unsigned off) {
  s4 r = __builtin_amdgcn_ds_read_tr16_b64_v4i16((LAS s4 *)(x.sm + off));
  return __builtin_bit_cast(v2u, r);
}
DI f4 shsum(f4 s) {
#pragma unroll
  for (int o = 4; o < 64; o <<= 1) {
    s.x += __shfl_xor(s.x, o); s.y += __shfl_xor(s.y, o); s.z += __shfl_xor(s.z, o); s.w += __shfl_xor(s.w, o);
  }
  return s;
}

DI void poll_flags(Ctx &x, unsigned off, unsigned tag) {
  for (unsigned sp = 0;; ++sp) {
    unsigned f0, f1, f2, f3;
    const unsigned vo = (unsigned)x.lane * 64u;
    LDC(f0, vo, off); LDC(f1, vo, off + 4096u); LDC(f2, vo, off + 8192u); LDC(f3, vo, off + 12288u);
    asm volatile("s_waitcnt vmcnt(0)" : "+v"(f0), "+v"(f1), "+v"(f2), "+v"(f3)::"memory");
    if (__all(f0 == tag && f1 == tag && f2 == tag && f3 == tag)) break;
    if (sp > SPIN_CAP) {
      if (x.lane == 0) __hip_atomic_store(x.ctl + OFF_ABORT / 4, 3u, RLX_AGENT);
      break;
    }
    __builtin_amdgcn_s_sleep(8);
  }
}
DI unsigned sigv(int L, int ph) { return (unsigned)(L * 8 + ph + 1); }

DI void compute_path(Ctx &x, const Params &p, v4u (&xres)[2], const v2u (&wph)[2], unsigned tag) {
  const unsigned vz[4] = {0u, 0u, 0u, 0u};
  v4u x1r[2], x2r[2];
#pragma unroll 1
  for (int L = 0; L < 2; ++L) {
    const unsigned lb0 = OFF_L0 + (unsigned)L * L_STRIDE;
    v4u phq[2][2];
    GBAR();
    {
      refresh(x);
      FS(0);
      unsigned lb = lb0;
      asm volatile("" : "+s"(lb));
      v4u bf[16][2];
      Stats st[2];
      const unsigned so = lb + O_XB + (unsigned)x.TA * 16u * KB, sst = lb + O_XS + (unsigned)x.TA * 1024u;
      unsigned cn;
      pro2(x, bf, st, so, sst, cn);
      FS(1); FSP(0);
      BAR();
      if (L == 0 && threadIdx.x == 0) __hip_atomic_store(x.ctl + OFF_FLAGS2 / 4 + x.b * 16, tag, RLX_AGENT);
      chk2(x, bf, st, so, sst, cn);
      FS(2);
      f4 acc[6][2];
#pragma unroll
      for (int f = 0; f < 6; ++f) { acc[f][0] = f4{0.f, 0.f, 0.f, 0.f}; acc[f][1] = f4{0.f, 0.f, 0.f, 0.f}; }
      unsigned mx = 0;
      kloop2<6, POS1, 0, 16>(x, bf, acc, so, sigv(L, 0), mx);
      if (__builtin_expect(anybad(mx), 0)) redo2<6, POS1>(x, acc, so);
      FS(3);
      BAR();
      MARK("P1epi");
      f4 val[2][6];
#pragma unroll
      for (int j = 0; j < 2; ++j) {
        float mean, var;
        stats_fin(x, st[j], sst + (unsigned)j * 1024u, mean, var);
        const float rstd = __builtin_amdgcn_rsqf(var + 1e-5f), mr = mean * rstd;
#pragma unroll
        for (int f = 0; f < 6; ++f) val[j][f] = acc[f][j] * rstd - ldcs(x, f) * mr;
      }
      {
        v2u pl[4], mi[4];
        const f4 kk[4] = {val[0][2], val[0][3], val[1][2], val[1][3]};
        featmap4(kk, wph, pl, mi);
#pragma unroll
        for (int i = 0; i < 4; ++i) {
          const int j = i >> 1, h = i & 1;
          const unsigned img = IMG + (unsigned)((2 * x.m + j) * 16 + x.tok) * 32u + (unsigned)x.g * 8u + (unsigned)(h * 3) * 4096u;
          *(LAS v2u *)(x.sm + img) = pl[i];
          *(LAS v2u *)(x.sm + img + 4096u) = mi[i];
          *(LAS v2u *)(x.sm + img + 8192u) = v2u{pk2(val[j][4 + h].x, val[j][4 + h].y), pk2(val[j][4 + h].z, val[j][4 + h].w)};
        }
      }
      MARK("P1epiend");
      FS(4);
      BAR();
      FS(5);
      {
        v2u pl[4], mi[4];
        TIE(val[0][0]); TIE(val[0][1]); TIE(val[1][0]); TIE(val[1][1]);
        const f4 qq[4] = {val[0][0], val[0][1], val[1][0], val[1][1]};
        featmap4(qq, wph, pl, mi);
#pragma unroll
        for (int i = 0; i < 4; ++i) phq[i >> 1][i & 1] = v4u{pl[i].x, pl[i].y, mi[i].x, mi[i].y};
      }
      FS(6);
    }
    GBAR();
    GBAR();
    {
      refresh(x);
      unsigned lb = lb0;
      asm volatile("" : "+s"(lb));
      BAR();
      FS(7);
      v4u ka[2], za[2];
#pragma unroll
      for (int h = 0; h < 2; ++h) {
        ka[h] = *lds4(x, M_KVA + (unsigned)h * 1024u + x.lane16);
        const v2u z0 = *(const LAS v2u *)(x.sm + M_ZA + (unsigned)(h * 2) * 32u + (unsigned)x.g * 8u);
        const v2u z1 = *(const LAS v2u *)(x.sm + M_ZA + (unsigned)(h * 2 + 1) * 32u + (unsigned)x.g * 8u);
        za[h] = v4u{z0.x, z0.y, z1.x, z1.y};
      }
#pragma unroll
      for (int j = 0; j < 2; ++j) {
        f4 o[2];
#pragma unroll
        for (int h = 0; h < 2; ++h) {
          const f4 num = mfma32(ka[h], phq[j][h], f4{0.f, 0.f, 0.f, 0.f});
          const f4 den = mfma32(za[h], phq[j][h], f4{0.f, 0.f, 0.f, 0.f});
          o[h] = num * __builtin_amdgcn_rcpf(den.x + 1e-6f);
        }
        stg(x.rs, x.lane16, lb + O_OB + (unsigned)((x.TA + j) * 16 + x.pp) * KB, pack8(o[0], o[1]));
      }
      FS(8);
      {
        const unsigned ob = lb + O_OB + (unsigned)(x.TA * 16 + x.pp) * KB;
        warm(x, (unsigned)((x.lane >> 3) & 1) * 16u * KB + (unsigned)(x.lane & 7) * 128u, ob, ob);
      }
    }
    GBAR();
    {
      refresh(x);
      unsigned lb = lb0;
      asm volatile("" : "+s"(lb));
      SLEEP(DLY_2);
      FS(9);
      v4u bf[16];
      const unsigned so = lb + O_OB + (unsigned)x.T * 16u * KB;
      Stats st;
      unsigned cn;
      const unsigned cvo = (unsigned)(x.lane & 15) * KB + 1020u;
      pro1<16, false, false>(x, bf, so, vz, cvo, st, 0u, cn);
      FS(10); FSP(1);
      BAR();
      chk1<16, false, false>(x, bf, so, vz, cvo, st, 0u, cn);
      FS(11);
      f4 acc[4];
#pragma unroll
      for (int f = 0; f < 4; ++f) acc[f] = f4{0.f, 0.f, 0.f, 0.f};
      kloop1<POS2, 16, false, 0, 16>(x, bf, acc, so, vz, sigv(L, 2));
      FS(12);
      x1r[0] = pack8(ulo(xres[0]) + acc[0], uhi(xres[0]) + acc[1]);
      x1r[1] = pack8(ulo(xres[1]) + acc[2], uhi(xres[1]) + acc[3]);
      const unsigned fo = lb + O_X1B + (unsigned)(x.T * 16 + 2 * x.c) * KB;
      stg(x.rs, x.lane16, fo, x1r[0]); stg(x.rs, x.lane16, fo + KB, x1r[1]);
      put_stats(x, x1r[0], x1r[1], lb + O_X1S);
      sig_set(x, sigv(L, 2), M_SIG2);
      FS(13);
      warm(x, (unsigned)(x.lane & 15) * 128u, fo, lb + O_X1S + (unsigned)(x.T * 8 + x.c) * 128u);
    }
    float mean1, rstd1;
    GBAR();
    {
      refresh(x);
      unsigned lb = lb0;
      asm volatile("" : "+s"(lb));
      SLEEP(DLY_3);
      v4u bf[16];
      Stats st;
      const unsigned so = lb + O_X1B + (unsigned)x.T * 16u * KB;
      unsigned cn;
      const unsigned cvo = (unsigned)(x.lane & 15) * KB + 1020u;
      pro1<16, false, true>(x, bf, so, vz, cvo, st, lb + O_X1S + (unsigned)x.T * 1024u, cn);
      FSP(2);
      BAR();
      chk1<16, false, true>(x, bf, so, vz, cvo, st, lb + O_X1S + (unsigned)x.T * 1024u, cn);
      FS(14);
      f4 acc[4];
#pragma unroll
      for (int f = 0; f < 4; ++f) acc[f] = f4{0.f, 0.f, 0.f, 0.f};
      kloop1<POS3, 16, false, 0, 16>(x, bf, acc, so, vz, sigv(L, 3));
      FS(15);
      float var;
      stats_fin(x, st, lb + O_X1S + (unsigned)x.T * 1024u, mean1, var);
      rstd1 = __builtin_amdgcn_rsqf(var + 1e-5f);
      const float a = rstd1 * __builtin_amdgcn_rsqf(__builtin_fmaf(var * rstd1, rstd1, 1e-5f)), ma = mean1 * a;
      f4 y[4];
#pragma unroll
      for (int f = 0; f < 4; ++f) y[f] = gelu4(acc[f] * a - ldcs(x, 6 + f) * ma);
      const unsigned fo = lb + O_HB + (unsigned)(x.T * 16 + 2 * x.c) * KB;
      stg(x.rs, x.lane16, fo, pack8(y[0], y[1])); stg(x.rs, x.lane16, fo + KB, pack8(y[2], y[3]));
      sig_set(x, sigv(L, 3), M_SIG2);
      FS(16);
      warm(x, (unsigned)(x.lane & 15) * 128u, fo, fo);
    }
    GBAR();
    {
      refresh(x);
      unsigned lb = lb0;
      asm volatile("" : "+s"(lb));
      SLEEP(DLY_4);
      v4u bf[16];
      unsigned vo[4];
#pragma unroll
      for (int q = 0; q < 4; ++q) {
        const int d = q == 0 ? -1 : (q == 1 ? 1 : (q == 2 ? -2 : 2));
        const int src = 16 * x.T + x.tok + d;
        vo[q] = (src >= 0 && src < 2048) ? (unsigned)((src >> 4) * 16 * 1024 + (x.g * 16 + (src & 15)) * 16) : OOB;
      }
      const unsigned so = lb + O_HB;
      unsigned cvo;
      {
        const int s_ = x.lane & 15, nb = x.T + (((s_ >> 2) & 1) ? 1 : -1);
        const int tt = (x.lane & 16) ? nb : x.T;
        cvo = (tt >= 0 && tt < 128) ? (unsigned)(tt * 16 + s_) * KB + 1020u : OOB;
      }
      Stats st;
      unsigned cn;
      pro1<16, true, false>(x, bf, so, vo, cvo, st, 0u, cn);
      FSP(3);
      BAR();
      chk1<16, true, false>(x, bf, so, vo, cvo, st, 0u, cn);
      FS(17);
      f4 acc[4];
#pragma unroll
      for (int f = 0; f < 4; ++f) acc[f] = f4{0.f, 0.f, 0.f, 0.f};
      MARK("P4loop");
      kloop1<POS4, 16, true, 0, 16>(x, bf, acc, so, vo, sigv(L, 4));
      MARK("P4end");
      FS(18);
      const float mr = mean1 * rstd1;
      x2r[0] = pack8(ulo(x1r[0]) * rstd1 - mr + acc[0], uhi(x1r[0]) * rstd1 - mr + acc[1]);
      x2r[1] = pack8(ulo(x1r[1]) * rstd1 - mr + acc[2], uhi(x1r[1]) * rstd1 - mr + acc[3]);
      const unsigned fo = lb + O_X2B + (unsigned)(x.T * 16 + 2 * x.c) * KB;
      stg(x.rs, x.lane16, fo, x2r[0]); stg(x.rs, x.lane16, fo + KB, x2r[1]);
      put_stats(x, x2r[0], x2r[1], lb + O_X2S);
      sig_set(x, sigv(L, 4), M_SIG2);
      FS(19);
      warm(x, (unsigned)(x.lane & 15) * 128u, fo, lb + O_X2S + (unsigned)(x.T * 8 + x.c) * 128u);
    }
    GBAR();
    {
      refresh(x);
      unsigned lb = lb0;
      asm volatile("" : "+s"(lb));
      SLEEP(DLY_5);
      v4u bf[16][2];
      Stats st[2];
      const unsigned so = lb + O_X2B + (unsigned)x.TA * 16u * KB, sst = lb + O_X2S + (unsigned)x.TA * 1024u;
      unsigned cn;
      pro2(x, bf, st, so, sst, cn);
      FSP(4);
      BAR();
      chk2(x, bf, st, so, sst, cn);
      FS(20);
      f4 acc[7][2];
#pragma unroll
      for (int f = 0; f < 7; ++f) { acc[f][0] = f4{0.f, 0.f, 0.f, 0.f}; acc[f][1] = f4{0.f, 0.f, 0.f, 0.f}; }
      unsigned mx = 0;
      MARK("P5loop");
      kloop2<7, POS5, 0, 13>(x, bf, acc, so, sigv(L, 5), mx);
      BAR();
      kloop2<7, POS5, 13, 16>(x, bf, acc, so, sigv(L, 5), mx);
      if (__builtin_expect(anybad(mx), 0)) redo2<7, POS5>(x, acc, so);
      FS(21);
      BAR();
      FS(22);
      MARK("P5epi");
      const int u = x.pp >> 1, odd = x.pp & 1;
#pragma unroll
      for (int j = 0; j < 2; ++j) {
        float mean, var;
        stats_fin(x, st[j], sst + (unsigned)j * 1024u, mean, var);
        const float rstd = __builtin_amdgcn_rsqf(var + 1e-5f), mr = mean * rstd;
        const unsigned hb = lb + O_H3B + (unsigned)((x.TA + j) * 56 + 7 * u) * KB;
#pragma unroll
        for (int r = 0; r < 3; ++r) {
          const f4 y0 = gelu4(acc[2 * r][j] * rstd - ldcs(x, 10 + 2 * r) * mr), y1 = gelu4(acc[2 * r + 1][j] * rstd - ldcs(x, 11 + 2 * r) * mr);
          stg(x.rs, x.lane16, hb + (unsigned)(odd ? 4 + r : r) * KB, pack8(y0, y1));
        }
        const f4 y = gelu4(acc[6][j] * rstd - ldcs(x, 16) * mr);
        stg2(x.rs, x.lane16 + (unsigned)odd * 8u, hb + 3 * KB, v2u{clampw(pk2(y.x, y.y)), clampw(pk2(y.z, y.w))});
      }
      FS(23);
      {
        const unsigned hb0 = lb + O_H3B + (unsigned)(x.TA * 56 + 7 * u + (odd ? 3 : 0)) * KB;
        warm(x, (unsigned)(x.lane >> 5) * 56u * KB + (unsigned)(x.lane & 31) * 128u, hb0, hb0);
      }
    }
    GBAR();
    {
      refresh(x);
      unsigned lb = lb0;
      asm volatile("" : "+s"(lb));
      SLEEP(DLY_6);
      v4u hf[56];
      const unsigned so = lb + O_H3B + (unsigned)x.T * 56u * KB;
      Stats st;
      unsigned cn;
      const unsigned cvo = (unsigned)min(x.lane, 55) * KB + 1020u;
      pro1<56, false, false>(x, hf, so, vz, cvo, st, 0u, cn);
      FSP(5);
      BAR();
      chk1<56, false, false>(x, hf, so, vz, cvo, st, 0u, cn);
      FS(24);
      f4 acc[4];
#pragma unroll
      for (int f = 0; f < 4; ++f) acc[f] = f4{0.f, 0.f, 0.f, 0.f};
      kloop1<POS6, 56, false, 0, 17>(x, hf, acc, so, vz, 0u);
      BAR();
      FS(25);
      kloop1<POS6, 56, false, 17, 39>(x, hf, acc, so, vz, 0u);
      BAR();
      FS(26);
      kloop1<POS6, 56, false, 39, 56>(x, hf, acc, so, vz, 0u);
      FS(27);
      BAR();
      xres[0] = pack8(ulo(x1r[0]) + (ulo(x2r[0]) + acc[0]), uhi(x1r[0]) + (uhi(x2r[0]) + acc[1]));
      xres[1] = pack8(ulo(x1r[1]) + (ulo(x2r[1]) + acc[2]), uhi(x1r[1]) + (uhi(x2r[1]) + acc[3]));
      if (L == 0) {
        const unsigned fo = lb + L_STRIDE + O_XB + (unsigned)(x.T * 16 + 2 * x.c) * KB;
        stg(x.rs, x.lane16, fo, xres[0]); stg(x.rs, x.lane16, fo + KB, xres[1]);
        put_stats(x, xres[0], xres[1], lb + L_STRIDE + O_XS);
        warm(x, (unsigned)(x.lane & 15) * 128u, fo, lb + L_STRIDE + O_XS + (unsigned)(x.T * 8 + x.c) * 128u);
        SLEEP(DLY_1);
      } else {
        GAS unsigned char *op = (GAS unsigned char *)p.out + ((size_t)(16 * x.T + x.tok) * 512 + 64 * x.c + 4 * x.g) * 2;
        *(GAS v2u *)op = v2u{xres[0].x, xres[0].y};
        *(GAS v2u *)(op + 32) = v2u{xres[0].z, xres[0].w};
        *(GAS v2u *)(op + 64) = v2u{xres[1].x, xres[1].y};
        *(GAS v2u *)(op + 96) = v2u{xres[1].z, xres[1].w};
      }
      FS(28); FSP(6);
    }
  }
}

DI void poison_h3(Ctx &x, unsigned lb, int j) {
  const int u = x.pp >> 1, odd = x.pp & 1;
  const unsigned hb = lb + O_H3B + (unsigned)((x.TA + j) * 56 + 7 * u) * KB;
#pragma unroll
  for (int r = 0; r < 3; ++r) poi4(x.rs, x.lane16, hb + (unsigned)(odd ? 4 + r : r) * KB);
  poi2(x.rs, x.lane16 + (unsigned)odd * 8u, hb + 3 * KB);
}
DI void helper_path(Ctx &x, unsigned tag) {
#pragma unroll 1
  for (int L = 0; L < 2; ++L) {
    const unsigned lb0 = OFF_L0 + (unsigned)L * L_STRIDE;
    GBAR();
    {
      refresh(x);
      unsigned lb = lb0;
      asm volatile("" : "+s"(lb));
      FS(0);
      DRAIN_BAR();
      FS(1);
      if (L != 0) {
        sig_wait(x, sigv(L, 0));
        dmaR<96, 128>(x);
      }
      BAR();
      if (L != 0) dmaR<128, 156>(x);
      {
        const unsigned fo = (unsigned)(x.T * 16 + 2 * x.c) * KB, sto = (unsigned)(x.T * 8 + x.c) * 128u;
        poi4(x.rs, x.lane16, lb + O_X1B + fo); poi4(x.rs, x.lane16, lb + O_X1B + fo + KB);
        poi4(x.rs, x.lane16, lb + O_X2B + fo); poi4(x.rs, x.lane16, lb + O_X2B + fo + KB);
        if (x.g == 0) { poi2(x.rs, (unsigned)x.tok * 8u, lb + O_X1S + sto); poi2(x.rs, (unsigned)x.tok * 8u, lb + O_X2S + sto); }
      }
      FS(2);
      BAR();
      FS(3);
      {
        const int h = x.m >> 1, sg = x.m & 1;
        const unsigned lo = (unsigned)(4 * x.g + (x.tok >> 2)) * 32u + (unsigned)(x.tok & 3) * 8u;
        const unsigned ia = IMG + (unsigned)(h * 3 + sg) * 4096u + lo, iv = IMG + (unsigned)(h * 3 + 2) * 4096u + lo;
        f4 kv = {0.f, 0.f, 0.f, 0.f}, z = {0.f, 0.f, 0.f, 0.f};
#pragma unroll
        for (int tb = 0; tb < 4; ++tb) {
          const v2u a0 = trread(x, ia + tb * 1024u), a1 = trread(x, ia + tb * 1024u + 512u);
          const v2u b0 = trread(x, iv + tb * 1024u), b1 = trread(x, iv + tb * 1024u + 512u);
          const v4u a = {a0.x, a0.y, a1.x, a1.y};
          kv = mfma32(a, v4u{b0.x, b0.y, b1.x, b1.y}, kv);
          z = mfma32(a, v4u{0x3F803F80u, 0x3F803F80u, 0x3F803F80u, 0x3F803F80u}, z);
        }
        stg2(x.rs, (unsigned)x.lane * 8u, lb + O_KVP + (unsigned)((x.pp * 16 + x.tau) * 4 + x.m) * KB, v2u{clampw(pk2(kv.x, kv.y)), clampw(pk2(kv.z, kv.w))});
        if (x.tok == 0) stg(x.rs, (unsigned)x.g * 16u, lb + O_KVZ + (unsigned)((x.pp * 4 + x.m) * 16 + x.tau) * 64u, clamp4(__builtin_bit_cast(v4u, z)));
      }
      poison_h3(x, lb, 0); poison_h3(x, lb, 1);
      FS(4);
    }
    GBAR();
    {
      refresh(x);
      unsigned lb = lb0;
      asm volatile("" : "+s"(lb));
      if (L == 0) {
        if (x.m == 0) {
          poll_flags(x, OFF_FLAGS2, tag);
          *(volatile LAS unsigned *)(x.sm + M_RDY) = 1u;
        } else {
          for (unsigned sp = 0; sp < (1u << 20); ++sp) {
            if (__builtin_amdgcn_readfirstlane(*(volatile LAS unsigned *)(x.sm + M_RDY))) break;
            __builtin_amdgcn_s_sleep(2);
          }
        }
        v4u cv[3];
#pragma unroll
        for (int jj = 0; jj < 3; ++jj) {
          const int u = min(6 + x.m + 4 * jj, 16);
          cv[jj] = ldg(x.rs, x.lane16, OFF_CSP + (u < 10 ? 96u + (unsigned)(x.c * 4 + u - 6) : 128u + (unsigned)(x.pp * 7 + u - 10)) * KB);
        }
#pragma unroll
        for (int jj = 0; jj < 3; ++jj) {
          const f4 s = shsum(__builtin_bit_cast(f4, cv[jj]));
          const int u = 6 + x.m + 4 * jj;
          if (u < 17 && x.lane < 4) *(LAS f4 *)(x.sm + M_CS + (unsigned)u * 64u + (unsigned)x.lane * 16u) = s;
        }
      }
      SLEEP(DLY_KV);
      v4u pv[16];
      const unsigned so = lb + O_KVP + (unsigned)(x.pp * 64 + x.m) * KB, sz = lb + O_KVZ + (unsigned)(x.pp * 4 + x.m) * KB;
      v4u zv;
      {
        const unsigned cvo = (unsigned)(x.lane & 15) * 4096u + (KV16 ? 508u : 1020u);
        while (true) {
          unsigned cn;
          LDC(cn, cvo, so);
          LDA(zv, x.lane16, sz);
          WAITV2(0, cn, zv);
          if (!anybad(max(cn, max4(zv))) || giveup(x)) break;
        }
      }
      bool again;
      f4 s;
      v2u pw[16];
      do {
        asm volatile("" ::: "memory");
#pragma unroll
        for (int i = 0; i < 16; ++i) pw[i] = __builtin_amdgcn_raw_buffer_load_b64(x.rs, (unsigned)x.lane * 8u, so + (unsigned)i * 4096u, AUX_SC1);
        unsigned mx = 0;
#pragma unroll
        for (int i = 0; i < 16; ++i) mx = max(mx, max(pw[i].x, pw[i].y));
        again = anybad(mx) && !giveup(x);
      } while (__builtin_expect(again, 0));
      s = ulo(v4u{pw[0].x, pw[0].y, 0u, 0u});
#pragma unroll
      for (int i = 1; i < 16; ++i) s += ulo(v4u{pw[i].x, pw[i].y, 0u, 0u});
      *(LAS v2u *)(x.sm + M_KVA + (unsigned)(x.m >> 1) * 1024u + x.lane16 + (unsigned)(x.m & 1) * 8u) = v2u{pk2(s.x, s.y), pk2(s.z, s.w)};
      const f4 zs = shsum(__builtin_bit_cast(f4, zv));
      if (x.lane < 4) *(LAS v2u *)(x.sm + M_ZA + (unsigned)x.m * 32u + (unsigned)x.lane * 8u) = v2u{pk2(zs.x, zs.y), pk2(zs.z, zs.w)};
      FS(5); FSP(0);
      GBAR();
      BAR();
      FS(6);
      if (L == 0) dmaR<96, 156>(x);
      dmaR<156, 188>(x);
    }
    GBAR();
    {
      refresh(x);
      unsigned lb = lb0;
      asm volatile("" : "+s"(lb));
      DRAIN_BAR();
      FS(7);
      sig_wait(x, sigv(L, 2));
      dmaR<188, 220>(x);
      sig_wait(x, sigv(L, 2), M_SIG2);
      dmaR<220, 252>(x);
    }
    GBAR();
    {
      refresh(x);
      unsigned lb = lb0;
      asm volatile("" : "+s"(lb));
      DRAIN_BAR();
      FS(8);
      sig_wait(x, sigv(L, 3));
      FS(15);
      dmaR<252, 284>(x);
      sig_wait(x, sigv(L, 3), M_SIG2);
      dmaR<284, 316>(x);
      FS(16);
    }
    GBAR();
    {
      refresh(x);
      unsigned lb = lb0;
      asm volatile("" : "+s"(lb));
      DRAIN_BAR();
      if (L == 0) {
        const unsigned fo = lb + L_STRIDE + O_XB + (unsigned)(x.T * 16 + 2 * x.c) * KB;
        poi4(x.rs, x.lane16, fo); poi4(x.rs, x.lane16, fo + KB);
        if (x.g == 0) poi2(x.rs, (unsigned)x.tok * 8u, lb + L_STRIDE + O_XS + (unsigned)(x.T * 8 + x.c) * 128u);
      }
      FS(9);
      sig_wait(x, sigv(L, 4));
      FS(18);
      dmaR<316, 348>(x);
      sig_wait(x, sigv(L, 4), M_SIG2);
      dmaR<348, 380>(x);
      FS(19);
    }
    GBAR();
    {
      refresh(x);
      unsigned lb = lb0;
      asm volatile("" : "+s"(lb));
      DRAIN_BAR();
      if (L == 0) {
        const unsigned fo = lb + L_STRIDE + O_HB + (unsigned)(x.T * 16 + 2 * x.c) * KB;
        poi4(x.rs, x.lane16, fo); poi4(x.rs, x.lane16, fo + KB);
        poi4(x.rs, x.lane16, lb + L_STRIDE + O_OB + (unsigned)(x.TA * 16 + x.pp) * KB);
        poi4(x.rs, x.lane16, lb + L_STRIDE + O_OB + (unsigned)((x.TA + 1) * 16 + x.pp) * KB);
      }
      FS(10);
      dmaRT<380, 400>(x);
      DRAIN_BAR();
      sig_wait(x, sigv(L, 5));
      dmaR<400, 444>(x);
      BAR();
      FS(11);
      dmaR6<444, 556>(x);
    }
    GBAR();
    {
      refresh(x);
      v4u wr[17];
      {
        const unsigned ws0 = OFF_WT + (W6_F + (unsigned)(x.c * 224 + 156 + x.m)) * KB;
#pragma unroll
        for (int i = 0; i < 17; ++i) LDA(wr[i], x.lane16, ws0 + (unsigned)i * 4096u);
      }
      WAITV_BAR(17);
      FS(12);
      BAR();
      asm volatile("s_waitcnt vmcnt(0)" : "+v"(wr[0]), "+v"(wr[1]), "+v"(wr[2]), "+v"(wr[3]), "+v"(wr[4]), "+v"(wr[5]), "+v"(wr[6]), "+v"(wr[7]),
                   "+v"(wr[8]), "+v"(wr[9]), "+v"(wr[10]), "+v"(wr[11]), "+v"(wr[12]), "+v"(wr[13]), "+v"(wr[14]), "+v"(wr[15]), "+v"(wr[16])::"memory");
#pragma unroll
      for (int i = 0; i < 17; ++i) *(LAS v4u *)(x.sm + ((556 + 4 * i) % RING + x.m) * KB + x.lane16) = wr[i];
      DRAIN_BAR();
      FS(13);
      dmaR<624, 712>(x, L == 0);
      BAR();
      dmaR<712, 720>(x, L == 0);
      FS(14);
    }
  }
}

}

__global__ void __launch_bounds__(512, 2) mega_kernel(Params p) {
  extern __shared__ __attribute__((aligned(16))) unsigned char smem_[];
  Ctx x;
  x.sm = (LAS unsigned char *)smem_;
  x.lds0 = (unsigned)(size_t)smem_;
  x.lane = threadIdx.x & 63;
  x.w = __builtin_amdgcn_readfirstlane(threadIdx.x >> 6);
  x.b = blockIdx.x;
  refresh(x);
  const int b = x.b;
  x.spins = 0;
  x.ctl = (gu32 *)p.ws;
  x.rs = __builtin_amdgcn_make_buffer_rsrc((void *)p.ws, 0, WS_BYTES, 0x00020000);
  const bool cw = x.w < 4;

  FSA(0);
  const unsigned tag = __hip_atomic_load(x.ctl + OFF_EPOCH / 4, RLX_AGENT) + 1u;
  unsigned pf0 = 0, pf1 = 0;
  if (x.w == 7) {
    unsigned long long pc;
    asm volatile("s_getpc_b64 %0" : "=s"(pc));
    const GAS unsigned char *cp = (const GAS unsigned char *)(pc & ~127ull) + (size_t)(((b >> 3) + 32 * (x.lane & 15)) * 128);
    const GAS unsigned char *wp = (const GAS unsigned char *)p.ws + (size_t)min(x.lane, 28) * (2 * MB) + 1 * MB;
    if (x.lane >= 32) {
      const int q = x.lane & 7;
      const unsigned short *ip = q == 0 ? p.x : q == 1 ? p.wqkv : q == 2 ? p.wo : q == 3 ? p.w1 : q == 4 ? p.w2 : q == 5 ? p.w3 : q == 6 ? p.w4 : p.x;
      const size_t half = (q == 2 || q == 3 || q == 4) ? 256 * KB : 1 * MB;
      wp = (const GAS unsigned char *)ip + ((x.lane & 8) ? half : 0);
    }
    asm volatile("global_load_dword %0, %2, off\n\tglobal_load_dword %1, %3, off" : "=&v"(pf0), "=&v"(pf1) : "v"(cp), "v"(wp) : "memory");
    asm volatile("s_waitcnt vmcnt(0)" : "+v"(pf0), "+v"(pf1)::"memory");
  }
  v4u xa = {}, xb = {};
  v2u wa = {}, wb = {};
  if (cw) {
    const GAS unsigned char *xp = (const GAS unsigned char *)p.x + ((size_t)(16 * x.T + (x.lane >> 3)) * 512 + 64 * x.c) * 2 + (x.lane & 7) * 16;
    xa = *(const GAS v4u *)xp; xb = *(const GAS v4u *)(xp + 8 * 1024);
    const GAS unsigned char *wp = (const GAS unsigned char *)p.wphi + (size_t)(2 * x.pp) * 512 + x.lane * 8;
    wa = *(const GAS v2u *)wp; wb = *(const GAS v2u *)(wp + 512);
  }
  if (threadIdx.x < 9) *(LAS unsigned *)(x.sm + M_SIG + threadIdx.x * 4u) = 0u;
  const int bid = ((x.w + 4) & 7) * 256 + b;
  const unsigned short *src = p.wqkv; int ld = 1536, ra = 0, rb = 0, n0 = 0, kstep = 0, csid = -1; unsigned fr[4] = {0u, 0u, 0u, 0u}, ci[4] = {0u, 0u, 0u, 0u};
  if (bid < 384) {
    kstep = bid & 15; const int nb = bid >> 4; n0 = nb * 64; ra = 32 * kstep; rb = ra + 16;
    const int sec = nb >> 3, h0 = 4 * (nb & 7);
#pragma unroll
    for (int ct = 0; ct < 4; ++ct) {
      const int hh = h0 + ct;
      fr[ct] = W1_F + (unsigned)(((hh >> 1) * 16 + kstep) * 6 + sec * 2 + (hh & 1));
      ci[ct] = (unsigned)((hh >> 1) * 6 + sec * 2 + (hh & 1));
    }
    csid = 0;
  } else if (bid < 768) {
    const int mi = (bid - 384) >> 7, r = (bid - 384) & 127; kstep = r & 15; const int nb = r >> 4;
    src = mi == 0 ? p.wo : (mi == 1 ? p.w1 : p.w2); ld = 512; n0 = nb * 64; ra = 32 * kstep; rb = ra + 16;
#pragma unroll
    for (int ct = 0; ct < 4; ++ct) { fr[ct] = W2_F + (unsigned)(mi * 512 + (nb * 16 + kstep) * 4 + ct); ci[ct] = 96u + (unsigned)(nb * 4 + ct); }
    if (mi == 1) csid = 1;
  } else if (bid < 1216) {
    const int r = bid - 768; kstep = r & 15; const int nb = r >> 4; src = p.w3; ld = 1792; n0 = nb * 64; ra = 32 * kstep; rb = ra + 16;
#pragma unroll
    for (int ct = 0; ct < 4; ++ct) { const int F = nb * 4 + ct; fr[ct] = W5_F + (unsigned)(((F / 7) * 16 + kstep) * 7 + F % 7); ci[ct] = 128u + (unsigned)F; }
    csid = 2;
  } else if (bid < 1664) {
    const int r = bid - 1216; const int nb = r & 7; kstep = r >> 3; src = p.w4; ld = 512; n0 = nb * 64;
    const int u = kstep / 7, q = kstep % 7;
    if (q < 3) { ra = 224 * u + 32 * q; rb = ra + 16; }
    else if (q == 3) { ra = 224 * u + 96; rb = 224 * u + 208; }
    else { ra = 224 * u + 112 + 32 * (q - 4); rb = ra + 16; }
#pragma unroll
    for (int ct = 0; ct < 4; ++ct) fr[ct] = W6_F + (unsigned)((nb * 56 + kstep) * 4 + ct);
  }
  if (bid < 1664) {
    const int ip = x.lane >> 5, uu = (x.lane >> 4) & 1, q = (x.lane >> 2) & 3, c1 = ((x.lane >> 1) & 1) ^ ip, bb = x.lane & 1;
    const int rl = 4 * ip + q, ch = 2 * (2 * uu + c1) + bb;
    const unsigned stg0 = x.lds0 + (unsigned)x.w * 4096u;
#pragma unroll
    for (int u = 0; u < 4; ++u) {
      const int row = (u < 2 ? ra + 8 * u : rb + 8 * (u - 2)) + rl;
      const GAS unsigned char *gp = (const GAS unsigned char *)src + ((size_t)row * ld + n0 + ch * 8) * 2;
      asm volatile("s_mov_b32 m0, %0\n\ts_nop 0\n\tglobal_load_lds_dwordx4 %1, off" ::"s"(stg0 + (unsigned)u * 1024u), "v"(gp) : "memory");
    }
  }
  auto retile = [&]() {
    asm volatile("s_waitcnt vmcnt(0)" ::: "memory");
    const int gq = (x.lane & 15) >> 2, gp_ = x.lane & 3, gi = x.g & 1;
#pragma unroll
    for (int ct = 0; ct < 4; ++ct) {
      const unsigned L = (unsigned)((gi << 5) | ((ct >> 1) << 4) | (gq << 2) | (((ct & 1) ^ gi) << 1) | (gp_ >> 1));
      const unsigned a0 = (unsigned)x.w * 4096u + (unsigned)(x.g >> 1) * 1024u + L * 16u + (unsigned)(gp_ & 1) * 8u;
      const v2u lo = trread(x, a0), hi = trread(x, a0 + 2048u);
      const v4u fragv = {lo.x, lo.y, hi.x, hi.y};
      stg(x.rs, x.lane16, __builtin_amdgcn_readfirstlane(OFF_WT + fr[ct] * KB), fragv);
      if (csid >= 0) {
        const f4 cs = mfma32(fragv, v4u{0x3F803F80u, 0x3F803F80u, 0x3F803F80u, 0x3F803F80u}, f4{0.f, 0.f, 0.f, 0.f});
        const unsigned co = __builtin_amdgcn_readfirstlane(OFF_CSP + (ci[ct] * 16u + (unsigned)kstep) * 64u);
        if (x.tok == 0) stg(x.rs, (unsigned)x.g * 16u, co, __builtin_bit_cast(v4u, cs));
      }
    }
  };
  if (bid < 384) retile();
  FSA(1);
  v4u xres[2];
  v2u wph[2];
  if (cw) {
    const unsigned xs = XSTG + (unsigned)x.m * 3072u;
    *(LAS v4u *)(x.sm + xs + x.lane16) = xa; *(LAS v4u *)(x.sm + xs + 1024u + x.lane16) = xb;
    *(LAS v2u *)(x.sm + xs + 2048u + (unsigned)x.lane * 8u) = wa; *(LAS v2u *)(x.sm + xs + 2560u + (unsigned)x.lane * 8u) = wb;
    v2u xin[4];
#pragma unroll
    for (int q = 0; q < 4; ++q) xin[q] = *(const LAS v2u *)(x.sm + xs + (unsigned)x.tok * 128u + (unsigned)q * 32u + (unsigned)x.g * 8u);
#pragma unroll
    for (int h = 0; h < 2; ++h) wph[h] = trread(x, xs + 2048u + (unsigned)h * 512u + (unsigned)(4 * x.g + (x.tok >> 2)) * 32u + (unsigned)(x.tok & 3) * 8u);
    xres[0] = clamp4(v4u{xin[0].x, xin[0].y, xin[1].x, xin[1].y});
    xres[1] = clamp4(v4u{xin[2].x, xin[2].y, xin[3].x, xin[3].y});
    const unsigned fo = OFF_L0 + O_XB + (unsigned)(x.T * 16 + 2 * x.c) * KB;
    stg(x.rs, x.lane16, fo, xres[0]); stg(x.rs, x.lane16, fo + KB, xres[1]);
    put_stats(x, xres[0], xres[1], OFF_L0 + O_XS);
  } else {
    xres[0] = xres[1] = v4u{0u, 0u, 0u, 0u};
    wph[0] = wph[1] = v2u{0u, 0u};
  }
  FSA(2);
  asm volatile("s_waitcnt vmcnt(0) lgkmcnt(0)\n\ts_barrier" ::: "memory");
  FSA(3);
  if (threadIdx.x == 0) __hip_atomic_store(x.ctl + OFF_FLAGS / 4 + b * 16, tag, RLX_AGENT);
  if (bid >= 384 && bid < 1664) retile();
  if (!cw) {
    poi4(x.rs, x.lane16, OFF_L0 + O_KVP + (unsigned)((x.pp * 16 + x.tau) * 4 + x.m) * KB);
    if (x.tok == 0) poi4(x.rs, (unsigned)x.g * 16u, OFF_L0 + O_KVZ + (unsigned)((x.pp * 4 + x.m) * 16 + x.tau) * 64u);
    poi4(x.rs, x.lane16, OFF_L0 + O_OB + (unsigned)(x.TA * 16 + x.pp) * KB);
    poi4(x.rs, x.lane16, OFF_L0 + O_OB + (unsigned)((x.TA + 1) * 16 + x.pp) * KB);
    poi4(x.rs, x.lane16, OFF_L0 + O_HB + (unsigned)(x.T * 16 + 2 * x.c) * KB);
    poi4(x.rs, x.lane16, OFF_L0 + O_HB + (unsigned)(x.T * 16 + 2 * x.c + 1) * KB);
    poi4(x.rs, x.lane16, OFF_L0 + L_STRIDE + O_KVP + (unsigned)((x.pp * 16 + x.tau) * 4 + x.m) * KB);
    if (x.tok == 0) poi4(x.rs, (unsigned)x.g * 16u, OFF_L0 + L_STRIDE + O_KVZ + (unsigned)((x.pp * 4 + x.m) * 16 + x.tau) * 64u);
  }
  if (x.w == 4) {
    poll_flags(x, OFF_FLAGS, tag);
    if (b == 0 && x.lane == 0) __hip_atomic_store(x.ctl + OFF_EPOCH / 4, tag, RLX_AGENT);
  }
  BAR();
  FSA(4);
  if (cw) {
    compute_path(x, p, xres, wph, tag);
  } else {
    v4u cv[2];
#pragma unroll
    for (int jj = 0; jj < 2; ++jj) LDA(cv[jj], x.lane16, OFF_CSP + (unsigned)(x.pp * 6 + min(x.m + 4 * jj, 5)) * KB);
    dmaR<POS1, POS2>(x);
#pragma unroll
    for (int jj = 0; jj < 2; ++jj) {
      WAITV1(24, cv[jj]);
      const f4 s = shsum(__builtin_bit_cast(f4, cv[jj]));
      const int u = x.m + 4 * jj;
      if (u < 6 && x.lane < 4) *(LAS f4 *)(x.sm + M_CS + (unsigned)u * 64u + (unsigned)x.lane * 16u) = s;
    }
    helper_path(x, tag);
  }
  asm volatile("s_waitcnt vmcnt(0)" ::: "memory");
}

extern "C" void kernel_launch(void *const *d_in, const int *in_sizes, int n_in, void *d_out, int out_size, void *d_ws, size_t ws_size,
                              hipStream_t stream) {
  Params p;
  p.x = (const unsigned short *)d_in[0];
  p.wqkv = (const unsigned short *)d_in[1];
  p.wphi = (const unsigned short *)d_in[2];
  p.wo = (const unsigned short *)d_in[3];
  p.w1 = (const unsigned short *)d_in[5];
  p.w2 = (const unsigned short *)d_in[6];
  p.w3 = (const unsigned short *)d_in[7];
  p.w4 = (const unsigned short *)d_in[8];
  p.out = (unsigned short *)d_out;
  p.ws = (unsigned char *)d_ws;
  (void)hipFuncSetAttribute((const void *)mega_kernel, hipFuncAttributeMaxDynamicSharedMemorySize, LDS_BYTES);
  hipLaunchKernelGGL(mega_kernel, dim3(256), dim3(512), LDS_BYTES, stream, p);
}
```

```cpp
#include <hip/hip_runtime.h>
#include <stdint.h>

#define GAS __attribute__((address_space(1)))
#define LAS __attribute__((address_space(3)))
#define DI __device__ __forceinline__
typedef unsigned v4u __attribute__((ext_vector_type(4)));
typedef unsigned v2u __attribute__((ext_vector_type(2)));
typedef float f4 __attribute__((ext_vector_type(4)));
typedef float f2 __attribute__((ext_vector_type(2)));
typedef short s8 __attribute__((ext_vector_type(8)));
typedef short s4 __attribute__((ext_vector_type(4)));
typedef __bf16 bf2 __attribute__((ext_vector_type(2)));
typedef GAS unsigned gu32;
typedef __amdgpu_buffer_rsrc_t rsrc_t;
#define RLX_AGENT __ATOMIC_RELAXED, __HIP_MEMORY_SCOPE_AGENT

#define DBG_GBAR 0
#define DBG_NOPOISON 0
#define DBG_STAMP 0
#define SPEC 0
#define PREFETCH 1
#define KV16 1
#define WARM 0

namespace {
constexpr unsigned MB = 1u << 20, KB = 1024u;
constexpr unsigned OFF_EPOCH = 0, OFF_ABORT = 256, OFF_GBAR = 512, OFF_FLAGS = 16384, OFF_FLAGS2 = 32768;
constexpr unsigned OFF_WT = 1 * MB;
constexpr unsigned W1_F = 0, W2_F = 1536, W5_F = 3072, W6_F = 4864;
constexpr unsigned OFF_CSP = 8 * MB;
constexpr unsigned OFF_L0 = 9 * MB, L_STRIDE = 24 * MB;
constexpr unsigned O_XB = 0, O_XS = 2 * MB, O_KVP = 2 * MB + 512 * KB, O_KVZ = 3 * MB + 512 * KB, O_OB = 4 * MB, O_X1B = 6 * MB,
                   O_X1S = 8 * MB, O_HB = 9 * MB, O_X2B = 11 * MB, O_X2S = 13 * MB, O_H3B = 14 * MB;
constexpr unsigned WS_BYTES = OFF_L0 + 2 * L_STRIDE;
constexpr unsigned OOB = 0x7F000000u;
constexpr int RING = 156;
constexpr unsigned MISC = RING * KB;
constexpr unsigned M_CS = MISC, M_KVA = MISC + 1088, M_ZA = MISC + 3136, M_SIG = MISC + 3264, M_SIG2 = MISC + 3280, M_RDY = MISC + 3296, M_FLAG = MISC + 3300;
constexpr unsigned XSTG = 64 * KB;
constexpr unsigned IMG = 0;
constexpr unsigned LDS_BYTES = 163840;
constexpr int POS1 = 0, POS2 = 96, POS3 = 160, POS4 = 224, POS5 = 288, POS6 = 400, POSE = 624;
constexpr unsigned SENT = 0xFFFFFFFFu;
constexpr unsigned SPIN_CAP = 1u << 13;
constexpr int AUX_SC1 = 16;
#define DLY_KV 14
#define DLY_1 19
#define DLY_2 19
#define DLY_3 19
#define DLY_4 19
#define DLY_5 19
#define DLY_6 19

struct Params {
  const unsigned short *x, *wqkv, *wphi, *wo, *w1, *w2, *w3, *w4;
  unsigned short *out;
  unsigned char *ws;
};

DI unsigned pk2(float a, float b) { f2 v = {a, b}; bf2 r = __builtin_convertvector(v, bf2); return __builtin_bit_cast(unsigned, r); }
DI float asf(unsigned u) { return __builtin_bit_cast(float, u); }
DI float blo(unsigned u) { return __builtin_bit_cast(float, u << 16); }
DI float bhi(unsigned u) { return __builtin_bit_cast(float, u & 0xFFFF0000u); }
DI unsigned clampw(unsigned u) { return u < 0xFFFFFFFEu ? u : 0xFFFFFFFEu; }
DI unsigned clampf(float f) { return clampw(__builtin_bit_cast(unsigned, f)); }
DI v4u clamp4(v4u v) { return v4u{clampw(v.x), clampw(v.y), clampw(v.z), clampw(v.w)}; }
DI v4u pack8(f4 a, f4 b) { return clamp4(v4u{pk2(a.x, a.y), pk2(a.z, a.w), pk2(b.x, b.y), pk2(b.z, b.w)}); }
DI f4 ulo(v4u v) { return f4{blo(v.x), bhi(v.x), blo(v.y), bhi(v.y)}; }
DI f4 uhi(v4u v) { return f4{blo(v.z), bhi(v.z), blo(v.w), bhi(v.w)}; }
DI f4 mfma32(v4u a, v4u b, f4 c) {
  return __builtin_amdgcn_mfma_f32_16x16x32_bf16(__builtin_bit_cast(s8, a), __builtin_bit_cast(s8, b), c, 0, 0, 0);
}
DI f4 mfma16(v2u a, v2u b, f4 c) {
  return __builtin_amdgcn_mfma_f32_16x16x16bf16_1k(__builtin_bit_cast(s4, a), __builtin_bit_cast(s4, b), c, 0, 0, 0);
}
DI v4u ldg(rsrc_t rs, unsigned voff, unsigned soff) { return __builtin_amdgcn_raw_buffer_load_b128(rs, voff, soff, AUX_SC1); }
DI void stg(rsrc_t rs, unsigned voff, unsigned soff, v4u v) {
  __builtin_amdgcn_raw_buffer_store_b128(v, rs, voff, soff, AUX_SC1);
  asm volatile("s_nop 1" ::"v"(v));
}
DI void stg2(rsrc_t rs, unsigned voff, unsigned soff, v2u v) { __builtin_amdgcn_raw_buffer_store_b64(v, rs, voff, soff, AUX_SC1); }
DI void poi4(rsrc_t rs, unsigned voff, unsigned soff) { if (!DBG_NOPOISON) stg(rs, voff, soff, v4u{0xFFFFFFFFu, 0xFFFFFFFFu, 0xFFFFFFFFu, 0xFFFFFFFFu}); }
DI void poi2(rsrc_t rs, unsigned voff, unsigned soff) { if (!DBG_NOPOISON) stg2(rs, voff, soff, v2u{0xFFFFFFFFu, 0xFFFFFFFFu}); }
DI unsigned max4(v4u v) { return max(max(v.x, v.y), max(v.z, v.w)); }
DI bool anybad(unsigned mx) { return __any(mx == SENT) != 0; }
DI float xsum(float x) {
  unsigned u = __builtin_bit_cast(unsigned, x);
  auto r = __builtin_amdgcn_permlane16_swap(u, u, false, false);
  x = __builtin_bit_cast(float, (unsigned)r[0]) + __builtin_bit_cast(float, (unsigned)r[1]);
  u = __builtin_bit_cast(unsigned, x);
  auto q = __builtin_amdgcn_permlane32_swap(u, u, false, false);
  return __builtin_bit_cast(float, (unsigned)q[0]) + __builtin_bit_cast(float, (unsigned)q[1]);
}
DI float xmax(float x) {
  unsigned u = __builtin_bit_cast(unsigned, x);
  auto r = __builtin_amdgcn_permlane16_swap(u, u, false, false);
  x = fmaxf(__builtin_bit_cast(float, (unsigned)r[0]), __builtin_bit_cast(float, (unsigned)r[1]));
  u = __builtin_bit_cast(unsigned, x);
  auto q = __builtin_amdgcn_permlane32_swap(u, u, false, false);
  return fmaxf(__builtin_bit_cast(float, (unsigned)q[0]), __builtin_bit_cast(float, (unsigned)q[1]));
}
template <bool MAX> DI float red_op(float a, float b) { return MAX ? fmaxf(a, b) : a + b; }
DI unsigned fbits(float f) { return __builtin_bit_cast(unsigned, f); }
template <bool MAX> DI void xred4(float &a, float &b, float &c, float &d) {
  const auto r1 = __builtin_amdgcn_permlane16_swap(fbits(a), fbits(b), false, false);
  const auto r2 = __builtin_amdgcn_permlane16_swap(fbits(c), fbits(d), false, false);
  const float c1 = red_op<MAX>(asf(r1[0]), asf(r1[1])), c2 = red_op<MAX>(asf(r2[0]), asf(r2[1]));
  const auto r3 = __builtin_amdgcn_permlane32_swap(fbits(c1), fbits(c2), false, false);
  const unsigned e = fbits(red_op<MAX>(asf(r3[0]), asf(r3[1])));
  const auto r4 = __builtin_amdgcn_permlane32_swap(e, e, false, false);
  const unsigned f0 = r4[0], f1 = r4[1];
  const auto r5 = __builtin_amdgcn_permlane16_swap(f0, f0, false, false);
  const auto r6 = __builtin_amdgcn_permlane16_swap(f1, f1, false, false);
  a = asf(r5[0]); b = asf(r5[1]); c = asf(r6[0]); d = asf(r6[1]);
}
template <bool MAX> DI void xred2(float &a, float &b) {
  const auto r1 = __builtin_amdgcn_permlane16_swap(fbits(a), fbits(b), false, false);
  const unsigned c = fbits(red_op<MAX>(asf(r1[0]), asf(r1[1])));
  const auto r3 = __builtin_amdgcn_permlane32_swap(c, c, false, false);
  const unsigned e = fbits(red_op<MAX>(asf(r3[0]), asf(r3[1])));
  const auto r5 = __builtin_amdgcn_permlane16_swap(e, e, false, false);
  a = asf(r5[0]); b = asf(r5[1]);
}
DI f2 gelu2(f2 x) {
  const f2 c1 = {-2.302208198f, -2.302208198f}, c2 = {-0.1029432397f, -0.1029432397f}, one = {1.0f, 1.0f};
  const f2 t = x * __builtin_elementwise_fma(x * x, c2, c1);
  const f2 d = one + f2{__builtin_amdgcn_exp2f(t.x), __builtin_amdgcn_exp2f(t.y)};
  return x * f2{__builtin_amdgcn_rcpf(d.x), __builtin_amdgcn_rcpf(d.y)};
}
DI f4 gelu4(f4 y) {
  const f2 a = gelu2(f2{y.x, y.y}), b = gelu2(f2{y.z, y.w});
  return f4{a.x, a.y, b.x, b.y};
}

#define FSL 1
#define FS(i) do {} while (0)
#define FSA(i) do { unsigned long long t_; asm volatile("s_memtime %0\n\ts_waitcnt lgkmcnt(0)" : "=s"(t_) :: "memory"); \
  int ww_ = x.w; asm volatile("" : "+s"(ww_)); \
  unsigned a_ = (ww_ == 0) ? (70 + (i)) * 4u : ((ww_ == 4) ? 400u + (70 + (i)) * 4u : 780u); \
  asm volatile("" : "+s"(a_)); \
  *(LAS unsigned *)(x.sm + M_FLAG + a_) = (unsigned)t_; } while (0)
#define FSP(i) do {} while (0)
#define SB() __builtin_amdgcn_sched_barrier(0)
#define MARK(s) asm volatile("; MARK " s)
#define BAR() asm volatile("s_waitcnt lgkmcnt(0)\n\ts_barrier" ::: "memory")
#define WAITV_BAR(n) asm volatile("s_waitcnt vmcnt(" #n ") lgkmcnt(0)\n\ts_barrier" ::: "memory")
#define DRAIN_BAR() asm volatile("s_waitcnt vmcnt(0) lgkmcnt(0)\n\ts_barrier" ::: "memory")
DI void sleepn(int n) {
  for (int i = 0; i < n; ++i) __builtin_amdgcn_s_sleep(1);
}

struct Ctx {
  rsrc_t rs;
  LAS unsigned char *sm;
  unsigned lds0;
  unsigned lane16;
  LAS unsigned char *ab[3];
  int lane, w, m, g, tok;
  int t, c, tau, pp, b;
  int T, TA;
  unsigned spins;
  gu32 *ctl;
};
DI void refresh(Ctx &x) {
  asm volatile("" : "+s"(x.w), "+s"(x.b), "+v"(x.lane));
  x.lane16 = (unsigned)x.lane * 16u;
  x.g = x.lane >> 4; x.tok = x.lane & 15;
  x.m = x.w & 3;
  const int xc = x.b & 7, j = x.b >> 3;
  x.t = 4 * xc + (j >> 3); x.c = j & 7;
  x.tau = 2 * xc + (j >> 4); x.pp = j & 15;
  x.T = 4 * x.t + x.m;
  x.TA = 8 * x.tau + 2 * x.m;
  x.ab[0] = x.sm + x.lane16;
  unsigned o1 = x.lane16 + 60u * KB, o2 = x.lane16 + 120u * KB;
  asm volatile("" : "+v"(o1), "+v"(o2));
  x.ab[1] = x.sm + o1; x.ab[2] = x.sm + o2;
}

template <int LO, int HI, int THR> DI void dmaSeg(const Ctx &x, bool live) {
  constexpr int Q = LO % POSE;
  int w = x.w, b = x.b;
  asm volatile("" : "+s"(w), "+s"(b));
  const int m = w & 3, j = b >> 3;
  unsigned fr;
  if constexpr (Q < POS2) fr = W1_F + (unsigned)((j & 15) * 96 + Q + m);
  else if constexpr (Q < POS5) fr = W2_F + (unsigned)(((Q - POS2) / 64) * 512 + (j & 7) * 64 + (Q - POS2) % 64 + m);
  else if constexpr (Q < POS6) fr = W5_F + (unsigned)((j & 15) * 112 + (Q - POS5) + m);
  else fr = W6_F + (unsigned)((j & 7) * 224 + (Q - POS6) + m);
  unsigned so = live ? OFF_WT + fr * KB : OOB;
  unsigned lds = x.lds0 + (unsigned)(LO % RING + m) * KB;
  const unsigned wrap = x.lds0 + (unsigned)(RING + m) * KB;
#pragma unroll 1
  for (int i = 0; i < (HI - LO) / 4; ++i) {
    asm volatile("s_mov_b32 m0, %0\n\ts_nop 0\n\tbuffer_load_dwordx4 %1, %2, %3 offen sc1 lds" ::"s"(lds), "v"(x.lane16), "s"(x.rs), "s"(so) : "memory");
    if constexpr (THR == 3) asm volatile("s_waitcnt vmcnt(3)" ::: "memory");
    if constexpr (THR == 5) asm volatile("s_waitcnt vmcnt(5)" ::: "memory");
    so += 4u * KB;
    lds += 4u * KB;
    lds = lds >= wrap ? lds - RING * KB : lds;
  }
}
constexpr int mat_end(int p) {
  const int base = p - p % POSE, q = p % POSE;
  return base + (q < POS2 ? POS2 : q < POS3 ? POS3 : q < POS4 ? POS4 : q < POS5 ? POS5 : q < POS6 ? POS6 : POSE);
}
template <int LO, int HI, int THR = -1> DI void dmaR(const Ctx &x, bool live = true) {
  if constexpr (LO < HI) {
    constexpr int E = mat_end(LO) < HI ? mat_end(LO) : HI;
    dmaSeg<LO, E, THR>(x, live);
    dmaR<E, HI, THR>(x, live);
  }
}
template <int LO, int HI> DI void dmaRT(const Ctx &x, bool live = true) { dmaR<LO, HI, 3>(x, live); }
template <int LO, int HI> DI void dmaR6(const Ctx &x, bool live = true) { dmaR<LO, HI, 5>(x, live); }
DI void sig_set(const Ctx &x, unsigned v, unsigned word = M_SIG) { *(volatile LAS unsigned *)(x.sm + word + (unsigned)x.m * 4u) = v; }
DI void sig_wait(const Ctx &x, unsigned v, unsigned word = M_SIG) {
  for (unsigned sp = 0; sp < (1u << 20); ++sp) {
    const v4u s = *(volatile LAS v4u *)(x.sm + word);
    if (__builtin_amdgcn_readfirstlane(min(min(s.x, s.y), min(s.z, s.w))) >= v) break;
    __builtin_amdgcn_s_sleep(1);
  }
}

#define LDA(dst, voff, soff) \
  asm volatile("buffer_load_dwordx4 %0, %1, %2, %3 offen sc1" : "=v"(dst) : "v"(voff), "s"(x.rs), "s"(soff) : "memory")
#define LDA2(dst, voff, soff, imm) \
  asm volatile("buffer_load_dwordx2 %0, %1, %2, %3 offen offset:%4 sc1" : "=v"(dst) : "v"(voff), "s"(x.rs), "s"(soff), "i"(imm) : "memory")
#define WAITV1(n, r) asm volatile("s_waitcnt vmcnt(%1)" : "+v"(r) : "i"(n) : "memory")
#define WAITV2(n, r, q) asm volatile("s_waitcnt vmcnt(%2)" : "+v"(r), "+v"(q) : "i"(n) : "memory")
#define TIE(r) asm volatile("" : "+v"(r) : : "memory")
DI bool giveup(Ctx &x) {
  if (++x.spins > SPIN_CAP) {
    if (x.lane == 0) __hip_atomic_store(x.ctl + OFF_ABORT / 4, 1u, RLX_AGENT);
    return true;
  }
  __builtin_amdgcn_s_sleep(1);
  return false;
}
DI v4u load_sure(Ctx &x, unsigned voff, unsigned soff) {
  v4u v;
  while (true) {
    asm volatile("buffer_load_dwordx4 %0, %1, %2, %3 offen sc1\n\ts_waitcnt vmcnt(0)" : "=&v"(v) : "v"(voff), "s"(x.rs), "s"(soff) : "memory");
    if (!anybad(max4(v)) || giveup(x)) break;
  }
  return v;
}
DI v2u load_sure2(Ctx &x, unsigned voff, unsigned soff) {
  v2u v;
  while (true) {
    asm volatile("buffer_load_dwordx2 %0, %1, %2, %3 offen sc1\n\ts_waitcnt vmcnt(0)" : "=&v"(v) : "v"(voff), "s"(x.rs), "s"(soff) : "memory");
    if (!anybad(max(v.x, v.y)) || giveup(x)) break;
  }
  return v;
}

DI void warm(Ctx &x, unsigned voff, unsigned soff, unsigned soff2) {
}
#define LDC(dst, voff, soff) \
  asm volatile("buffer_load_dword %0, %1, %2, %3 offen sc1" : "=v"(dst) : "v"(voff), "s"(x.rs), "s"(soff) : "memory")

#define GBAR() do {} while (0)
#define SLEEP(n) sleepn(n)

DI void put_stats(const Ctx &x, v4u u, v4u v, unsigned sbuf) {
  const f4 a = ulo(u), b = uhi(u), c = ulo(v), d = uhi(v);
  const f4 s = (a + b) + (c + d), q = (a * a + b * b) + (c * c + d * d);
  float s1 = (s.x + s.y) + (s.z + s.w), s2 = (q.x + q.y) + (q.z + q.w);
  xred2<false>(s1, s2);
  if (x.g == 0) stg2(x.rs, (unsigned)x.tok * 8u, sbuf + (unsigned)(x.T * 8 + x.c) * 128u, v2u{clampf(s1), clampf(s2)});
}
struct Stats { v2u v[2]; };
DI void stats_load(const Ctx &x, Stats &s, unsigned soffT) {
  const unsigned vo = (unsigned)(x.g * 256 + x.tok * 8);
  LDA2(s.v[0], vo, soffT, 0); LDA2(s.v[1], vo, soffT, 128);
}
DI void stats_fin(Ctx &x, Stats &s, unsigned soffT, float &mean, float &var) {
  TIE(s.v[0]); TIE(s.v[1]);
  if (__builtin_expect(anybad(max(max(s.v[0].x, s.v[0].y), max(s.v[1].x, s.v[1].y))), 0)) {
    s.v[0] = load_sure2(x, (unsigned)(x.g * 256 + x.tok * 8), soffT);
    s.v[1] = load_sure2(x, (unsigned)(x.g * 256 + x.tok * 8 + 128), soffT);
  }
  float s1 = asf(s.v[0].x) + asf(s.v[1].x), s2 = asf(s.v[0].y) + asf(s.v[1].y);
  xred2<false>(s1, s2);
  mean = s1 * (1.0f / 512.0f);
  var = fmaxf(__builtin_fmaf(-mean, mean, s2 * (1.0f / 512.0f)), 0.0f);
}
DI const LAS v4u *lds4(const Ctx &x, unsigned off) { return (const LAS v4u *)(x.sm + off); }
template <int NT, int P> DI void ldaN(const Ctx &x, v4u (&a)[NT]) {
#pragma unroll
  for (int f = 0; f < NT; ++f) {
    const int slot = (P + f) % RING;
    a[f] = *(const LAS v4u *)(x.ab[slot / 60] + (slot % 60) * KB);
  }
}
template <int NT> DI void tieN(v4u (&a)[NT]) {
  if constexpr (NT == 4) asm volatile("" : "+v"(a[0]), "+v"(a[1]), "+v"(a[2]), "+v"(a[3]));
  else if constexpr (NT == 6) asm volatile("" : "+v"(a[0]), "+v"(a[1]), "+v"(a[2]), "+v"(a[3]), "+v"(a[4]), "+v"(a[5]));
  else asm volatile("" : "+v"(a[0]), "+v"(a[1]), "+v"(a[2]), "+v"(a[3]), "+v"(a[4]), "+v"(a[5]), "+v"(a[6]));
}
DI unsigned mx4(unsigned m, v4u v) {
  asm("v_max3_u32 %0, %0, %1, %2\n\tv_max3_u32 %0, %0, %3, %4" : "+v"(m) : "v"(v.x), "v"(v.y), "v"(v.z), "v"(v.w));
  return m;
}
DI f4 ldcs(const Ctx &x, int tile) { return *(const LAS f4 *)(x.sm + M_CS + (unsigned)tile * 64u + (unsigned)x.g * 16u); }
constexpr int imin(int a, int b) { return a < b ? a : b; }

template <bool SH> DI unsigned vof(const Ctx &x, int k, const unsigned (&vo)[4]) { return SH ? vo[k >> 2] : x.lane16; }
template <int NK> constexpr int depth1() { return 8; }
template <int POS, int NK, bool SH, int K0, int K1, int I> struct KL1 {
  static DI void run(Ctx &x, v4u (&bf)[NK], f4 (&acc)[4], unsigned &mx, v4u (&cur)[4], v4u (&nxt)[4], unsigned so, const unsigned (&vo)[4],
                     unsigned sig) {
    v4u nn[4];
    constexpr int DP = depth1<NK>();
    WAITV1(imin(DP - 1, NK - 1 - I), bf[I]);
    tieN<4>(cur);
    SB();
    if constexpr (I + 2 < K1) nn[0] = *(const LAS v4u *)(x.ab[((POS + 4 * (I + 2)) % RING) / 60] + (((POS + 4 * (I + 2)) % RING) % 60) * KB);
    SB();
    acc[0] = mfma32(cur[0], bf[I], acc[0]);
    SB();
    if constexpr (I + DP < NK) LDA(bf[I + DP], vof<SH>(x, I + DP, vo), so + (unsigned)(I + DP) * KB);
    if constexpr (I + DP == NK - 1) { if (sig) sig_set(x, sig); }
    SB();
    if constexpr (I + 2 < K1) {
#pragma unroll
      for (int f = 1; f < 4; ++f) {
        constexpr int P = POS + 4 * (I + 2);
        nn[f] = *(const LAS v4u *)(x.ab[((P + f) % RING) / 60] + (((P + f) % RING) % 60) * KB);
      }
    }
#pragma unroll
    for (int f = 1; f < 4; ++f) acc[f] = mfma32(cur[f], bf[I], acc[f]);
    mx = mx4(mx, bf[I]);
#pragma unroll
    for (int f = 1; f < 4; ++f) {
      __builtin_amdgcn_sched_group_barrier(0x008, 1, 0);
      __builtin_amdgcn_sched_group_barrier(0x100, 1, 0);
    }
    SB();
    if constexpr (I + 1 < K1) KL1<POS, NK, SH, K0, K1, I + 1>::run(x, bf, acc, mx, nxt, nn, so, vo, sig);
  }
};
template <int POS, bool SH, int K1, int I> struct KL1R {
  static DI void run(Ctx &x, f4 (&acc)[4], unsigned so, const unsigned (&vo)[4]) {
    const v4u bv = load_sure(x, vof<SH>(x, I, vo), so + (unsigned)I * KB);
    v4u a[4];
    ldaN<4, POS + 4 * I>(x, a);
#pragma unroll
    for (int f = 0; f < 4; ++f) acc[f] = mfma32(a[f], bv, acc[f]);
    if constexpr (I + 1 < K1) KL1R<POS, SH, K1, I + 1>::run(x, acc, so, vo);
  }
};
template <bool ST> DI unsigned canary_poll1(Ctx &x, unsigned so, unsigned cvo, Stats &st, unsigned sst) {
  unsigned cn;
  LDC(cn, cvo, so);
  if constexpr (ST) stats_load(x, st, sst);
  WAITV1(0, cn);
  if constexpr (ST) { TIE(st.v[0]); TIE(st.v[1]); cn = max(cn, max(max(st.v[0].x, st.v[0].y), max(st.v[1].x, st.v[1].y))); }
  return cn;
}
template <int NK, bool SH, bool ST>
DI void pro1(Ctx &x, v4u (&bf)[NK], unsigned so, const unsigned (&vo)[4], unsigned cvo, Stats &st, unsigned sst, unsigned &cn) {
  while (anybad(canary_poll1<ST>(x, so, cvo, st, sst)) && !giveup(x)) {}
  cn = 0;
#pragma unroll
  for (int i = 0; i < depth1<NK>(); ++i) LDA(bf[i], vof<SH>(x, i, vo), so + (unsigned)i * KB);
}
template <int NK, bool SH, bool ST>
DI void chk1(Ctx &x, v4u (&bf)[NK], unsigned so, const unsigned (&vo)[4], unsigned cvo, Stats &st, unsigned sst, unsigned cn) {
}
template <int POS, int NK, bool SH, int K0, int K1>
DI void kloop1(Ctx &x, v4u (&bf)[NK], f4 (&acc)[4], unsigned so, const unsigned (&vo)[4], unsigned sig) {
  f4 save[4];
#pragma unroll
  for (int f = 0; f < 4; ++f) save[f] = acc[f];
  unsigned mx = 0;
  v4u a0[4], a1[4];
  ldaN<4, POS + 4 * K0>(x, a0);
  ldaN<4, POS + 4 * (K0 + 1)>(x, a1);
  KL1<POS, NK, SH, K0, K1, K0>::run(x, bf, acc, mx, a0, a1, so, vo, sig);
  if (__builtin_expect(anybad(mx), 0)) {
#pragma unroll
    for (int f = 0; f < 4; ++f) acc[f] = save[f];
    KL1R<POS, SH, K1, K0>::run(x, acc, so, vo);
  }
}
template <int NT, int POS, int K0, int K1, int I> struct KL2 {
  static DI void run(Ctx &x, v4u (&bf)[16][2], f4 (&acc)[NT][2], unsigned &mx, v4u (&cur)[NT], unsigned so, unsigned sig) {
    v4u nxt[NT];
    WAITV2(2 * imin(3, 15 - I), bf[I][0], bf[I][1]);
    tieN<NT>(cur);
    SB();
    constexpr int PN = POS + (I + 1) * NT;
    if constexpr (I + 1 < K1) nxt[0] = *(const LAS v4u *)(x.ab[(PN % RING) / 60] + ((PN % RING) % 60) * KB);
    SB();
    acc[0][0] = mfma32(cur[0], bf[I][0], acc[0][0]);
    acc[0][1] = mfma32(cur[0], bf[I][1], acc[0][1]);
    SB();
    if constexpr (I + 4 < 16) LDA(bf[I + 4][0], x.lane16, so + (unsigned)(I + 4) * KB);
    SB();
    acc[1][0] = mfma32(cur[1], bf[I][0], acc[1][0]);
    acc[1][1] = mfma32(cur[1], bf[I][1], acc[1][1]);
    SB();
    if constexpr (I + 4 < 16) LDA(bf[I + 4][1], x.lane16, so + (unsigned)(16 + I + 4) * KB);
    if constexpr (I == (NT == 6 ? 6 : 11)) sig_set(x, sig);
    SB();
    if constexpr (I + 1 < K1) {
#pragma unroll
      for (int f = 1; f < NT; ++f) nxt[f] = *(const LAS v4u *)(x.ab[((PN + f) % RING) / 60] + (((PN + f) % RING) % 60) * KB);
    }
#pragma unroll
    for (int f = 2; f < NT; ++f) {
      acc[f][0] = mfma32(cur[f], bf[I][0], acc[f][0]);
      acc[f][1] = mfma32(cur[f], bf[I][1], acc[f][1]);
    }
    mx = mx4(mx4(mx, bf[I][0]), bf[I][1]);
#pragma unroll
    for (int f = 1; f < NT; ++f) {
      __builtin_amdgcn_sched_group_barrier(0x100, 1, 0);
      __builtin_amdgcn_sched_group_barrier(0x008, 1, 0);
    }
    SB();
    if constexpr (I + 1 < K1) KL2<NT, POS, K0, K1, I + 1>::run(x, bf, acc, mx, nxt, so, sig);
  }
};
template <int NT, int POS, int K1, int I> struct KL2R {
  static DI void run(Ctx &x, f4 (&acc)[NT][2], unsigned so) {
    const v4u b0 = load_sure(x, x.lane16, so + (unsigned)I * KB), b1 = load_sure(x, x.lane16, so + (unsigned)(16 + I) * KB);
    v4u a[NT];
    ldaN<NT, POS + I * NT>(x, a);
#pragma unroll
    for (int f = 0; f < NT; ++f) {
      acc[f][0] = mfma32(a[f], b0, acc[f][0]);
      acc[f][1] = mfma32(a[f], b1, acc[f][1]);
    }
    if constexpr (I + 1 < K1) KL2R<NT, POS, K1, I + 1>::run(x, acc, so);
  }
};
DI unsigned stmax(const Stats (&st)[2]) {
  return max(max(max(st[0].v[0].x, st[0].v[0].y), max(st[0].v[1].x, st[0].v[1].y)), max(max(st[1].v[0].x, st[1].v[0].y), max(st[1].v[1].x, st[1].v[1].y)));
}
DI void poll2(Ctx &x, Stats (&st)[2], unsigned so, unsigned sst) {
  const unsigned cvo = (unsigned)(x.lane & 31) * KB + 1020u;
  while (true) {
    unsigned cn;
    LDC(cn, cvo, so);
    stats_load(x, st[0], sst); stats_load(x, st[1], sst + 1024u);
    WAITV1(0, cn);
    TIE(st[0].v[0]); TIE(st[0].v[1]); TIE(st[1].v[0]); TIE(st[1].v[1]);
    if (!anybad(max(cn, stmax(st))) || giveup(x)) break;
  }
}
DI void first2(Ctx &x, v4u (&bf)[16][2], unsigned so) {
#pragma unroll
  for (int i = 0; i < 4; ++i) { LDA(bf[i][0], x.lane16, so + (unsigned)i * KB); LDA(bf[i][1], x.lane16, so + (unsigned)(16 + i) * KB); }
}
DI void pro2(Ctx &x, v4u (&bf)[16][2], Stats (&st)[2], unsigned so, unsigned sst, unsigned &cn) {
  poll2(x, st, so, sst);
  cn = 0;
  first2(x, bf, so);
}
DI void chk2(Ctx &x, v4u (&bf)[16][2], Stats (&st)[2], unsigned so, unsigned sst, unsigned cn) {
}
template <int NT, int POS, int K0, int K1>
DI void kloop2(Ctx &x, v4u (&bf)[16][2], f4 (&acc)[NT][2], unsigned so, unsigned sig, unsigned &mx) {
  v4u a0[NT];
  ldaN<NT, POS + K0 * NT>(x, a0);
  KL2<NT, POS, K0, K1, K0>::run(x, bf, acc, mx, a0, so, sig);
}
template <int NT, int POS> DI void redo2(Ctx &x, f4 (&acc)[NT][2], unsigned so) {
#pragma unroll
  for (int f = 0; f < NT; ++f) { acc[f][0] = f4{0.f, 0.f, 0.f, 0.f}; acc[f][1] = f4{0.f, 0.f, 0.f, 0.f}; }
  KL2R<NT, POS, 16, 0>::run(x, acc, so);
}

DI void featmap4(const f4 (&v)[4], const v2u (&wphi)[2], v2u (&plus)[4], v2u (&minus)[4]) {
  const float L2E = 1.4426950408889634f;
  f4 p[4];
#pragma unroll
  for (int i = 0; i < 4; ++i) p[i] = mfma16(wphi[i & 1], v2u{pk2(v[i].x, v[i].y), pk2(v[i].z, v[i].w)}, f4{0.f, 0.f, 0.f, 0.f});
  float mx[4], nm[4];
#pragma unroll
  for (int i = 0; i < 4; ++i) {
    p[i] *= L2E;
    mx[i] = fmaxf(fmaxf(p[i].x, p[i].y), fmaxf(p[i].z, p[i].w));
    nm[i] = -fminf(fminf(p[i].x, p[i].y), fminf(p[i].z, p[i].w));
  }
  xred4<true>(mx[0], mx[1], mx[2], mx[3]);
  xred4<true>(nm[0], nm[1], nm[2], nm[3]);
  f4 e[4], n[4];
  float se[4], sn[4];
#pragma unroll
  for (int i = 0; i < 4; ++i) {
    const f4 a = p[i] - mx[i], b = -p[i] - nm[i];
    e[i] = f4{__builtin_amdgcn_exp2f(a.x), __builtin_amdgcn_exp2f(a.y), __builtin_amdgcn_exp2f(a.z), __builtin_amdgcn_exp2f(a.w)};
    n[i] = f4{__builtin_amdgcn_exp2f(b.x), __builtin_amdgcn_exp2f(b.y), __builtin_amdgcn_exp2f(b.z), __builtin_amdgcn_exp2f(b.w)};
    se[i] = (e[i].x + e[i].y) + (e[i].z + e[i].w);
    sn[i] = (n[i].x + n[i].y) + (n[i].z + n[i].w);
  }
  xred4<false>(se[0], se[1], se[2], se[3]);
  xred4<false>(sn[0], sn[1], sn[2], sn[3]);
#pragma unroll
  for (int i = 0; i < 4; ++i) {
    const f4 a = e[i] * __builtin_amdgcn_rcpf(se[i]), b = n[i] * __builtin_amdgcn_rcpf(sn[i]);
    plus[i] = v2u{pk2(a.x, a.y), pk2(a.z, a.w)};
    minus[i] = v2u{pk2(b.x, b.y), pk2(b.z, b.w)};
  }
}
DI v2u trread(const Ctx &x, unsigned off) {
  s4 r = __builtin_amdgcn_ds_read_tr16_b64_v4i16((LAS s4 *)(x.sm + off));
  return __builtin_bit_cast(v2u, r);
}
DI f4 shsum(f4 s) {
#pragma unroll
  for (int o = 4; o < 64; o <<= 1) {
    s.x += __shfl_xor(s.x, o); s.y += __shfl_xor(s.y, o); s.z += __shfl_xor(s.z, o); s.w += __shfl_xor(s.w, o);
  }
  return s;
}

DI void poll_flags(Ctx &x, unsigned off, unsigned tag) {
  for (unsigned sp = 0;; ++sp) {
    unsigned f0, f1, f2, f3;
    const unsigned vo = (unsigned)x.lane * 64u;
    LDC(f0, vo, off); LDC(f1, vo, off + 4096u); LDC(f2, vo, off + 8192u); LDC(f3, vo, off + 12288u);
    asm volatile("s_waitcnt vmcnt(0)" : "+v"(f0), "+v"(f1), "+v"(f2), "+v"(f3)::"memory");
    if (__all(f0 == tag && f1 == tag && f2 == tag && f3 == tag)) break;
    if (sp > SPIN_CAP) {
      if (x.lane == 0) __hip_atomic_store(x.ctl + OFF_ABORT / 4, 3u, RLX_AGENT);
      break;
    }
    __builtin_amdgcn_s_sleep(8);
  }
}
DI unsigned sigv(int L, int ph) { return (unsigned)(L * 8 + ph + 1); }

DI void compute_path(Ctx &x, const Params &p, v4u (&xres)[2], const v2u (&wph)[2], unsigned tag) {
  const unsigned vz[4] = {0u, 0u, 0u, 0u};
  v4u x1r[2], x2r[2];
#pragma unroll 1
  for (int L = 0; L < 2; ++L) {
    const unsigned lb0 = OFF_L0 + (unsigned)L * L_STRIDE;
    v4u phq[2][2];
    GBAR();
    {
      refresh(x);
      FS(0);
      unsigned lb = lb0;
      asm volatile("" : "+s"(lb));
      v4u bf[16][2];
      Stats st[2];
      const unsigned so = lb + O_XB + (unsigned)x.TA * 16u * KB, sst = lb + O_XS + (unsigned)x.TA * 1024u;
      unsigned cn;
      pro2(x, bf, st, so, sst, cn);
      FS(1); FSP(0);
      BAR();
      if (L == 0 && threadIdx.x == 0) __hip_atomic_store(x.ctl + OFF_FLAGS2 / 4 + x.b * 16, tag, RLX_AGENT);
      chk2(x, bf, st, so, sst, cn);
      FS(2);
      f4 acc[6][2];
#pragma unroll
      for (int f = 0; f < 6; ++f) { acc[f][0] = f4{0.f, 0.f, 0.f, 0.f}; acc[f][1] = f4{0.f, 0.f, 0.f, 0.f}; }
      unsigned mx = 0;
      kloop2<6, POS1, 0, 16>(x, bf, acc, so, sigv(L, 0), mx);
      if (__builtin_expect(anybad(mx), 0)) redo2<6, POS1>(x, acc, so);
      FS(3);
      BAR();
      MARK("P1epi");
      f4 val[2][6];
#pragma unroll
      for (int j = 0; j < 2; ++j) {
        float mean, var;
        stats_fin(x, st[j], sst + (unsigned)j * 1024u, mean, var);
        const float rstd = __builtin_amdgcn_rsqf(var + 1e-5f), mr = mean * rstd;
#pragma unroll
        for (int f = 0; f < 6; ++f) val[j][f] = acc[f][j] * rstd - ldcs(x, f) * mr;
      }
      {
        v2u pl[4], mi[4];
        const f4 kk[4] = {val[0][2], val[0][3], val[1][2], val[1][3]};
        featmap4(kk, wph, pl, mi);
#pragma unroll
        for (int i = 0; i < 4; ++i) {
          const int j = i >> 1, h = i & 1;
          const unsigned img = IMG + (unsigned)((2 * x.m + j) * 16 + x.tok) * 32u + (unsigned)x.g * 8u + (unsigned)(h * 3) * 4096u;
          *(LAS v2u *)(x.sm + img) = pl[i];
          *(LAS v2u *)(x.sm + img + 4096u) = mi[i];
          *(LAS v2u *)(x.sm + img + 8192u) = v2u{pk2(val[j][4 + h].x, val[j][4 + h].y), pk2(val[j][4 + h].z, val[j][4 + h].w)};
        }
      }
      MARK("P1epiend");
      FS(4);
      BAR();
      FS(5);
      {
        v2u pl[4], mi[4];
        TIE(val[0][0]); TIE(val[0][1]); TIE(val[1][0]); TIE(val[1][1]);
        const f4 qq[4] = {val[0][0], val[0][1], val[1][0], val[1][1]};
        featmap4(qq, wph, pl, mi);
#pragma unroll
        for (int i = 0; i < 4; ++i) phq[i >> 1][i & 1] = v4u{pl[i].x, pl[i].y, mi[i].x, mi[i].y};
      }
      FS(6);
    }
    GBAR();
    GBAR();
    {
      refresh(x);
      unsigned lb = lb0;
      asm volatile("" : "+s"(lb));
      BAR();
      FS(7);
      v4u ka[2], za[2];
#pragma unroll
      for (int h = 0; h < 2; ++h) {
        ka[h] = *lds4(x, M_KVA + (unsigned)h * 1024u + x.lane16);
        const v2u z0 = *(const LAS v2u *)(x.sm + M_ZA + (unsigned)(h * 2) * 32u + (unsigned)x.g * 8u);
        const v2u z1 = *(const LAS v2u *)(x.sm + M_ZA + (unsigned)(h * 2 + 1) * 32u + (unsigned)x.g * 8u);
        za[h] = v4u{z0.x, z0.y, z1.x, z1.y};
      }
#pragma unroll
      for (int j = 0; j < 2; ++j) {
        f4 o[2];
#pragma unroll
        for (int h = 0; h < 2; ++h) {
          const f4 num = mfma32(ka[h], phq[j][h], f4{0.f, 0.f, 0.f, 0.f});
          const f4 den = mfma32(za[h], phq[j][h], f4{0.f, 0.f, 0.f, 0.f});
          o[h] = num * __builtin_amdgcn_rcpf(den.x + 1e-6f);
        }
        stg(x.rs, x.lane16, lb + O_OB + (unsigned)((x.TA + j) * 16 + x.pp) * KB, pack8(o[0], o[1]));
      }
      FS(8);
      {
        const unsigned ob = lb + O_OB + (unsigned)(x.TA * 16 + x.pp) * KB;
        warm(x, (unsigned)((x.lane >> 3) & 1) * 16u * KB + (unsigned)(x.lane & 7) * 128u, ob, ob);
      }
    }
    GBAR();
    {
      refresh(x);
      unsigned lb = lb0;
      asm volatile("" : "+s"(lb));
      SLEEP(DLY_2);
      FS(9);
      v4u bf[16];
      const unsigned so = lb + O_OB + (unsigned)x.T * 16u * KB;
      Stats st;
      unsigned cn;
      const unsigned cvo = (unsigned)(x.lane & 15) * KB + 1020u;
      pro1<16, false, false>(x, bf, so, vz, cvo, st, 0u, cn);
      FS(10); FSP(1);
      BAR();
      chk1<16, false, false>(x, bf, so, vz, cvo, st, 0u, cn);
      FS(11);
      f4 acc[4];
#pragma unroll
      for (int f = 0; f < 4; ++f) acc[f] = f4{0.f, 0.f, 0.f, 0.f};
      kloop1<POS2, 16, false, 0, 16>(x, bf, acc, so, vz, sigv(L, 2));
      FS(12);
      x1r[0] = pack8(ulo(xres[0]) + acc[0], uhi(xres[0]) + acc[1]);
      x1r[1] = pack8(ulo(xres[1]) + acc[2], uhi(xres[1]) + acc[3]);
      const unsigned fo = lb + O_X1B + (unsigned)(x.T * 16 + 2 * x.c) * KB;
      stg(x.rs, x.lane16, fo, x1r[0]); stg(x.rs, x.lane16, fo + KB, x1r[1]);
      put_stats(x, x1r[0], x1r[1], lb + O_X1S);
      sig_set(x, sigv(L, 2), M_SIG2);
      FS(13);
      warm(x, (unsigned)(x.lane & 15) * 128u, fo, lb + O_X1S + (unsigned)(x.T * 8 + x.c) * 128u);
    }
    float mean1, rstd1;
    GBAR();
    {
      refresh(x);
      unsigned lb = lb0;
      asm volatile("" : "+s"(lb));
      SLEEP(DLY_3);
      v4u bf[16];
      Stats st;
      const unsigned so = lb + O_X1B + (unsigned)x.T * 16u * KB;
      unsigned cn;
      const unsigned cvo = (unsigned)(x.lane & 15) * KB + 1020u;
      pro1<16, false, true>(x, bf, so, vz, cvo, st, lb + O_X1S + (unsigned)x.T * 1024u, cn);
      FSP(2);
      BAR();
      chk1<16, false, true>(x, bf, so, vz, cvo, st, lb + O_X1S + (unsigned)x.T * 1024u, cn);
      FS(14);
      f4 acc[4];
#pragma unroll
      for (int f = 0; f < 4; ++f) acc[f] = f4{0.f, 0.f, 0.f, 0.f};
      kloop1<POS3, 16, false, 0, 16>(x, bf, acc, so, vz, sigv(L, 3));
      FS(15);
      float var;
      stats_fin(x, st, lb + O_X1S + (unsigned)x.T * 1024u, mean1, var);
      rstd1 = __builtin_amdgcn_rsqf(var + 1e-5f);
      const float a = rstd1 * __builtin_amdgcn_rsqf(__builtin_fmaf(var * rstd1, rstd1, 1e-5f)), ma = mean1 * a;
      f4 y[4];
#pragma unroll
      for (int f = 0; f < 4; ++f) y[f] = gelu4(acc[f] * a - ldcs(x, 6 + f) * ma);
      const unsigned fo = lb + O_HB + (unsigned)(x.T * 16 + 2 * x.c) * KB;
      stg(x.rs, x.lane16, fo, pack8(y[0], y[1])); stg(x.rs, x.lane16, fo + KB, pack8(y[2], y[3]));
      sig_set(x, sigv(L, 3), M_SIG2);
      FS(16);
      warm(x, (unsigned)(x.lane & 15) * 128u, fo, fo);
    }
    GBAR();
    {
      refresh(x);
      unsigned lb = lb0;
      asm volatile("" : "+s"(lb));
      SLEEP(DLY_4);
      v4u bf[16];
      unsigned vo[4];
#pragma unroll
      for (int q = 0; q < 4; ++q) {
        const int d = q == 0 ? -1 : (q == 1 ? 1 : (q == 2 ? -2 : 2));
        const int src = 16 * x.T + x.tok + d;
        vo[q] = (src >= 0 && src < 2048) ? (unsigned)((src >> 4) * 16 * 1024 + (x.g * 16 + (src & 15)) * 16) : OOB;
      }
      const unsigned so = lb + O_HB;
      unsigned cvo;
      {
        const int s_ = x.lane & 15, nb = x.T + (((s_ >> 2) & 1) ? 1 : -1);
        const int tt = (x.lane & 16) ? nb : x.T;
        cvo = (tt >= 0 && tt < 128) ? (unsigned)(tt * 16 + s_) * KB + 1020u : OOB;
      }
      Stats st;
      unsigned cn;
      pro1<16, true, false>(x, bf, so, vo, cvo, st, 0u, cn);
      FSP(3);
      BAR();
      chk1<16, true, false>(x, bf, so, vo, cvo, st, 0u, cn);
      FS(17);
      f4 acc[4];
#pragma unroll
      for (int f = 0; f < 4; ++f) acc[f] = f4{0.f, 0.f, 0.f, 0.f};
      MARK("P4loop");
      kloop1<POS4, 16, true, 0, 16>(x, bf, acc, so, vo, sigv(L, 4));
      MARK("P4end");
      FS(18);
      const float mr = mean1 * rstd1;
      x2r[0] = pack8(ulo(x1r[0]) * rstd1 - mr + acc[0], uhi(x1r[0]) * rstd1 - mr + acc[1]);
      x2r[1] = pack8(ulo(x1r[1]) * rstd1 - mr + acc[2], uhi(x1r[1]) * rstd1 - mr + acc[3]);
      const unsigned fo = lb + O_X2B + (unsigned)(x.T * 16 + 2 * x.c) * KB;
      stg(x.rs, x.lane16, fo, x2r[0]); stg(x.rs, x.lane16, fo + KB, x2r[1]);
      put_stats(x, x2r[0], x2r[1], lb + O_X2S);
      sig_set(x, sigv(L, 4), M_SIG2);
      FS(19);
      warm(x, (unsigned)(x.lane & 15) * 128u, fo, lb + O_X2S + (unsigned)(x.T * 8 + x.c) * 128u);
    }
    GBAR();
    {
      refresh(x);
      unsigned lb = lb0;
      asm volatile("" : "+s"(lb));
      SLEEP(DLY_5);
      v4u bf[16][2];
      Stats st[2];
      const unsigned so = lb + O_X2B + (unsigned)x.TA * 16u * KB, sst = lb + O_X2S + (unsigned)x.TA * 1024u;
      unsigned cn;
      pro2(x, bf, st, so, sst, cn);
      FSP(4);
      BAR();
      chk2(x, bf, st, so, sst, cn);
      FS(20);
      f4 acc[7][2];
#pragma unroll
      for (int f = 0; f < 7; ++f) { acc[f][0] = f4{0.f, 0.f, 0.f, 0.f}; acc[f][1] = f4{0.f, 0.f, 0.f, 0.f}; }
      unsigned mx = 0;
      MARK("P5loop");
      kloop2<7, POS5, 0, 13>(x, bf, acc, so, sigv(L, 5), mx);
      BAR();
      kloop2<7, POS5, 13, 16>(x, bf, acc, so, sigv(L, 5), mx);
      if (__builtin_expect(anybad(mx), 0)) redo2<7, POS5>(x, acc, so);
      FS(21);
      BAR();
      FS(22);
      MARK("P5epi");
      const int u = x.pp >> 1, odd = x.pp & 1;
#pragma unroll
      for (int j = 0; j < 2; ++j) {
        float mean, var;
        stats_fin(x, st[j], sst + (unsigned)j * 1024u, mean, var);
        const float rstd = __builtin_amdgcn_rsqf(var + 1e-5f), mr = mean * rstd;
        const unsigned hb = lb + O_H3B + (unsigned)((x.TA + j) * 56 + 7 * u) * KB;
#pragma unroll
        for (int r = 0; r < 3; ++r) {
          const f4 y0 = gelu4(acc[2 * r][j] * rstd - ldcs(x, 10 + 2 * r) * mr), y1 = gelu4(acc[2 * r + 1][j] * rstd - ldcs(x, 11 + 2 * r) * mr);
          stg(x.rs, x.lane16, hb + (unsigned)(odd ? 4 + r : r) * KB, pack8(y0, y1));
        }
        const f4 y = gelu4(acc[6][j] * rstd - ldcs(x, 16) * mr);
        stg2(x.rs, x.lane16 + (unsigned)odd * 8u, hb + 3 * KB, v2u{clampw(pk2(y.x, y.y)), clampw(pk2(y.z, y.w))});
      }
      FS(23);
      {
        const unsigned hb0 = lb + O_H3B + (unsigned)(x.TA * 56 + 7 * u + (odd ? 3 : 0)) * KB;
        warm(x, (unsigned)(x.lane >> 5) * 56u * KB + (unsigned)(x.lane & 31) * 128u, hb0, hb0);
      }
    }
    GBAR();
    {
      refresh(x);
      unsigned lb = lb0;
      asm volatile("" : "+s"(lb));
      SLEEP(DLY_6);
      v4u hf[56];
      const unsigned so = lb + O_H3B + (unsigned)x.T * 56u * KB;
      Stats st;
      unsigned cn;
      const unsigned cvo = (unsigned)min(x.lane, 55) * KB + 1020u;
      pro1<56, false, false>(x, hf, so, vz, cvo, st, 0u, cn);
      FSP(5);
      BAR();
      chk1<56, false, false>(x, hf, so, vz, cvo, st, 0u, cn);
      FS(24);
      f4 acc[4];
#pragma unroll
      for (int f = 0; f < 4; ++f) acc[f] = f4{0.f, 0.f, 0.f, 0.f};
      kloop1<POS6, 56, false, 0, 17>(x, hf, acc, so, vz, 0u);
      BAR();
      FS(25);
      kloop1<POS6, 56, false, 17, 39>(x, hf, acc, so, vz, 0u);
      BAR();
      FS(26);
      kloop1<POS6, 56, false, 39, 56>(x, hf, acc, so, vz, 0u);
      FS(27);
      BAR();
      xres[0] = pack8(ulo(x1r[0]) + (ulo(x2r[0]) + acc[0]), uhi(x1r[0]) + (uhi(x2r[0]) + acc[1]));
      xres[1] = pack8(ulo(x1r[1]) + (ulo(x2r[1]) + acc[2]), uhi(x1r[1]) + (uhi(x2r[1]) + acc[3]));
      if (L == 0) {
        const unsigned fo = lb + L_STRIDE + O_XB + (unsigned)(x.T * 16 + 2 * x.c) * KB;
        stg(x.rs, x.lane16, fo, xres[0]); stg(x.rs, x.lane16, fo + KB, xres[1]);
        put_stats(x, xres[0], xres[1], lb + L_STRIDE + O_XS);
        warm(x, (unsigned)(x.lane & 15) * 128u, fo, lb + L_STRIDE + O_XS + (unsigned)(x.T * 8 + x.c) * 128u);
        SLEEP(DLY_1);
      } else {
        GAS unsigned char *op = (GAS unsigned char *)p.out + ((size_t)(16 * x.T + x.tok) * 512 + 64 * x.c + 4 * x.g) * 2;
        *(GAS v2u *)op = v2u{xres[0].x, xres[0].y};
        *(GAS v2u *)(op + 32) = v2u{xres[0].z, xres[0].w};
        *(GAS v2u *)(op + 64) = v2u{xres[1].x, xres[1].y};
        *(GAS v2u *)(op + 96) = v2u{xres[1].z, xres[1].w};
      }
      FS(28); FSP(6);
    }
  }
}

DI void poison_h3(Ctx &x, unsigned lb, int j) {
  const int u = x.pp >> 1, odd = x.pp & 1;
  const unsigned hb = lb + O_H3B + (unsigned)((x.TA + j) * 56 + 7 * u) * KB;
#pragma unroll
  for (int r = 0; r < 3; ++r) poi4(x.rs, x.lane16, hb + (unsigned)(odd ? 4 + r : r) * KB);
  poi2(x.rs, x.lane16 + (unsigned)odd * 8u, hb + 3 * KB);
}
DI void helper_path(Ctx &x, unsigned tag) {
#pragma unroll 1
  for (int L = 0; L < 2; ++L) {
    const unsigned lb0 = OFF_L0 + (unsigned)L * L_STRIDE;
    GBAR();
    {
      refresh(x);
      unsigned lb = lb0;
      asm volatile("" : "+s"(lb));
      FS(0);
      DRAIN_BAR();
      FS(1);
      if (L != 0) {
        sig_wait(x, sigv(L, 0));
        dmaR<96, 128>(x);
      }
      BAR();
      if (L != 0) dmaR<128, 156>(x);
      {
        const unsigned fo = (unsigned)(x.T * 16 + 2 * x.c) * KB, sto = (unsigned)(x.T * 8 + x.c) * 128u;
        poi4(x.rs, x.lane16, lb + O_X1B + fo); poi4(x.rs, x.lane16, lb + O_X1B + fo + KB);
        poi4(x.rs, x.lane16, lb + O_X2B + fo); poi4(x.rs, x.lane16, lb + O_X2B + fo + KB);
        if (x.g == 0) { poi2(x.rs, (unsigned)x.tok * 8u, lb + O_X1S + sto); poi2(x.rs, (unsigned)x.tok * 8u, lb + O_X2S + sto); }
      }
      FS(2);
      BAR();
      FS(3);
      {
        const int h = x.m >> 1, sg = x.m & 1;
        const unsigned lo = (unsigned)(4 * x.g + (x.tok >> 2)) * 32u + (unsigned)(x.tok & 3) * 8u;
        const unsigned ia = IMG + (unsigned)(h * 3 + sg) * 4096u + lo, iv = IMG + (unsigned)(h * 3 + 2) * 4096u + lo;
        f4 kv = {0.f, 0.f, 0.f, 0.f}, z = {0.f, 0.f, 0.f, 0.f};
#pragma unroll
        for (int tb = 0; tb < 4; ++tb) {
          const v2u a0 = trread(x, ia + tb * 1024u), a1 = trread(x, ia + tb * 1024u + 512u);
          const v2u b0 = trread(x, iv + tb * 1024u), b1 = trread(x, iv + tb * 1024u + 512u);
          const v4u a = {a0.x, a0.y, a1.x, a1.y};
          kv = mfma32(a, v4u{b0.x, b0.y, b1.x, b1.y}, kv);
          z = mfma32(a, v4u{0x3F803F80u, 0x3F803F80u, 0x3F803F80u, 0x3F803F80u}, z);
        }
        stg2(x.rs, (unsigned)x.lane * 8u, lb + O_KVP + (unsigned)((x.pp * 16 + x.tau) * 4 + x.m) * KB, v2u{clampw(pk2(kv.x, kv.y)), clampw(pk2(kv.z, kv.w))});
        if (x.tok == 0) stg(x.rs, (unsigned)x.g * 16u, lb + O_KVZ + (unsigned)((x.pp * 4 + x.m) * 16 + x.tau) * 64u, clamp4(__builtin_bit_cast(v4u, z)));
      }
      poison_h3(x, lb, 0); poison_h3(x, lb, 1);
      FS(4);
    }
    GBAR();
    {
      refresh(x);
      unsigned lb = lb0;
      asm volatile("" : "+s"(lb));
      if (L == 0) {
        if (x.m == 0) {
          poll_flags(x, OFF_FLAGS2, tag);
          *(volatile LAS unsigned *)(x.sm + M_RDY) = 1u;
        } else {
          for (unsigned sp = 0; sp < (1u << 20); ++sp) {
            if (__builtin_amdgcn_readfirstlane(*(volatile LAS unsigned *)(x.sm + M_RDY))) break;
            __builtin_amdgcn_s_sleep(2);
          }
        }
        v4u cv[3];
#pragma unroll
        for (int jj = 0; jj < 3; ++jj) {
          const int u = min(6 + x.m + 4 * jj, 16);
          cv[jj] = ldg(x.rs, x.lane16, OFF_CSP + (u < 10 ? 96u + (unsigned)(x.c * 4 + u - 6) : 128u + (unsigned)(x.pp * 7 + u - 10)) * KB);
        }
#pragma unroll
        for (int jj = 0; jj < 3; ++jj) {
          const f4 s = shsum(__builtin_bit_cast(f4, cv[jj]));
          const int u = 6 + x.m + 4 * jj;
          if (u < 17 && x.lane < 4) *(LAS f4 *)(x.sm + M_CS + (unsigned)u * 64u + (unsigned)x.lane * 16u) = s;
        }
      }
      SLEEP(DLY_KV);
      v4u pv[16];
      const unsigned so = lb + O_KVP + (unsigned)(x.pp * 64 + x.m) * KB, sz = lb + O_KVZ + (unsigned)(x.pp * 4 + x.m) * KB;
      v4u zv;
      {
        const unsigned cvo = (unsigned)(x.lane & 15) * 4096u + (KV16 ? 508u : 1020u);
        while (true) {
          unsigned cn;
          LDC(cn, cvo, so);
          LDA(zv, x.lane16, sz);
          WAITV2(0, cn, zv);
          if (!anybad(max(cn, max4(zv))) || giveup(x)) break;
        }
      }
      bool again;
      f4 s;
      v2u pw[16];
      do {
        asm volatile("" ::: "memory");
#pragma unroll
        for (int i = 0; i < 16; ++i) pw[i] = __builtin_amdgcn_raw_buffer_load_b64(x.rs, (unsigned)x.lane * 8u, so + (unsigned)i * 4096u, AUX_SC1);
        unsigned mx = 0;
#pragma unroll
        for (int i = 0; i < 16; ++i) mx = max(mx, max(pw[i].x, pw[i].y));
        again = anybad(mx) && !giveup(x);
      } while (__builtin_expect(again, 0));
      s = ulo(v4u{pw[0].x, pw[0].y, 0u, 0u});
#pragma unroll
      for (int i = 1; i < 16; ++i) s += ulo(v4u{pw[i].x, pw[i].y, 0u, 0u});
      *(LAS v2u *)(x.sm + M_KVA + (unsigned)(x.m >> 1) * 1024u + x.lane16 + (unsigned)(x.m & 1) * 8u) = v2u{pk2(s.x, s.y), pk2(s.z, s.w)};
      const f4 zs = shsum(__builtin_bit_cast(f4, zv));
      if (x.lane < 4) *(LAS v2u *)(x.sm + M_ZA + (unsigned)x.m * 32u + (unsigned)x.lane * 8u) = v2u{pk2(zs.x, zs.y), pk2(zs.z, zs.w)};
      FS(5); FSP(0);
      GBAR();
      BAR();
      FS(6);
      if (L == 0) dmaR<96, 156>(x);
      dmaR<156, 188>(x);
    }
    GBAR();
    {
      refresh(x);
      unsigned lb = lb0;
      asm volatile("" : "+s"(lb));
      DRAIN_BAR();
      FS(7);
      sig_wait(x, sigv(L, 2));
      dmaR<188, 220>(x);
      sig_wait(x, sigv(L, 2), M_SIG2);
      dmaR<220, 252>(x);
    }
    GBAR();
    {
      refresh(x);
      unsigned lb = lb0;
      asm volatile("" : "+s"(lb));
      DRAIN_BAR();
      FS(8);
      sig_wait(x, sigv(L, 3));
      FS(15);
      dmaR<252, 284>(x);
      sig_wait(x, sigv(L, 3), M_SIG2);
      dmaR<284, 316>(x);
      FS(16);
    }
    GBAR();
    {
      refresh(x);
      unsigned lb = lb0;
      asm volatile("" : "+s"(lb));
      DRAIN_BAR();
      if (L == 0) {
        const unsigned fo = lb + L_STRIDE + O_XB + (unsigned)(x.T * 16 + 2 * x.c) * KB;
        poi4(x.rs, x.lane16, fo); poi4(x.rs, x.lane16, fo + KB);
        if (x.g == 0) poi2(x.rs, (unsigned)x.tok * 8u, lb + L_STRIDE + O_XS + (unsigned)(x.T * 8 + x.c) * 128u);
      }
      FS(9);
      sig_wait(x, sigv(L, 4));
      FS(18);
      dmaR<316, 348>(x);
      sig_wait(x, sigv(L, 4), M_SIG2);
      dmaR<348, 380>(x);
      FS(19);
    }
    GBAR();
    {
      refresh(x);
      unsigned lb = lb0;
      asm volatile("" : "+s"(lb));
      DRAIN_BAR();
      if (L == 0) {
        const unsigned fo = lb + L_STRIDE + O_HB + (unsigned)(x.T * 16 + 2 * x.c) * KB;
        poi4(x.rs, x.lane16, fo); poi4(x.rs, x.lane16, fo + KB);
        poi4(x.rs, x.lane16, lb + L_STRIDE + O_OB + (unsigned)(x.TA * 16 + x.pp) * KB);
        poi4(x.rs, x.lane16, lb + L_STRIDE + O_OB + (unsigned)((x.TA + 1) * 16 + x.pp) * KB);
      }
      FS(10);
      dmaRT<380, 400>(x);
      DRAIN_BAR();
      sig_wait(x, sigv(L, 5));
      dmaR<400, 444>(x);
      BAR();
      FS(11);
      dmaR6<444, 556>(x);
    }
    GBAR();
    {
      refresh(x);
      v4u wr[17];
      {
        const unsigned ws0 = OFF_WT + (W6_F + (unsigned)(x.c * 224 + 156 + x.m)) * KB;
#pragma unroll
        for (int i = 0; i < 17; ++i) LDA(wr[i], x.lane16, ws0 + (unsigned)i * 4096u);
      }
      WAITV_BAR(17);
      FS(12);
      BAR();
      asm volatile("s_waitcnt vmcnt(0)" : "+v"(wr[0]), "+v"(wr[1]), "+v"(wr[2]), "+v"(wr[3]), "+v"(wr[4]), "+v"(wr[5]), "+v"(wr[6]), "+v"(wr[7]),
                   "+v"(wr[8]), "+v"(wr[9]), "+v"(wr[10]), "+v"(wr[11]), "+v"(wr[12]), "+v"(wr[13]), "+v"(wr[14]), "+v"(wr[15]), "+v"(wr[16])::"memory");
#pragma unroll
      for (int i = 0; i < 17; ++i) *(LAS v4u *)(x.sm + ((556 + 4 * i) % RING + x.m) * KB + x.lane16) = wr[i];
      DRAIN_BAR();
      FS(13);
      dmaR<624, 712>(x, L == 0);
      BAR();
      dmaR<712, 720>(x, L == 0);
      FS(14);
    }
  }
}

}

__global__ void __launch_bounds__(512, 2) mega_kernel(Params p) {
  extern __shared__ __attribute__((aligned(16))) unsigned char smem_[];
  Ctx x;
  x.sm = (LAS unsigned char *)smem_;
  x.lds0 = (unsigned)(size_t)smem_;
  x.lane = threadIdx.x & 63;
  x.w = __builtin_amdgcn_readfirstlane(threadIdx.x >> 6);
  x.b = blockIdx.x;
  refresh(x);
  const int b = x.b;
  x.spins = 0;
  x.ctl = (gu32 *)p.ws;
  x.rs = __builtin_amdgcn_make_buffer_rsrc((void *)p.ws, 0, WS_BYTES, 0x00020000);
  const bool cw = x.w < 4;

  FSA(0);
  const unsigned tag = __hip_atomic_load(x.ctl + OFF_EPOCH / 4, RLX_AGENT) + 1u;
  unsigned pf0 = 0, pf1 = 0;
  if (x.w == 7) {
    unsigned long long pc;
    asm volatile("s_getpc_b64 %0" : "=s"(pc));
    const GAS unsigned char *cp = (const GAS unsigned char *)(pc & ~127ull) + (size_t)(((b >> 3) + 32 * (x.lane & 15)) * 128);
    const GAS unsigned char *wp = (const GAS unsigned char *)p.ws + (size_t)min(x.lane, 28) * (2 * MB) + 1 * MB;
    if (x.lane >= 32) {
      const int q = x.lane & 7;
      const unsigned short *ip = q == 0 ? p.x : q == 1 ? p.wqkv : q == 2 ? p.wo : q == 3 ? p.w1 : q == 4 ? p.w2 : q == 5 ? p.w3 : q == 6 ? p.w4 : p.x;
      const size_t half = (q == 2 || q == 3 || q == 4) ? 256 * KB : 1 * MB;
      wp = (const GAS unsigned char *)ip + ((x.lane & 8) ? half : 0);
    }
    asm volatile("global_load_dword %0, %2, off\n\tglobal_load_dword %1, %3, off" : "=&v"(pf0), "=&v"(pf1) : "v"(cp), "v"(wp) : "memory");
    asm volatile("s_waitcnt vmcnt(0)" : "+v"(pf0), "+v"(pf1)::"memory");
  }
  v4u xa = {}, xb = {};
  v2u wa = {}, wb = {};
  if (cw) {
    const GAS unsigned char *xp = (const GAS unsigned char *)p.x + ((size_t)(16 * x.T + (x.lane >> 3)) * 512 + 64 * x.c) * 2 + (x.lane & 7) * 16;
    xa = *(const GAS v4u *)xp; xb = *(const GAS v4u *)(xp + 8 * 1024);
    const GAS unsigned char *wp = (const GAS unsigned char *)p.wphi + (size_t)(2 * x.pp) * 512 + x.lane * 8;
    wa = *(const GAS v2u *)wp; wb = *(const GAS v2u *)(wp + 512);
  }
  if (threadIdx.x < 9) *(LAS unsigned *)(x.sm + M_SIG + threadIdx.x * 4u) = 0u;
  const int bid = ((x.w + 4) & 7) * 256 + b;
  const unsigned short *src = p.wqkv; int ld = 1536, ra = 0, rb = 0, n0 = 0, kstep = 0, csid = -1; unsigned fr[4] = {0u, 0u, 0u, 0u}, ci[4] = {0u, 0u, 0u, 0u};
  if (bid < 384) {
    kstep = bid & 15; const int nb = bid >> 4; n0 = nb * 64; ra = 32 * kstep; rb = ra + 16;
    const int sec = nb >> 3, h0 = 4 * (nb & 7);
#pragma unroll
    for (int ct = 0; ct < 4; ++ct) {
      const int hh = h0 + ct;
      fr[ct] = W1_F + (unsigned)(((hh >> 1) * 16 + kstep) * 6 + sec * 2 + (hh & 1));
      ci[ct] = (unsigned)((hh >> 1) * 6 + sec * 2 + (hh & 1));
    }
    csid = 0;
  } else if (bid < 768) {
    const int mi = (bid - 384) >> 7, r = (bid - 384) & 127; kstep = r & 15; const int nb = r >> 4;
    src = mi == 0 ? p.wo : (mi == 1 ? p.w1 : p.w2); ld = 512; n0 = nb * 64; ra = 32 * kstep; rb = ra + 16;
#pragma unroll
    for (int ct = 0; ct < 4; ++ct) { fr[ct] = W2_F + (unsigned)(mi * 512 + (nb * 16 + kstep) * 4 + ct); ci[ct] = 96u + (unsigned)(nb * 4 + ct); }
    if (mi == 1) csid = 1;
  } else if (bid < 1216) {
    const int r = bid - 768; kstep = r & 15; const int nb = r >> 4; src = p.w3; ld = 1792; n0 = nb * 64; ra = 32 * kstep; rb = ra + 16;
#pragma unroll
    for (int ct = 0; ct < 4; ++ct) { const int F = nb * 4 + ct; fr[ct] = W5_F + (unsigned)(((F / 7) * 16 + kstep) * 7 + F % 7); ci[ct] = 128u + (unsigned)F; }
    csid = 2;
  } else if (bid < 1664) {
    const int r = bid - 1216; const int nb = r & 7; kstep = r >> 3; src = p.w4; ld = 512; n0 = nb * 64;
    const int u = kstep / 7, q = kstep % 7;
    if (q < 3) { ra = 224 * u + 32 * q; rb = ra + 16; }
    else if (q == 3) { ra = 224 * u + 96; rb = 224 * u + 208; }
    else { ra = 224 * u + 112 + 32 * (q - 4); rb = ra + 16; }
#pragma unroll
    for (int ct = 0; ct < 4; ++ct) fr[ct] = W6_F + (unsigned)((nb * 56 + kstep) * 4 + ct);
  }
  if (bid < 1664) {
    const int ip = x.lane >> 5, uu = (x.lane >> 4) & 1, q = (x.lane >> 2) & 3, c1 = ((x.lane >> 1) & 1) ^ ip, bb = x.lane & 1;
    const int rl = 4 * ip + q, ch = 2 * (2 * uu + c1) + bb;
    const unsigned stg0 = x.lds0 + (unsigned)x.w * 4096u;
#pragma unroll
    for (int u = 0; u < 4; ++u) {
      const int row = (u < 2 ? ra + 8 * u : rb + 8 * (u - 2)) + rl;
      const GAS unsigned char *gp = (const GAS unsigned char *)src + ((size_t)row * ld + n0 + ch * 8) * 2;
      asm volatile("s_mov_b32 m0, %0\n\ts_nop 0\n\tglobal_load_lds_dwordx4 %1, off" ::"s"(stg0 + (unsigned)u * 1024u), "v"(gp) : "memory");
    }
  }
  auto retile = [&]() {
    asm volatile("s_waitcnt vmcnt(0)" ::: "memory");
    const int gq = (x.lane & 15) >> 2, gp_ = x.lane & 3, gi = x.g & 1;
#pragma unroll
    for (int ct = 0; ct < 4; ++ct) {
      const unsigned L = (unsigned)((gi << 5) | ((ct >> 1) << 4) | (gq << 2) | (((ct & 1) ^ gi) << 1) | (gp_ >> 1));
      const unsigned a0 = (unsigned)x.w * 4096u + (unsigned)(x.g >> 1) * 1024u + L * 16u + (unsigned)(gp_ & 1) * 8u;
      const v2u lo = trread(x, a0), hi = trread(x, a0 + 2048u);
      const v4u fragv = {lo.x, lo.y, hi.x, hi.y};
      stg(x.rs, x.lane16, __builtin_amdgcn_readfirstlane(OFF_WT + fr[ct] * KB), fragv);
      if (csid >= 0) {
        const f4 cs = mfma32(fragv, v4u{0x3F803F80u, 0x3F803F80u, 0x3F803F80u, 0x3F803F80u}, f4{0.f, 0.f, 0.f, 0.f});
        const unsigned co = __builtin_amdgcn_readfirstlane(OFF_CSP + (ci[ct] * 16u + (unsigned)kstep) * 64u);
        if (x.tok == 0) stg(x.rs, (unsigned)x.g * 16u, co, __builtin_bit_cast(v4u, cs));
      }
    }
  };
  if (bid < 384) retile();
  FSA(1);
  v4u xres[2];
  v2u wph[2];
  if (cw) {
    const unsigned xs = XSTG + (unsigned)x.m * 3072u;
    *(LAS v4u *)(x.sm + xs + x.lane16) = xa; *(LAS v4u *)(x.sm + xs + 1024u + x.lane16) = xb;
    *(LAS v2u *)(x.sm + xs + 2048u + (unsigned)x.lane * 8u) = wa; *(LAS v2u *)(x.sm + xs + 2560u + (unsigned)x.lane * 8u) = wb;
    v2u xin[4];
#pragma unroll
    for (int q = 0; q < 4; ++q) xin[q] = *(const LAS v2u *)(x.sm + xs + (unsigned)x.tok * 128u + (unsigned)q * 32u + (unsigned)x.g * 8u);
#pragma unroll
    for (int h = 0; h < 2; ++h) wph[h] = trread(x, xs + 2048u + (unsigned)h * 512u + (unsigned)(4 * x.g + (x.tok >> 2)) * 32u + (unsigned)(x.tok & 3) * 8u);
    xres[0] = clamp4(v4u{xin[0].x, xin[0].y, xin[1].x, xin[1].y});
    xres[1] = clamp4(v4u{xin[2].x, xin[2].y, xin[3].x, xin[3].y});
    const unsigned fo = OFF_L0 + O_XB + (unsigned)(x.T * 16 + 2 * x.c) * KB;
    stg(x.rs, x.lane16, fo, xres[0]); stg(x.rs, x.lane16, fo + KB, xres[1]);
    put_stats(x, xres[0], xres[1], OFF_L0 + O_XS);
  } else {
    xres[0] = xres[1] = v4u{0u, 0u, 0u, 0u};
    wph[0] = wph[1] = v2u{0u, 0u};
  }
  FSA(2);
  asm volatile("s_waitcnt vmcnt(0) lgkmcnt(0)\n\ts_barrier" ::: "memory");
  FSA(3);
  if (threadIdx.x == 0) __hip_atomic_store(x.ctl + OFF_FLAGS / 4 + b * 16, tag, RLX_AGENT);
  if (bid >= 384 && bid < 1664) retile();
  if (!cw) {
    poi4(x.rs, x.lane16, OFF_L0 + O_KVP + (unsigned)((x.pp * 16 + x.tau) * 4 + x.m) * KB);
    if (x.tok == 0) poi4(x.rs, (unsigned)x.g * 16u, OFF_L0 + O_KVZ + (unsigned)((x.pp * 4 + x.m) * 16 + x.tau) * 64u);
    poi4(x.rs, x.lane16, OFF_L0 + O_OB + (unsigned)(x.TA * 16 + x.pp) * KB);
    poi4(x.rs, x.lane16, OFF_L0 + O_OB + (unsigned)((x.TA + 1) * 16 + x.pp) * KB);
    poi4(x.rs, x.lane16, OFF_L0 + O_HB + (unsigned)(x.T * 16 + 2 * x.c) * KB);
    poi4(x.rs, x.lane16, OFF_L0 + O_HB + (unsigned)(x.T * 16 + 2 * x.c + 1) * KB);
    poi4(x.rs, x.lane16, OFF_L0 + L_STRIDE + O_KVP + (unsigned)((x.pp * 16 + x.tau) * 4 + x.m) * KB);
    if (x.tok == 0) poi4(x.rs, (unsigned)x.g * 16u, OFF_L0 + L_STRIDE + O_KVZ + (unsigned)((x.pp * 4 + x.m) * 16 + x.tau) * 64u);
  }
  if (x.w == 4) {
    poll_flags(x, OFF_FLAGS, tag);
    if (b == 0 && x.lane == 0) __hip_atomic_store(x.ctl + OFF_EPOCH / 4, tag, RLX_AGENT);
  }
  BAR();
  FSA(4);
  if (cw) {
    compute_path(x, p, xres, wph, tag);
  } else {
    v4u cv[2];
#pragma unroll
    for (int jj = 0; jj < 2; ++jj) LDA(cv[jj], x.lane16, OFF_CSP + (unsigned)(x.pp * 6 + min(x.m + 4 * jj, 5)) * KB);
    dmaR<POS1, POS2>(x);
#pragma unroll
    for (int jj = 0; jj < 2; ++jj) {
      WAITV1(24, cv[jj]);
      const f4 s = shsum(__builtin_bit_cast(f4, cv[jj]));
      const int u = x.m + 4 * jj;
      if (u < 6 && x.lane < 4) *(LAS f4 *)(x.sm + M_CS + (unsigned)u * 64u + (unsigned)x.lane * 16u) = s;
    }
    helper_path(x, tag);
  }
  asm volatile("s_waitcnt vmcnt(0)" ::: "memory");
}

extern "C" void kernel_launch(void *const *d_in, const int *in_sizes, int n_in, void *d_out, int out_size, void *d_ws, size_t ws_size,
                              hipStream_t stream) {
  Params p;
  p.x = (const unsigned short *)d_in[0];
  p.wqkv = (const unsigned short *)d_in[1];
  p.wphi = (const unsigned short *)d_in[2];
  p.wo = (const unsigned short *)d_in[3];
  p.w1 = (const unsigned short *)d_in[5];
  p.w2 = (const unsigned short *)d_in[6];
  p.w3 = (const unsigned short *)d_in[7];
  p.w4 = (const unsigned short *)d_in[8];
  p.out = (unsigned short *)d_out;
  p.ws = (unsigned char *)d_ws;
  (void)hipFuncSetAttribute((const void *)mega_kernel, hipFuncAttributeMaxDynamicSharedMemorySize, LDS_BYTES);
  hipLaunchKernelGGL(mega_kernel, dim3(256), dim3(512), LDS_BYTES, stream, p);
}
```
